```python
import torch

def _softmax(x, axis=-1):
    return torch.softmax(x, dim=axis)

def _gelu(x):
    return torch.nn.functional.gelu(x, approximate='tanh')

def _silu(x):
    return torch.nn.functional.silu(x)

def _sigmoid(x):
    return torch.sigmoid(x)

def _layernorm(x, axis=-1, eps=1e-05):
    m = x.mean(dim=axis, keepdim=True)
    v = x.var(dim=axis, keepdim=True, unbiased=False)
    return (x - m) / torch.sqrt(v + eps)

def _scatter_add(tgt, idx, src, axis=0):
    return tgt.clone().index_add_(axis, idx, src)

def _segment_sum(src, seg, n):
    out = torch.zeros((n,) + src.shape[1:], dtype=src.dtype)
    return out.index_add_(0, seg, src)

def _pad(x, pw):
    flat = tuple((v for pair in reversed(pw) for v in pair))
    return torch.nn.functional.pad(x, flat)
kernel_name = 'concat_heads__memory_aug_recurrent_mem_tr__compressed_kv_cla__prompt'

def setup_inputs():

    _w = lambda a: (lambda t: t.to(torch.bfloat16) if t.is_floating_point() else t)(torch.as_tensor(a))

    def _A_setup():
        B, S, Dm = (1, 2048, 512)
        H, Dh = (32, 128)
        M, seg = (8, 32)
        seg = min(seg, S)
        S = S // seg * seg
        return {'x': _w((lambda *s: torch.randn(*s, dtype=torch.bfloat16) * 0.05)(B, S, Dm)), 'mem0': _w((lambda *s: torch.randn(*s, dtype=torch.bfloat16) * 0.4)(B, M, Dm)), 'Wq': _w((lambda *s: torch.randn(*s, dtype=torch.bfloat16) * 6.4)(Dm, H, Dh) * Dm ** (-0.5)), 'Wk': _w((lambda *s: torch.randn(*s, dtype=torch.bfloat16) * 6.4)(Dm, H, Dh) * Dm ** (-0.5)), 'Wv': _w((lambda *s: torch.randn(*s, dtype=torch.bfloat16) * 0.4)(Dm, H, Dh) * Dm ** (-0.5)), 'Wo': _w((lambda *s: torch.randn(*s, dtype=torch.bfloat16) * 0.4)(H, Dh, Dm) * (H * Dh) ** (-0.5)), 'seg_len': seg}
    _A_in = _A_setup()

    def _B_setup():
        B, H, Sq, Sk, D, M = (1, 32, 2048, 2048, 128, 512)
        Dc = 4 * D
        return {'x': _w((lambda *s: torch.randn(*s, dtype=torch.bfloat16) * 1.6)(B, Sq, M)), 'c_kv': _w((lambda *s: torch.randn(*s, dtype=torch.bfloat16) * 0.2)(B, Sk, Dc)), 'w_q': _w((lambda *s: torch.randn(*s, dtype=torch.bfloat16) * 3.2)(M, H, D) * M ** (-0.5)), 'w_uk': _w((lambda *s: torch.randn(*s, dtype=torch.bfloat16) * 3.2)(Dc, H, D) * Dc ** (-0.5)), 'w_uv': _w((lambda *s: torch.randn(*s, dtype=torch.bfloat16) * 0.2)(Dc, H, D) * Dc ** (-0.5)), 'w_o': _w((lambda *s: torch.randn(*s, dtype=torch.bfloat16) * 0.2)(H, D, M) * (H * D) ** (-0.5))}
    _B_in = _B_setup()
    return {'A_x': _A_in['x'], 'A_mem0': _A_in['mem0'], 'A_Wq': _A_in['Wq'], 'A_Wk': _A_in['Wk'], 'A_Wv': _A_in['Wv'], 'A_Wo': _A_in['Wo'], 'A_seg_len': _A_in['seg_len'], 'B_x': _B_in['x'], 'B_c_kv': _B_in['c_kv'], 'B_w_q': _B_in['w_q'], 'B_w_uk': _B_in['w_uk'], 'B_w_uv': _B_in['w_uv'], 'B_w_o': _B_in['w_o']}

def reference(A_x, A_mem0, A_Wq, A_Wk, A_Wv, A_Wo, A_seg_len, B_x, B_c_kv, B_w_q, B_w_uk, B_w_uv, B_w_o):

    def _A_ref(x, mem0, Wq, Wk, Wv, Wo, seg_len):
        B, S, Dm = x.shape
        H, Dh = (Wq.shape[1], Wq.shape[2])
        M = mem0.shape[1]
        scale = Dh ** (-0.5)
        n_seg = S // seg_len
        L = M + seg_len
        idx = torch.arange(L)
        mask = (idx[:, None] < M) | (idx[:, None] >= idx[None, :])
        mem = mem0
        outs = []
        for s in range(n_seg):
            xs = x[:, s * seg_len:(s + 1) * seg_len, :].clone()
            h = torch.concatenate([mem, xs], axis=1)
            q = torch.einsum('btd,dhe->bhte', h, Wq)
            k = torch.einsum('btd,dhe->bhte', h, Wk)
            v = torch.einsum('btd,dhe->bhte', h, Wv)
            scores = torch.einsum('bhqe,bhke->bhqk', q, k) * scale
            scores = torch.where(mask[None, None], scores, -1000000000.0)
            p = _softmax(scores, axis=-1)
            o = torch.einsum('bhqk,bhke->bhqe', p, v)
            o = torch.einsum('bhte,hed->btd', o, Wo)
            mem = o[:, :M, :].clone()
            outs.append(o[:, M:, :])
        return torch.concatenate(outs, axis=1)

    def _B_ref(x, c_kv, w_q, w_uk, w_uv, w_o):
        B, Sq, M = (x.shape[0], x.shape[1], x.shape[2])
        Sk = c_kv.shape[1]
        H, D = (w_q.shape[1], w_q.shape[2])
        q = torch.einsum('bqm,mhd->bhqd', x, w_q)
        k = torch.einsum('bkc,chd->bhkd', c_kv, w_uk)
        v = torch.einsum('bkc,chd->bhkd', c_kv, w_uv)
        scores = torch.einsum('bhqd,bhkd->bhqk', q, k) * D ** (-0.5)
        off = Sk - Sq
        qi = torch.arange(Sq)[:, None]
        kj = torch.arange(Sk)[None, :]
        cmask = kj <= qi + off
        scores = torch.where(cmask[None, None], scores, -1000000000.0)
        p = _softmax(scores, axis=-1)
        ctx = torch.einsum('bhqk,bhkd->bhqd', p, v)
        return torch.einsum('bhqd,hdm->bqm', ctx, w_o)
    a = _A_ref(A_x, A_mem0, A_Wq, A_Wk, A_Wv, A_Wo, A_seg_len)
    b = _B_ref(B_x, B_c_kv, B_w_q, B_w_uk, B_w_uv, B_w_o)
    return torch.concatenate([a, b], axis=-1)
```

```cpp
#include <hip/hip_runtime.h>
#include <stdint.h>
struct Params { const void* p[15]; int t[4]; };
__device__ const unsigned kItems[16384] = {0,0,0,0,0,0,0,0,0,0,0,0,0,0,0,0,0,0,0,0,0,0,0,0,0,0,0,0,0,0,0,0,0,0,0,0,0,0,0,0,0,0,0,0,0,0,0,0,0,0,0,0,0,0,0,0,0,0,0,0,0,0,0,0,0,0,0,0,0,0,0,0,0,0,0,0,0,0,0,0,0,0,0,0,0,0,0,0,0,0,0,0,0,0,0,0,0,0,0,0,0,0,0,0,0,0,0,0,0,0,0,0,0,0,0,0,0,0,0,0,0,0,0,0,0,0,0,0,0x1,0x40,0x30,0x3000000,0,0,0x18,0x80007000,0x3,0,0x7,0x402c1800,0,0,0,0,0x4,0x58,0x7000000,0x10000,0xd000,0x1080100,0x2200,0,0x4,0x28,0x6000000,0,0x13000,0x2010c33,0x4800,0,0,0,0,0,0,0,0,0,0,0,0,0,0,0,0,0,0,0,0,0,0,0,0,0,0,0,0,0,0,0,0,0,0x1,0x40,0x30,0x3300000,0x600,0,0x18,0x80008200,0x3,0x6,0x7,0x402c1800,0,0,0,0,0x4,0x58,0x7000000,0x10000,0xe000,0x1080110,0x2200,0,0x4,0x28,0x6000000,0,0x14000,0x2010c31,0x4800,0,0,0,0,0,0,0,0,0,0,0,0,0,0,0,0,0,0,0,0,0,0,0,0,0,0,0,0,0,0,0,0,0,0x1,0x40,0x30,0x3600000,0xc00,0,0x18,0x80009400,0x3,0xc,0x7,0x402c1800,0,0,0,0,0x4,0x58,0x7000000,0x10000,0xf000,0x1080120,0x2200,0,0x4,0x28,0x6000000,0,0x15000,0x2010c2f,0x4800,0,0,0,0,0,0,0,0,0,0,0,0,0,0,0,0,0,0,0,0,0,0,0,0,0,0,0,0,0,0,0,0,0,0x1,0x40,0x30,0x3900000,0x1200,0,0x18,0x8000a600,0x3,0x12,0x7,0x402c1800,0,0,0,0,0x4,0x58,0x7000000,0x10000,0x10000,0x1080130,0x2200,0,0x4,0x28,0x6000000,0,0x16000,0x2010c2d,0x4800,0,0,0,0,0,0,0,0,0,0,0,0,0,0,0,0,0,0,0,0,0,0,0,0,0,0,0,0,0,0,0,0,0,0x1,0x40,0x30,0x3c00000,0x1800,0,0x18,0x8000b800,0x3,0x18,0x7,0x402c1800,0,0,0,0,0x4,0x28,0x6000000,0,0x11000,0x10a0100,0x4800,0,0x4,0x28,0x6000000,0,0x17000,0x2010c2b,0x4800,0,0,0,0,0,0,0,0,0,0,0,0,0,0,0,0,0,0,0,0,0,0,0,0,0,0,0,0,0,0,0,0,0,0x1,0x40,0x30,0x3e00000,0x1c00,0,0x18,0x8000c400,0x3,0x1c,0x7,0x402c1800,0,0,0,0,0x4,0x28,0x6000000,0,0x12000,0x10a0114,0x4800,0,0x4,0x28,0x6000000,0,0x18000,0x2010c29,0x4800,0,0,0,0,0,0,0,0,0,0,0,0,0,0,0,0,0,0,0,0,0,0,0,0,0,0,0,0,0,0,0,0,0,0,0,0,0,0,0,0,0,0,0,0,0,0,0,0,0,0,0,0,0,0,0,0,0,0,0,0,0,0,0,0,0,0,0,0,0,0,0,0,0,0,0,0,0,0,0,0,0,0,0,0,0,0,0,0,0,0,0,0,0,0,0,0,0,0,0,0,0,0,0,0,0,0,0,0,0,0,0,0,0,0,0,0,0,0,0,0,0,0,0,0,0,0,0,0,0,0,0,0,0,0,0,0,0,0,0,0,0,0,0,0,0,0,0,0,0,0,0,0,0,0,0,0,0,0,0,0,0,0x1,0x40,0x30,0x3030000,0,0xc0000,0x14,0x80007080,0x3,0,0x6,0x402c1800,0,0,0,0,0x3,0,0,0x402c1800,0,0,0,0,0x4,0x58,0x7010000,0x10100,0xd000,0x1080100,0x2200,0,0x4,0x28,0x6010000,0x100,0x13000,0x2010c33,0x4800,0,0,0,0,0,0,0,0,0,0,0,0,0,0,0,0,0,0,0,0,0,0,0,0,0,0x1,0x40,0x30,0x3330000,0x600,0xc0000,0x14,0x80008280,0x3,0x6,0x6,0x402c1800,0,0,0,0,0x3,0x6,0,0x402c1800,0,0,0,0,0x4,0x58,0x7010000,0x10100,0xe000,0x1080110,0x2200,0,0x4,0x28,0x6010000,0x100,0x14000,0x2010c31,0x4800,0,0,0,0,0,0,0,0,0,0,0,0,0,0,0,0,0,0,0,0,0,0,0,0,0,0x1,0x40,0x30,0x3630000,0xc00,0xc0000,0x14,0x80009480,0x3,0xc,0x6,0x402c1800,0,0,0,0,0x3,0xc,0,0x402c1800,0,0,0,0,0x4,0x58,0x7010000,0x10100,0xf000,0x1080120,0x2200,0,0x4,0x28,0x6010000,0x100,0x15000,0x2010c2f,0x4800,0,0,0,0,0,0,0,0,0,0,0,0,0,0,0,0,0,0,0,0,0,0,0,0,0,0x1,0x40,0x30,0x3930000,0x1200,0xc0000,0x14,0x8000a680,0x3,0x12,0x6,0x402c1800,0,0,0,0,0x3,0x12,0,0x402c1800,0,0,0,0,0x4,0x58,0x7010000,0x10100,0x10000,0x1080130,0x2200,0,0x4,0x28,0x6010000,0x100,0x16000,0x2010c2d,0x4800,0,0,0,0,0,0,0,0,0,0,0,0,0,0,0,0,0,0,0,0,0,0,0,0,0,0x1,0x40,0x30,0x3c30000,0x1800,0xc0000,0x14,0x8000b880,0x3,0x18,0x6,0x402c1800,0,0,0,0,0x3,0x18,0,0x402c1800,0,0,0,0,0x4,0x28,0x6010000,0x100,0x11000,0x10a0100,0x4800,0,0x4,0x28,0x6010000,0x100,0x17000,0x2010c2b,0x4800,0,0,0,0,0,0,0,0,0,0,0,0,0,0,0,0,0,0,0,0,0,0,0,0,0,0x1,0x40,0x30,0x3e30000,0x1c00,0xc0000,0x14,0x8000c480,0x3,0x1c,0x6,0x402c1800,0,0,0,0,0x3,0x1c,0,0x402c1800,0,0,0,0,0x4,0x28,0x6010000,0x100,0x12000,0x10a0114,0x4800,0,0x4,0x28,0x6010000,0x100,0x18000,0x2010c29,0x4800,0,0,0,0,0,0,0,0,0,0,0,0,0,0,0,0,0,0,0,0,0,0,0,0,0,0,0,0,0,0,0,0,0,0,0,0,0,0,0,0,0,0,0,0,0,0,0,0,0,0,0,0,0,0,0,0,0,0,0,0,0,0,0,0,0,0,0,0,0,0,0,0,0,0,0,0,0,0,0,0,0,0,0,0,0,0,0,0,0,0,0,0,0,0,0,0,0,0,0,0,0,0,0,0,0,0,0,0,0,0,0,0,0,0,0,0,0,0,0,0,0,0,0,0,0,0,0,0,0,0,0,0,0,0,0,0,0,0,0,0,0,0,0,0,0,0,0,0,0,0,0,0,0,0x1,0x40,0x30,0x3058000,0,0x160000,0x14,0x80007100,0x3,0,0x5,0x402c1800,0,0,0,0,0x3,0,0x1,0x402c1800,0,0,0,0,0x4,0x58,0x7020000,0x10200,0xd000,0x1080100,0x2200,0,0x4,0x28,0x6020000,0x200,0x13000,0x2010c33,0x4800,0,0,0,0,0,0,0,0,0,0,0,0,0,0,0,0,0,0,0,0,0,0,0,0,0,0x1,0x40,0x30,0x3358000,0x600,0x160000,0x14,0x80008300,0x3,0x6,0x5,0x402c1800,0,0,0,0,0x3,0x6,0x1,0x402c1800,0,0,0,0,0x4,0x58,0x7020000,0x10200,0xe000,0x1080110,0x2200,0,0x4,0x28,0x6020000,0x200,0x14000,0x2010c31,0x4800,0,0,0,0,0,0,0,0,0,0,0,0,0,0,0,0,0,0,0,0,0,0,0,0,0,0x1,0x40,0x30,0x3658000,0xc00,0x160000,0x14,0x80009500,0x3,0xc,0x5,0x402c1800,0,0,0,0,0x3,0xc,0x1,0x402c1800,0,0,0,0,0x4,0x58,0x7020000,0x10200,0xf000,0x1080120,0x2200,0,0x4,0x28,0x6020000,0x200,0x15000,0x2010c2f,0x4800,0,0,0,0,0,0,0,0,0,0,0,0,0,0,0,0,0,0,0,0,0,0,0,0,0,0x1,0x40,0x30,0x3958000,0x1200,0x160000,0x14,0x8000a700,0x3,0x12,0x5,0x402c1800,0,0,0,0,0x3,0x12,0x1,0x402c1800,0,0,0,0,0x4,0x58,0x7020000,0x10200,0x10000,0x1080130,0x2200,0,0x4,0x28,0x6020000,0x200,0x16000,0x2010c2d,0x4800,0,0,0,0,0,0,0,0,0,0,0,0,0,0,0,0,0,0,0,0,0,0,0,0,0,0x1,0x40,0x30,0x3c58000,0x1800,0x160000,0x14,0x8000b900,0x3,0x18,0x5,0x402c1800,0,0,0,0,0x3,0x18,0x1,0x402c1800,0,0,0,0,0x4,0x28,0x6020000,0x200,0x11000,0x10a0100,0x4800,0,0x4,0x28,0x6020000,0x200,0x17000,0x2010c2b,0x4800,0,0,0,0,0,0,0,0,0,0,0,0,0,0,0,0,0,0,0,0,0,0,0,0,0,0x1,0x40,0x30,0x3e58000,0x1c00,0x160000,0x14,0x8000c500,0x3,0x1c,0x5,0x402c1800,0,0,0,0,0x3,0x1c,0x1,0x402c1800,0,0,0,0,0x4,0x28,0x6020000,0x200,0x12000,0x10a0114,0x4800,0,0x4,0x28,0x6020000,0x200,0x18000,0x2010c29,0x4800,0,0,0,0,0,0,0,0,0,0,0,0,0,0,0,0,0,0,0,0,0,0,0,0,0,0,0,0,0,0,0,0,0,0,0,0,0,0,0,0,0,0,0,0,0,0,0,0,0,0,0,0,0,0,0,0,0,0,0,0,0,0,0,0,0,0,0,0,0,0,0,0,0,0,0,0,0,0,0,0,0,0,0,0,0,0,0,0,0,0,0,0,0,0,0,0,0,0,0,0,0,0,0,0,0,0,0,0,0,0,0,0,0,0,0,0,0,0,0,0,0,0,0,0,0,0,0,0,0,0,0,0,0,0,0,0,0,0,0,0,0,0,0,0,0,0,0,0,0,0,0,0,0,0x1,0x48,0x38,0x4000000,0,0,0x18,0x7200,0x3,0x1,0x7,0x402c1800,0,0,0,0,0x4,0x58,0x7030000,0x10300,0xd000,0x1080100,0x2200,0,0x4,0x28,0x6030000,0x300,0x13000,0x2010c33,0x4800,0,0,0,0,0,0,0,0,0,0,0,0,0,0,0,0,0,0,0,0,0,0,0,0,0,0,0,0,0,0,0,0,0,0x1,0x48,0x38,0x4300000,0x600,0,0x18,0x8400,0x3,0x7,0x7,0x402c1800,0,0,0,0,0x4,0x58,0x7030000,0x10300,0xe000,0x1080110,0x2200,0,0x4,0x28,0x6030000,0x300,0x14000,0x2010c31,0x4800,0,0,0,0,0,0,0,0,0,0,0,0,0,0,0,0,0,0,0,0,0,0,0,0,0,0,0,0,0,0,0,0,0,0x1,0x48,0x38,0x4600000,0xc00,0,0x18,0x9600,0x3,0xd,0x7,0x402c1800,0,0,0,0,0x4,0x58,0x7030000,0x10300,0xf000,0x1080120,0x2200,0,0x4,0x28,0x6030000,0x300,0x15000,0x2010c2f,0x4800,0,0,0,0,0,0,0,0,0,0,0,0,0,0,0,0,0,0,0,0,0,0,0,0,0,0,0,0,0,0,0,0,0,0x1,0x48,0x38,0x4900000,0x1200,0,0x18,0xa800,0x3,0x13,0x7,0x402c1800,0,0,0,0,0x4,0x58,0x7030000,0x10300,0x10000,0x1080130,0x2200,0,0x4,0x28,0x6030000,0x300,0x16000,0x2010c2d,0x4800,0,0,0,0,0,0,0,0,0,0,0,0,0,0,0,0,0,0,0,0,0,0,0,0,0,0,0,0,0,0,0,0,0,0x1,0x48,0x38,0x4c00000,0x1800,0,0x18,0xba00,0x3,0x19,0x7,0x402c1800,0,0,0,0,0x4,0x28,0x6030000,0x300,0x11000,0x10a0100,0x4800,0,0x4,0x28,0x6030000,0x300,0x17000,0x2010c2b,0x4800,0,0,0,0,0,0,0,0,0,0,0,0,0,0,0,0,0,0,0,0,0,0,0,0,0,0,0,0,0,0,0,0,0,0x1,0x48,0x38,0x4e00000,0x1c00,0,0x18,0xc600,0x3,0x1d,0x7,0x402c1800,0,0,0,0,0x4,0x28,0x6030000,0x300,0x12000,0x10a0114,0x4800,0,0x4,0x28,0x6030000,0x300,0x18000,0x2010c29,0x4800,0,0,0,0,0,0,0,0,0,0,0,0,0,0,0,0,0,0,0,0,0,0,0,0,0,0,0,0,0,0,0,0,0,0,0,0,0,0,0,0,0,0,0,0,0,0,0,0,0,0,0,0,0,0,0,0,0,0,0,0,0,0,0,0,0,0,0,0,0,0,0,0,0,0,0,0,0,0,0,0,0,0,0,0,0,0,0,0,0,0,0,0,0,0,0,0,0,0,0,0,0,0,0,0,0,0,0,0,0,0,0,0,0,0,0,0,0,0,0,0,0,0,0,0,0,0,0,0,0,0,0,0,0,0,0,0,0,0,0,0,0,0,0,0,0,0,0,0,0,0,0,0,0,0,0,0,0,0,0,0,0,0x1,0x48,0x38,0x4030000,0,0xc0000,0x14,0x7280,0x3,0x1,0x6,0x402c1800,0,0,0,0,0x3,0x1,0,0x402c1800,0,0,0,0,0x4,0x58,0x7000000,0x10001,0xd200,0x1080100,0x2200,0,0x4,0x28,0x6000000,0x1,0x13200,0x2010c33,0x4800,0,0,0,0,0,0,0,0,0,0,0,0,0,0,0,0,0,0,0,0,0,0,0,0,0,0x1,0x48,0x38,0x4330000,0x600,0xc0000,0x14,0x8480,0x3,0x7,0x6,0x402c1800,0,0,0,0,0x3,0x7,0,0x402c1800,0,0,0,0,0x4,0x58,0x7000000,0x10001,0xe200,0x1080110,0x2200,0,0x4,0x28,0x6000000,0x1,0x14200,0x2010c31,0x4800,0,0,0,0,0,0,0,0,0,0,0,0,0,0,0,0,0,0,0,0,0,0,0,0,0,0x1,0x48,0x38,0x4630000,0xc00,0xc0000,0x14,0x9680,0x3,0xd,0x6,0x402c1800,0,0,0,0,0x3,0xd,0,0x402c1800,0,0,0,0,0x4,0x58,0x7000000,0x10001,0xf200,0x1080120,0x2200,0,0x4,0x28,0x6000000,0x1,0x15200,0x2010c2f,0x4800,0,0,0,0,0,0,0,0,0,0,0,0,0,0,0,0,0,0,0,0,0,0,0,0,0,0x1,0x48,0x38,0x4930000,0x1200,0xc0000,0x14,0xa880,0x3,0x13,0x6,0x402c1800,0,0,0,0,0x3,0x13,0,0x402c1800,0,0,0,0,0x4,0x58,0x7000000,0x10001,0x10200,0x1080130,0x2200,0,0x4,0x28,0x6000000,0x1,0x16200,0x2010c2d,0x4800,0,0,0,0,0,0,0,0,0,0,0,0,0,0,0,0,0,0,0,0,0,0,0,0,0,0x1,0x48,0x38,0x4c30000,0x1800,0xc0000,0x14,0xba80,0x3,0x19,0x6,0x402c1800,0,0,0,0,0x3,0x19,0,0x402c1800,0,0,0,0,0x4,0x28,0x6000000,0x1,0x11200,0x10a0100,0x4800,0,0x4,0x28,0x6000000,0x1,0x17200,0x2010c2b,0x4800,0,0,0,0,0,0,0,0,0,0,0,0,0,0,0,0,0,0,0,0,0,0,0,0,0,0x1,0x48,0x38,0x4e30000,0x1c00,0xc0000,0x14,0xc680,0x3,0x1d,0x6,0x402c1800,0,0,0,0,0x3,0x1d,0,0x402c1800,0,0,0,0,0x4,0x28,0x6000000,0x1,0x12200,0x10a0114,0x4800,0,0x4,0x28,0x6000000,0x1,0x18200,0x2010c29,0x4800,0,0,0,0,0,0,0,0,0,0,0,0,0,0,0,0,0,0,0,0,0,0,0,0,0,0,0,0,0,0,0,0,0,0,0,0,0,0,0,0,0,0,0,0,0,0,0,0,0,0,0,0,0,0,0,0,0,0,0,0,0,0,0,0,0,0,0,0,0,0,0,0,0,0,0,0,0,0,0,0,0,0,0,0,0,0,0,0,0,0,0,0,0,0,0,0,0,0,0,0,0,0,0,0,0,0,0,0,0,0,0,0,0,0,0,0,0,0,0,0,0,0,0,0,0,0,0,0,0,0,0,0,0,0,0,0,0,0,0,0,0,0,0,0,0,0,0,0,0,0,0,0,0,0x1,0x48,0x38,0x4058000,0,0x160000,0x14,0x7300,0x3,0x1,0x5,0x402c1800,0,0,0,0,0x3,0x1,0x1,0x402c1800,0,0,0,0,0x4,0x58,0x7010000,0x10101,0xd200,0x1080100,0x2200,0,0x4,0x28,0x6010000,0x101,0x13200,0x2010c33,0x4800,0,0,0,0,0,0,0,0,0,0,0,0,0,0,0,0,0,0,0,0,0,0,0,0,0,0x1,0x48,0x38,0x4358000,0x600,0x160000,0x14,0x8500,0x3,0x7,0x5,0x402c1800,0,0,0,0,0x3,0x7,0x1,0x402c1800,0,0,0,0,0x4,0x58,0x7010000,0x10101,0xe200,0x1080110,0x2200,0,0x4,0x28,0x6010000,0x101,0x14200,0x2010c31,0x4800,0,0,0,0,0,0,0,0,0,0,0,0,0,0,0,0,0,0,0,0,0,0,0,0,0,0x1,0x48,0x38,0x4658000,0xc00,0x160000,0x14,0x9700,0x3,0xd,0x5,0x402c1800,0,0,0,0,0x3,0xd,0x1,0x402c1800,0,0,0,0,0x4,0x58,0x7010000,0x10101,0xf200,0x1080120,0x2200,0,0x4,0x28,0x6010000,0x101,0x15200,0x2010c2f,0x4800,0,0,0,0,0,0,0,0,0,0,0,0,0,0,0,0,0,0,0,0,0,0,0,0,0,0x1,0x48,0x38,0x4958000,0x1200,0x160000,0x14,0xa900,0x3,0x13,0x5,0x402c1800,0,0,0,0,0x3,0x13,0x1,0x402c1800,0,0,0,0,0x4,0x58,0x7010000,0x10101,0x10200,0x1080130,0x2200,0,0x4,0x28,0x6010000,0x101,0x16200,0x2010c2d,0x4800,0,0,0,0,0,0,0,0,0,0,0,0,0,0,0,0,0,0,0,0,0,0,0,0,0,0x1,0x48,0x38,0x4c58000,0x1800,0x160000,0x14,0xbb00,0x3,0x19,0x5,0x402c1800,0,0,0,0,0x3,0x19,0x1,0x402c1800,0,0,0,0,0x4,0x28,0x6010000,0x101,0x11200,0x10a0100,0x4800,0,0x4,0x28,0x6010000,0x101,0x17200,0x2010c2b,0x4800,0,0,0,0,0,0,0,0,0,0,0,0,0,0,0,0,0,0,0,0,0,0,0,0,0,0x1,0x48,0x38,0x4e58000,0x1c00,0x160000,0x14,0xc700,0x3,0x1d,0x5,0x402c1800,0,0,0,0,0x3,0x1d,0x1,0x402c1800,0,0,0,0,0x4,0x28,0x6010000,0x101,0x12200,0x10a0114,0x4800,0,0x4,0x28,0x6010000,0x101,0x18200,0x2010c29,0x4800,0,0,0,0,0,0,0,0,0,0,0,0,0,0,0,0,0,0,0,0,0,0,0,0,0,0,0,0,0,0,0,0,0,0,0,0,0,0,0,0,0,0,0,0,0,0,0,0,0,0,0,0,0,0,0,0,0,0,0,0,0,0,0,0,0,0,0,0,0,0,0,0,0,0,0,0,0,0,0,0,0,0,0,0,0,0,0,0,0,0,0,0,0,0,0,0,0,0,0,0,0,0,0,0,0,0,0,0,0,0,0,0,0,0,0,0,0,0,0,0,0,0,0,0,0,0,0,0,0,0,0,0,0,0,0,0,0,0,0,0,0,0,0,0,0,0,0,0,0,0,0,0,0,0x2,0x50,0x38,0x5000000,0,0,0x18,0x7400,0x3,0,0x3,0x402c1800,0,0,0,0,0x3,0x1,0x3,0x402c1800,0,0,0,0,0x4,0x58,0x7020000,0x10201,0xd200,0x1080100,0x2200,0,0x4,0x28,0x6020000,0x201,0x13200,0x2010c33,0x4800,0,0,0,0,0,0,0,0,0,0,0,0,0,0,0,0,0,0,0,0,0,0,0,0,0,0x2,0x50,0x38,0x5300000,0x600,0,0x18,0x8600,0x3,0x6,0x3,0x402c1800,0,0,0,0,0x3,0x7,0x3,0x402c1800,0,0,0,0,0x4,0x58,0x7020000,0x10201,0xe200,0x1080110,0x2200,0,0x4,0x28,0x6020000,0x201,0x14200,0x2010c31,0x4800,0,0,0,0,0,0,0,0,0,0,0,0,0,0,0,0,0,0,0,0,0,0,0,0,0,0x2,0x50,0x38,0x5600000,0xc00,0,0x18,0x9800,0x3,0xc,0x3,0x402c1800,0,0,0,0,0x3,0xd,0x3,0x402c1800,0,0,0,0,0x4,0x58,0x7020000,0x10201,0xf200,0x1080120,0x2200,0,0x4,0x28,0x6020000,0x201,0x15200,0x2010c2f,0x4800,0,0,0,0,0,0,0,0,0,0,0,0,0,0,0,0,0,0,0,0,0,0,0,0,0,0x2,0x50,0x38,0x5900000,0x1200,0,0x18,0xaa00,0x3,0x12,0x3,0x402c1800,0,0,0,0,0x3,0x13,0x3,0x402c1800,0,0,0,0,0x4,0x58,0x7020000,0x10201,0x10200,0x1080130,0x2200,0,0x4,0x28,0x6020000,0x201,0x16200,0x2010c2d,0x4800,0,0,0,0,0,0,0,0,0,0,0,0,0,0,0,0,0,0,0,0,0,0,0,0,0,0x2,0x50,0x38,0x5c00000,0x1800,0,0x18,0xbc00,0x3,0x18,0x3,0x402c1800,0,0,0,0,0x3,0x19,0x3,0x402c1800,0,0,0,0,0x4,0x28,0x6020000,0x201,0x11200,0x10a0100,0x4800,0,0x4,0x28,0x6020000,0x201,0x17200,0x2010c2b,0x4800,0,0,0,0,0,0,0,0,0,0,0,0,0,0,0,0,0,0,0,0,0,0,0,0,0,0x2,0x50,0x38,0x5e00000,0x1c00,0,0x18,0xc800,0x3,0x1c,0x3,0x402c1800,0,0,0,0,0x3,0x1d,0x3,0x402c1800,0,0,0,0,0x4,0x28,0x6020000,0x201,0x12200,0x10a0114,0x4800,0,0x4,0x28,0x6020000,0x201,0x18200,0x2010c29,0x4800,0,0,0,0,0,0,0,0,0,0,0,0,0,0,0,0,0,0,0,0,0,0,0,0,0,0,0,0,0,0,0,0,0,0,0,0,0,0,0,0,0,0,0,0,0,0,0,0,0,0,0,0,0,0,0,0,0,0,0,0,0,0,0,0,0,0,0,0,0,0,0,0,0,0,0,0,0,0,0,0,0,0,0,0,0,0,0,0,0,0,0,0,0,0,0,0,0,0,0,0,0,0,0,0,0,0,0,0,0,0,0,0,0,0,0,0,0,0,0,0,0,0,0,0,0,0,0,0,0,0,0,0,0,0,0,0,0,0,0,0,0,0,0,0,0,0,0,0,0,0,0,0,0,0x2,0x50,0x38,0x5030000,0,0xc0000,0x14,0x7480,0x3,0,0x4,0x402c1800,0,0,0,0,0x3,0,0x2,0x402c1800,0,0,0,0,0x4,0x58,0x7030000,0x10301,0xd200,0x1080100,0x2200,0,0x4,0x28,0x6030000,0x301,0x13200,0x2010c33,0x4800,0,0,0,0,0,0,0,0,0,0,0,0,0,0,0,0,0,0,0,0,0,0,0,0,0,0x2,0x50,0x38,0x5330000,0x600,0xc0000,0x14,0x8680,0x3,0x6,0x4,0x402c1800,0,0,0,0,0x3,0x6,0x2,0x402c1800,0,0,0,0,0x4,0x58,0x7030000,0x10301,0xe200,0x1080110,0x2200,0,0x4,0x28,0x6030000,0x301,0x14200,0x2010c31,0x4800,0,0,0,0,0,0,0,0,0,0,0,0,0,0,0,0,0,0,0,0,0,0,0,0,0,0x2,0x50,0x38,0x5630000,0xc00,0xc0000,0x14,0x9880,0x3,0xc,0x4,0x402c1800,0,0,0,0,0x3,0xc,0x2,0x402c1800,0,0,0,0,0x4,0x58,0x7030000,0x10301,0xf200,0x1080120,0x2200,0,0x4,0x28,0x6030000,0x301,0x15200,0x2010c2f,0x4800,0,0,0,0,0,0,0,0,0,0,0,0,0,0,0,0,0,0,0,0,0,0,0,0,0,0x2,0x50,0x38,0x5930000,0x1200,0xc0000,0x14,0xaa80,0x3,0x12,0x4,0x402c1800,0,0,0,0,0x3,0x12,0x2,0x402c1800,0,0,0,0,0x4,0x58,0x7030000,0x10301,0x10200,0x1080130,0x2200,0,0x4,0x28,0x6030000,0x301,0x16200,0x2010c2d,0x4800,0,0,0,0,0,0,0,0,0,0,0,0,0,0,0,0,0,0,0,0,0,0,0,0,0,0x2,0x50,0x38,0x5c30000,0x1800,0xc0000,0x14,0xbc80,0x3,0x18,0x4,0x402c1800,0,0,0,0,0x3,0x18,0x2,0x402c1800,0,0,0,0,0x4,0x28,0x6030000,0x301,0x11200,0x10a0100,0x4800,0,0x4,0x28,0x6030000,0x301,0x17200,0x2010c2b,0x4800,0,0,0,0,0,0,0,0,0,0,0,0,0,0,0,0,0,0,0,0,0,0,0,0,0,0x2,0x50,0x38,0x5e30000,0x1c00,0xc0000,0x14,0xc880,0x3,0x1c,0x4,0x402c1800,0,0,0,0,0x3,0x1c,0x2,0x402c1800,0,0,0,0,0x4,0x28,0x6030000,0x301,0x12200,0x10a0114,0x4800,0,0x4,0x28,0x6030000,0x301,0x18200,0x2010c29,0x4800,0,0,0,0,0,0,0,0,0,0,0,0,0,0,0,0,0,0,0,0,0,0,0,0,0,0,0,0,0,0,0,0,0,0,0,0,0,0,0,0,0,0,0,0,0,0,0,0,0,0,0,0,0,0,0,0,0,0,0,0,0,0,0,0,0,0,0,0,0,0,0,0,0,0,0,0,0,0,0,0,0,0,0,0,0,0,0,0,0,0,0,0,0,0,0,0,0,0,0,0,0,0,0,0,0,0,0,0,0,0,0,0,0,0,0,0,0,0,0,0,0,0,0,0,0,0,0,0,0,0,0,0,0,0,0,0,0,0,0,0,0,0,0,0,0,0,0,0,0,0,0,0,0,0x2,0x50,0x38,0x5058000,0,0x160000,0x14,0x7500,0x3,0x1,0x4,0x402c1800,0,0,0,0,0x3,0x1,0x2,0x402c1800,0,0,0,0,0x4,0x58,0x7000000,0x10002,0xd400,0x1080100,0x2200,0,0x4,0x28,0x6000000,0x2,0x13400,0x2010c33,0x4800,0,0,0,0,0,0,0,0,0,0,0,0,0,0,0,0,0,0,0,0,0,0,0,0,0,0x2,0x50,0x38,0x5358000,0x600,0x160000,0x14,0x8700,0x3,0x7,0x4,0x402c1800,0,0,0,0,0x3,0x7,0x2,0x402c1800,0,0,0,0,0x4,0x58,0x7000000,0x10002,0xe400,0x1080110,0x2200,0,0x4,0x28,0x6000000,0x2,0x14400,0x2010c31,0x4800,0,0,0,0,0,0,0,0,0,0,0,0,0,0,0,0,0,0,0,0,0,0,0,0,0,0x2,0x50,0x38,0x5658000,0xc00,0x160000,0x14,0x9900,0x3,0xd,0x4,0x402c1800,0,0,0,0,0x3,0xd,0x2,0x402c1800,0,0,0,0,0x4,0x58,0x7000000,0x10002,0xf400,0x1080120,0x2200,0,0x4,0x28,0x6000000,0x2,0x15400,0x2010c2f,0x4800,0,0,0,0,0,0,0,0,0,0,0,0,0,0,0,0,0,0,0,0,0,0,0,0,0,0x2,0x50,0x38,0x5958000,0x1200,0x160000,0x14,0xab00,0x3,0x13,0x4,0x402c1800,0,0,0,0,0x3,0x13,0x2,0x402c1800,0,0,0,0,0x4,0x58,0x7000000,0x10002,0x10400,0x1080130,0x2200,0,0x4,0x28,0x6000000,0x2,0x16400,0x2010c2d,0x4800,0,0,0,0,0,0,0,0,0,0,0,0,0,0,0,0,0,0,0,0,0,0,0,0,0,0x2,0x50,0x38,0x5c58000,0x1800,0x160000,0x14,0xbd00,0x3,0x19,0x4,0x402c1800,0,0,0,0,0x3,0x19,0x2,0x402c1800,0,0,0,0,0x4,0x28,0x6000000,0x2,0x11400,0x10a0100,0x4800,0,0x4,0x28,0x6000000,0x2,0x17400,0x2010c2b,0x4800,0,0,0,0,0,0,0,0,0,0,0,0,0,0,0,0,0,0,0,0,0,0,0,0,0,0x2,0x50,0x38,0x5e58000,0x1c00,0x160000,0x14,0xc900,0x3,0x1d,0x4,0x402c1800,0,0,0,0,0x3,0x1d,0x2,0x402c1800,0,0,0,0,0x4,0x28,0x6000000,0x2,0x12400,0x10a0114,0x4800,0,0x4,0x28,0x6000000,0x2,0x18400,0x2010c29,0x4800,0,0,0,0,0,0,0,0,0,0,0,0,0,0,0,0,0,0,0,0,0,0,0,0,0,0,0,0,0,0,0,0,0,0,0,0,0,0,0,0,0,0,0,0,0,0,0,0,0,0,0,0,0,0,0,0,0,0,0,0,0,0,0,0,0,0,0,0,0,0,0,0,0,0,0,0,0,0,0,0,0,0,0,0,0,0,0,0,0,0,0,0,0,0,0,0,0,0,0,0,0,0,0,0,0,0,0,0,0,0,0,0,0,0,0,0,0,0,0,0,0,0,0,0,0,0,0,0,0,0,0,0,0,0,0,0,0,0,0,0,0,0,0,0,0,0,0,0,0,0,0,0,0,0x1,0x40,0x30,0x3100000,0x200,0,0x18,0x80007600,0x3,0x2,0x7,0x402c1800,0,0,0,0,0x4,0x58,0x7010000,0x10102,0xd400,0x1080100,0x2200,0,0x4,0x28,0x6010000,0x102,0x13400,0x2010c33,0x4800,0,0,0,0,0,0,0,0,0,0,0,0,0,0,0,0,0,0,0,0,0,0,0,0,0,0,0,0,0,0,0,0,0,0x1,0x40,0x30,0x3400000,0x800,0,0x18,0x80008800,0x3,0x8,0x7,0x402c1800,0,0,0,0,0x4,0x58,0x7010000,0x10102,0xe400,0x1080110,0x2200,0,0x4,0x28,0x6010000,0x102,0x14400,0x2010c31,0x4800,0,0,0,0,0,0,0,0,0,0,0,0,0,0,0,0,0,0,0,0,0,0,0,0,0,0,0,0,0,0,0,0,0,0x1,0x40,0x30,0x3700000,0xe00,0,0x18,0x80009a00,0x3,0xe,0x7,0x402c1800,0,0,0,0,0x4,0x58,0x7010000,0x10102,0xf400,0x1080120,0x2200,0,0x4,0x28,0x6010000,0x102,0x15400,0x2010c2f,0x4800,0,0,0,0,0,0,0,0,0,0,0,0,0,0,0,0,0,0,0,0,0,0,0,0,0,0,0,0,0,0,0,0,0,0x1,0x40,0x30,0x3a00000,0x1400,0,0x18,0x8000ac00,0x3,0x14,0x7,0x402c1800,0,0,0,0,0x4,0x58,0x7010000,0x10102,0x10400,0x1080130,0x2200,0,0x4,0x28,0x6010000,0x102,0x16400,0x2010c2d,0x4800,0,0,0,0,0,0,0,0,0,0,0,0,0,0,0,0,0,0,0,0,0,0,0,0,0,0,0,0,0,0,0,0,0,0x1,0x40,0x30,0x3d00000,0x1a00,0,0x18,0x8000be00,0x3,0x1a,0x7,0x402c1800,0,0,0,0,0x4,0x28,0x6010000,0x102,0x11400,0x10a0100,0x4800,0,0x4,0x28,0x6010000,0x102,0x17400,0x2010c2b,0x4800,0,0,0,0,0,0,0,0,0,0,0,0,0,0,0,0,0,0,0,0,0,0,0,0,0,0,0,0,0,0,0,0,0,0x1,0x40,0x30,0x3f00000,0x1e00,0,0x18,0x8000ca00,0x3,0x1e,0x7,0x402c1800,0,0,0,0,0x4,0x28,0x6010000,0x102,0x12400,0x10a0114,0x4800,0,0x4,0x28,0x6010000,0x102,0x18400,0x2010c29,0x4800,0,0,0,0,0,0,0,0,0,0,0,0,0,0,0,0,0,0,0,0,0,0,0,0,0,0,0,0,0,0,0,0,0,0,0,0,0,0,0,0,0,0,0,0,0,0,0,0,0,0,0,0,0,0,0,0,0,0,0,0,0,0,0,0,0,0,0,0,0,0,0,0,0,0,0,0,0,0,0,0,0,0,0,0,0,0,0,0,0,0,0,0,0,0,0,0,0,0,0,0,0,0,0,0,0,0,0,0,0,0,0,0,0,0,0,0,0,0,0,0,0,0,0,0,0,0,0,0,0,0,0,0,0,0,0,0,0,0,0,0,0,0,0,0,0,0,0,0,0,0,0,0,0,0,0,0,0,0,0,0,0,0x1,0x40,0x30,0x3130000,0x200,0xc0000,0x14,0x80007680,0x3,0x2,0x6,0x402c1800,0,0,0,0,0x3,0x2,0,0x402c1800,0,0,0,0,0x4,0x58,0x7020000,0x10202,0xd400,0x1080100,0x2200,0,0x4,0x28,0x6020000,0x202,0x13400,0x2010c33,0x4800,0,0,0,0,0,0,0,0,0,0,0,0,0,0,0,0,0,0,0,0,0,0,0,0,0,0x1,0x40,0x30,0x3430000,0x800,0xc0000,0x14,0x80008880,0x3,0x8,0x6,0x402c1800,0,0,0,0,0x3,0x8,0,0x402c1800,0,0,0,0,0x4,0x58,0x7020000,0x10202,0xe400,0x1080110,0x2200,0,0x4,0x28,0x6020000,0x202,0x14400,0x2010c31,0x4800,0,0,0,0,0,0,0,0,0,0,0,0,0,0,0,0,0,0,0,0,0,0,0,0,0,0x1,0x40,0x30,0x3730000,0xe00,0xc0000,0x14,0x80009a80,0x3,0xe,0x6,0x402c1800,0,0,0,0,0x3,0xe,0,0x402c1800,0,0,0,0,0x4,0x58,0x7020000,0x10202,0xf400,0x1080120,0x2200,0,0x4,0x28,0x6020000,0x202,0x15400,0x2010c2f,0x4800,0,0,0,0,0,0,0,0,0,0,0,0,0,0,0,0,0,0,0,0,0,0,0,0,0,0x1,0x40,0x30,0x3a30000,0x1400,0xc0000,0x14,0x8000ac80,0x3,0x14,0x6,0x402c1800,0,0,0,0,0x3,0x14,0,0x402c1800,0,0,0,0,0x4,0x58,0x7020000,0x10202,0x10400,0x1080130,0x2200,0,0x4,0x28,0x6020000,0x202,0x16400,0x2010c2d,0x4800,0,0,0,0,0,0,0,0,0,0,0,0,0,0,0,0,0,0,0,0,0,0,0,0,0,0x1,0x40,0x30,0x3d30000,0x1a00,0xc0000,0x14,0x8000be80,0x3,0x1a,0x6,0x402c1800,0,0,0,0,0x3,0x1a,0,0x402c1800,0,0,0,0,0x4,0x28,0x6020000,0x202,0x11400,0x10a0100,0x4800,0,0x4,0x28,0x6020000,0x202,0x17400,0x2010c2b,0x4800,0,0,0,0,0,0,0,0,0,0,0,0,0,0,0,0,0,0,0,0,0,0,0,0,0,0x1,0x40,0x30,0x3f30000,0x1e00,0xc0000,0x14,0x8000ca80,0x3,0x1e,0x6,0x402c1800,0,0,0,0,0x3,0x1e,0,0x402c1800,0,0,0,0,0x4,0x28,0x6020000,0x202,0x12400,0x10a0114,0x4800,0,0x4,0x28,0x6020000,0x202,0x18400,0x2010c29,0x4800,0,0,0,0,0,0,0,0,0,0,0,0,0,0,0,0,0,0,0,0,0,0,0,0,0,0,0,0,0,0,0,0,0,0,0,0,0,0,0,0,0,0,0,0,0,0,0,0,0,0,0,0,0,0,0,0,0,0,0,0,0,0,0,0,0,0,0,0,0,0,0,0,0,0,0,0,0,0,0,0,0,0,0,0,0,0,0,0,0,0,0,0,0,0,0,0,0,0,0,0,0,0,0,0,0,0,0,0,0,0,0,0,0,0,0,0,0,0,0,0,0,0,0,0,0,0,0,0,0,0,0,0,0,0,0,0,0,0,0,0,0,0,0,0,0,0,0,0,0,0,0,0,0,0x1,0x40,0x30,0x3158000,0x200,0x160000,0x14,0x80007700,0x3,0x2,0x5,0x402c1800,0,0,0,0,0x3,0x2,0x1,0x402c1800,0,0,0,0,0x4,0x58,0x7030000,0x10302,0xd400,0x1080100,0x2200,0,0x4,0x28,0x6030000,0x302,0x13400,0x2010c33,0x4800,0,0,0,0,0,0,0,0,0,0,0,0,0,0,0,0,0,0,0,0,0,0,0,0,0,0x1,0x40,0x30,0x3458000,0x800,0x160000,0x14,0x80008900,0x3,0x8,0x5,0x402c1800,0,0,0,0,0x3,0x8,0x1,0x402c1800,0,0,0,0,0x4,0x58,0x7030000,0x10302,0xe400,0x1080110,0x2200,0,0x4,0x28,0x6030000,0x302,0x14400,0x2010c31,0x4800,0,0,0,0,0,0,0,0,0,0,0,0,0,0,0,0,0,0,0,0,0,0,0,0,0,0x1,0x40,0x30,0x3758000,0xe00,0x160000,0x14,0x80009b00,0x3,0xe,0x5,0x402c1800,0,0,0,0,0x3,0xe,0x1,0x402c1800,0,0,0,0,0x4,0x58,0x7030000,0x10302,0xf400,0x1080120,0x2200,0,0x4,0x28,0x6030000,0x302,0x15400,0x2010c2f,0x4800,0,0,0,0,0,0,0,0,0,0,0,0,0,0,0,0,0,0,0,0,0,0,0,0,0,0x1,0x40,0x30,0x3a58000,0x1400,0x160000,0x14,0x8000ad00,0x3,0x14,0x5,0x402c1800,0,0,0,0,0x3,0x14,0x1,0x402c1800,0,0,0,0,0x4,0x58,0x7030000,0x10302,0x10400,0x1080130,0x2200,0,0x4,0x28,0x6030000,0x302,0x16400,0x2010c2d,0x4800,0,0,0,0,0,0,0,0,0,0,0,0,0,0,0,0,0,0,0,0,0,0,0,0,0,0x1,0x40,0x30,0x3d58000,0x1a00,0x160000,0x14,0x8000bf00,0x3,0x1a,0x5,0x402c1800,0,0,0,0,0x3,0x1a,0x1,0x402c1800,0,0,0,0,0x4,0x28,0x6030000,0x302,0x11400,0x10a0100,0x4800,0,0x4,0x28,0x6030000,0x302,0x17400,0x2010c2b,0x4800,0,0,0,0,0,0,0,0,0,0,0,0,0,0,0,0,0,0,0,0,0,0,0,0,0,0x1,0x40,0x30,0x3f58000,0x1e00,0x160000,0x14,0x8000cb00,0x3,0x1e,0x5,0x402c1800,0,0,0,0,0x3,0x1e,0x1,0x402c1800,0,0,0,0,0x4,0x28,0x6030000,0x302,0x12400,0x10a0114,0x4800,0,0x4,0x28,0x6030000,0x302,0x18400,0x2010c29,0x4800,0,0,0,0,0,0,0,0,0,0,0,0,0,0,0,0,0,0,0,0,0,0,0,0,0,0,0,0,0,0,0,0,0,0,0,0,0,0,0,0,0,0,0,0,0,0,0,0,0,0,0,0,0,0,0,0,0,0,0,0,0,0,0,0,0,0,0,0,0,0,0,0,0,0,0,0,0,0,0,0,0,0,0,0,0,0,0,0,0,0,0,0,0,0,0,0,0,0,0,0,0,0,0,0,0,0,0,0,0,0,0,0,0,0,0,0,0,0,0,0,0,0,0,0,0,0,0,0,0,0,0,0,0,0,0,0,0,0,0,0,0,0,0,0,0,0,0,0,0,0,0,0,0,0x1,0x48,0x38,0x4100000,0x200,0,0x18,0x7800,0x3,0x3,0x7,0x402c1800,0,0,0,0,0x4,0x58,0x7000000,0x10003,0xd600,0x1080100,0x2200,0,0x4,0x28,0x6000000,0x3,0x13600,0x2010c33,0x4800,0,0,0,0,0,0,0,0,0,0,0,0,0,0,0,0,0,0,0,0,0,0,0,0,0,0,0,0,0,0,0,0,0,0x1,0x48,0x38,0x4400000,0x800,0,0x18,0x8a00,0x3,0x9,0x7,0x402c1800,0,0,0,0,0x4,0x58,0x7000000,0x10003,0xe600,0x1080110,0x2200,0,0x4,0x28,0x6000000,0x3,0x14600,0x2010c31,0x4800,0,0,0,0,0,0,0,0,0,0,0,0,0,0,0,0,0,0,0,0,0,0,0,0,0,0,0,0,0,0,0,0,0,0x1,0x48,0x38,0x4700000,0xe00,0,0x18,0x9c00,0x3,0xf,0x7,0x402c1800,0,0,0,0,0x4,0x58,0x7000000,0x10003,0xf600,0x1080120,0x2200,0,0x4,0x28,0x6000000,0x3,0x15600,0x2010c2f,0x4800,0,0,0,0,0,0,0,0,0,0,0,0,0,0,0,0,0,0,0,0,0,0,0,0,0,0,0,0,0,0,0,0,0,0x1,0x48,0x38,0x4a00000,0x1400,0,0x18,0xae00,0x3,0x15,0x7,0x402c1800,0,0,0,0,0x4,0x58,0x7000000,0x10003,0x10600,0x1080130,0x2200,0,0x4,0x28,0x6000000,0x3,0x16600,0x2010c2d,0x4800,0,0,0,0,0,0,0,0,0,0,0,0,0,0,0,0,0,0,0,0,0,0,0,0,0,0,0,0,0,0,0,0,0,0x1,0x48,0x38,0x4d00000,0x1a00,0,0x18,0xc000,0x3,0x1b,0x7,0x402c1800,0,0,0,0,0x4,0x28,0x6000000,0x3,0x11600,0x10a0100,0x4800,0,0x4,0x28,0x6000000,0x3,0x17600,0x2010c2b,0x4800,0,0,0,0,0,0,0,0,0,0,0,0,0,0,0,0,0,0,0,0,0,0,0,0,0,0,0,0,0,0,0,0,0,0x1,0x48,0x38,0x4f00000,0x1e00,0,0x18,0xcc00,0x3,0x1f,0x7,0x402c1800,0,0,0,0,0x4,0x28,0x6000000,0x3,0x12600,0x10a0114,0x4800,0,0x4,0x28,0x6000000,0x3,0x18600,0x2010c29,0x4800,0,0,0,0,0,0,0,0,0,0,0,0,0,0,0,0,0,0,0,0,0,0,0,0,0,0,0,0,0,0,0,0,0,0,0,0,0,0,0,0,0,0,0,0,0,0,0,0,0,0,0,0,0,0,0,0,0,0,0,0,0,0,0,0,0,0,0,0,0,0,0,0,0,0,0,0,0,0,0,0,0,0,0,0,0,0,0,0,0,0,0,0,0,0,0,0,0,0,0,0,0,0,0,0,0,0,0,0,0,0,0,0,0,0,0,0,0,0,0,0,0,0,0,0,0,0,0,0,0,0,0,0,0,0,0,0,0,0,0,0,0,0,0,0,0,0,0,0,0,0,0,0,0,0,0,0,0,0,0,0,0,0x1,0x48,0x38,0x4130000,0x200,0xc0000,0x14,0x7880,0x3,0x3,0x6,0x402c1800,0,0,0,0,0x3,0x3,0,0x402c1800,0,0,0,0,0x4,0x58,0x7010000,0x10103,0xd600,0x1080100,0x2200,0,0x4,0x28,0x6010000,0x103,0x13600,0x2010c33,0x4800,0,0,0,0,0,0,0,0,0,0,0,0,0,0,0,0,0,0,0,0,0,0,0,0,0,0x1,0x48,0x38,0x4430000,0x800,0xc0000,0x14,0x8a80,0x3,0x9,0x6,0x402c1800,0,0,0,0,0x3,0x9,0,0x402c1800,0,0,0,0,0x4,0x58,0x7010000,0x10103,0xe600,0x1080110,0x2200,0,0x4,0x28,0x6010000,0x103,0x14600,0x2010c31,0x4800,0,0,0,0,0,0,0,0,0,0,0,0,0,0,0,0,0,0,0,0,0,0,0,0,0,0x1,0x48,0x38,0x4730000,0xe00,0xc0000,0x14,0x9c80,0x3,0xf,0x6,0x402c1800,0,0,0,0,0x3,0xf,0,0x402c1800,0,0,0,0,0x4,0x58,0x7010000,0x10103,0xf600,0x1080120,0x2200,0,0x4,0x28,0x6010000,0x103,0x15600,0x2010c2f,0x4800,0,0,0,0,0,0,0,0,0,0,0,0,0,0,0,0,0,0,0,0,0,0,0,0,0,0x1,0x48,0x38,0x4a30000,0x1400,0xc0000,0x14,0xae80,0x3,0x15,0x6,0x402c1800,0,0,0,0,0x3,0x15,0,0x402c1800,0,0,0,0,0x4,0x58,0x7010000,0x10103,0x10600,0x1080130,0x2200,0,0x4,0x28,0x6010000,0x103,0x16600,0x2010c2d,0x4800,0,0,0,0,0,0,0,0,0,0,0,0,0,0,0,0,0,0,0,0,0,0,0,0,0,0x1,0x48,0x38,0x4d30000,0x1a00,0xc0000,0x14,0xc080,0x3,0x1b,0x6,0x402c1800,0,0,0,0,0x3,0x1b,0,0x402c1800,0,0,0,0,0x4,0x28,0x6010000,0x103,0x11600,0x10a0100,0x4800,0,0x4,0x28,0x6010000,0x103,0x17600,0x2010c2b,0x4800,0,0,0,0,0,0,0,0,0,0,0,0,0,0,0,0,0,0,0,0,0,0,0,0,0,0x1,0x48,0x38,0x4f30000,0x1e00,0xc0000,0x14,0xcc80,0x3,0x1f,0x6,0x402c1800,0,0,0,0,0x3,0x1f,0,0x402c1800,0,0,0,0,0x4,0x28,0x6010000,0x103,0x12600,0x10a0114,0x4800,0,0x4,0x28,0x6010000,0x103,0x18600,0x2010c29,0x4800,0,0,0,0,0,0,0,0,0,0,0,0,0,0,0,0,0,0,0,0,0,0,0,0,0,0,0,0,0,0,0,0,0,0,0,0,0,0,0,0,0,0,0,0,0,0,0,0,0,0,0,0,0,0,0,0,0,0,0,0,0,0,0,0,0,0,0,0,0,0,0,0,0,0,0,0,0,0,0,0,0,0,0,0,0,0,0,0,0,0,0,0,0,0,0,0,0,0,0,0,0,0,0,0,0,0,0,0,0,0,0,0,0,0,0,0,0,0,0,0,0,0,0,0,0,0,0,0,0,0,0,0,0,0,0,0,0,0,0,0,0,0,0,0,0,0,0,0,0,0,0,0,0,0x1,0x48,0x38,0x4158000,0x200,0x160000,0x14,0x7900,0x3,0x3,0x5,0x402c1800,0,0,0,0,0x3,0x3,0x1,0x402c1800,0,0,0,0,0x4,0x58,0x7020000,0x10203,0xd600,0x1080100,0x2200,0,0x4,0x28,0x6020000,0x203,0x13600,0x2010c33,0x4800,0,0,0,0,0,0,0,0,0,0,0,0,0,0,0,0,0,0,0,0,0,0,0,0,0,0x1,0x48,0x38,0x4458000,0x800,0x160000,0x14,0x8b00,0x3,0x9,0x5,0x402c1800,0,0,0,0,0x3,0x9,0x1,0x402c1800,0,0,0,0,0x4,0x58,0x7020000,0x10203,0xe600,0x1080110,0x2200,0,0x4,0x28,0x6020000,0x203,0x14600,0x2010c31,0x4800,0,0,0,0,0,0,0,0,0,0,0,0,0,0,0,0,0,0,0,0,0,0,0,0,0,0x1,0x48,0x38,0x4758000,0xe00,0x160000,0x14,0x9d00,0x3,0xf,0x5,0x402c1800,0,0,0,0,0x3,0xf,0x1,0x402c1800,0,0,0,0,0x4,0x58,0x7020000,0x10203,0xf600,0x1080120,0x2200,0,0x4,0x28,0x6020000,0x203,0x15600,0x2010c2f,0x4800,0,0,0,0,0,0,0,0,0,0,0,0,0,0,0,0,0,0,0,0,0,0,0,0,0,0x1,0x48,0x38,0x4a58000,0x1400,0x160000,0x14,0xaf00,0x3,0x15,0x5,0x402c1800,0,0,0,0,0x3,0x15,0x1,0x402c1800,0,0,0,0,0x4,0x58,0x7020000,0x10203,0x10600,0x1080130,0x2200,0,0x4,0x28,0x6020000,0x203,0x16600,0x2010c2d,0x4800,0,0,0,0,0,0,0,0,0,0,0,0,0,0,0,0,0,0,0,0,0,0,0,0,0,0x1,0x48,0x38,0x4d58000,0x1a00,0x160000,0x14,0xc100,0x3,0x1b,0x5,0x402c1800,0,0,0,0,0x3,0x1b,0x1,0x402c1800,0,0,0,0,0x4,0x28,0x6020000,0x203,0x11600,0x10a0100,0x4800,0,0x4,0x28,0x6020000,0x203,0x17600,0x2010c2b,0x4800,0,0,0,0,0,0,0,0,0,0,0,0,0,0,0,0,0,0,0,0,0,0,0,0,0,0x1,0x48,0x38,0x4f58000,0x1e00,0x160000,0x14,0xcd00,0x3,0x1f,0x5,0x402c1800,0,0,0,0,0x3,0x1f,0x1,0x402c1800,0,0,0,0,0x4,0x28,0x6020000,0x203,0x12600,0x10a0114,0x4800,0,0x4,0x28,0x6020000,0x203,0x18600,0x2010c29,0x4800,0,0,0,0,0,0,0,0,0,0,0,0,0,0,0,0,0,0,0,0,0,0,0,0,0,0,0,0,0,0,0,0,0,0,0,0,0,0,0,0,0,0,0,0,0,0,0,0,0,0,0,0,0,0,0,0,0,0,0,0,0,0,0,0,0,0,0,0,0,0,0,0,0,0,0,0,0,0,0,0,0,0,0,0,0,0,0,0,0,0,0,0,0,0,0,0,0,0,0,0,0,0,0,0,0,0,0,0,0,0,0,0,0,0,0,0,0,0,0,0,0,0,0,0,0,0,0,0,0,0,0,0,0,0,0,0,0,0,0,0,0,0,0,0,0,0,0,0,0,0,0,0,0,0x2,0x50,0x38,0x5100000,0x200,0,0x18,0x7a00,0x3,0x2,0x3,0x402c1800,0,0,0,0,0x3,0x3,0x3,0x402c1800,0,0,0,0,0x4,0x58,0x7030000,0x10303,0xd600,0x1080100,0x2200,0,0x4,0x28,0x6030000,0x303,0x13600,0x2010c33,0x4800,0,0,0,0,0,0,0,0,0,0,0,0,0,0,0,0,0,0,0,0,0,0,0,0,0,0x2,0x50,0x38,0x5400000,0x800,0,0x18,0x8c00,0x3,0x8,0x3,0x402c1800,0,0,0,0,0x3,0x9,0x3,0x402c1800,0,0,0,0,0x4,0x58,0x7030000,0x10303,0xe600,0x1080110,0x2200,0,0x4,0x28,0x6030000,0x303,0x14600,0x2010c31,0x4800,0,0,0,0,0,0,0,0,0,0,0,0,0,0,0,0,0,0,0,0,0,0,0,0,0,0x2,0x50,0x38,0x5700000,0xe00,0,0x18,0x9e00,0x3,0xe,0x3,0x402c1800,0,0,0,0,0x3,0xf,0x3,0x402c1800,0,0,0,0,0x4,0x58,0x7030000,0x10303,0xf600,0x1080120,0x2200,0,0x4,0x28,0x6030000,0x303,0x15600,0x2010c2f,0x4800,0,0,0,0,0,0,0,0,0,0,0,0,0,0,0,0,0,0,0,0,0,0,0,0,0,0x2,0x50,0x38,0x5a00000,0x1400,0,0x18,0xb000,0x3,0x14,0x3,0x402c1800,0,0,0,0,0x3,0x15,0x3,0x402c1800,0,0,0,0,0x4,0x58,0x7030000,0x10303,0x10600,0x1080130,0x2200,0,0x4,0x28,0x6030000,0x303,0x16600,0x2010c2d,0x4800,0,0,0,0,0,0,0,0,0,0,0,0,0,0,0,0,0,0,0,0,0,0,0,0,0,0x2,0x50,0x38,0x5d00000,0x1a00,0,0x18,0xc200,0x3,0x1a,0x3,0x402c1800,0,0,0,0,0x3,0x1b,0x3,0x402c1800,0,0,0,0,0x4,0x28,0x6030000,0x303,0x11600,0x10a0100,0x4800,0,0x4,0x28,0x6030000,0x303,0x17600,0x2010c2b,0x4800,0,0,0,0,0,0,0,0,0,0,0,0,0,0,0,0,0,0,0,0,0,0,0,0,0,0x2,0x50,0x38,0x5f00000,0x1e00,0,0x18,0xce00,0x3,0x1e,0x3,0x402c1800,0,0,0,0,0x3,0x1f,0x3,0x402c1800,0,0,0,0,0x4,0x28,0x6030000,0x303,0x12600,0x10a0114,0x4800,0,0x4,0x28,0x6030000,0x303,0x18600,0x2010c29,0x4800,0,0,0,0,0,0,0,0,0,0,0,0,0,0,0,0,0,0,0,0,0,0,0,0,0,0x5,0,0,0x2,0x20,0,0x6,0,0,0,0,0,0,0,0,0,0,0,0,0,0,0,0,0,0,0,0,0,0,0,0,0,0,0,0,0,0,0,0,0,0,0,0,0,0,0,0,0,0,0,0,0,0,0,0,0,0,0,0,0,0,0,0,0,0x5,0,0x1,0x2,0x20,0,0x6,0,0,0,0,0,0,0,0,0,0,0,0,0,0,0,0,0,0,0,0,0,0,0,0,0,0,0,0,0,0,0,0,0,0,0,0,0,0,0,0,0,0,0,0,0,0,0,0,0,0,0,0,0,0,0,0,0,0x2,0x50,0x38,0x5130000,0x200,0xc0000,0x14,0x7a80,0x3,0x2,0x4,0x402c1800,0,0,0,0,0x3,0x2,0x2,0x402c1800,0,0,0,0,0x4,0x58,0x7000000,0x10000,0xd800,0x1080108,0x2200,0,0x4,0x28,0x6000000,0,0x13800,0x2010c32,0x4800,0,0,0,0,0,0,0,0,0,0,0,0,0,0,0,0,0,0,0,0,0,0,0,0,0,0x2,0x50,0x38,0x5430000,0x800,0xc0000,0x14,0x8c80,0x3,0x8,0x4,0x402c1800,0,0,0,0,0x3,0x8,0x2,0x402c1800,0,0,0,0,0x4,0x58,0x7000000,0x10000,0xe800,0x1080118,0x2200,0,0x4,0x28,0x6000000,0,0x14800,0x2010c30,0x4800,0,0,0,0,0,0,0,0,0,0,0,0,0,0,0,0,0,0,0,0,0,0,0,0,0,0x2,0x50,0x38,0x5730000,0xe00,0xc0000,0x14,0x9e80,0x3,0xe,0x4,0x402c1800,0,0,0,0,0x3,0xe,0x2,0x402c1800,0,0,0,0,0x4,0x58,0x7000000,0x10000,0xf800,0x1080128,0x2200,0,0x4,0x28,0x6000000,0,0x15800,0x2010c2e,0x4800,0,0,0,0,0,0,0,0,0,0,0,0,0,0,0,0,0,0,0,0,0,0,0,0,0,0x2,0x50,0x38,0x5a30000,0x1400,0xc0000,0x14,0xb080,0x3,0x14,0x4,0x402c1800,0,0,0,0,0x3,0x14,0x2,0x402c1800,0,0,0,0,0x4,0x58,0x7000000,0x10000,0x10800,0x1080138,0x2200,0,0x4,0x28,0x6000000,0,0x16800,0x2010c2c,0x4800,0,0,0,0,0,0,0,0,0,0,0,0,0,0,0,0,0,0,0,0,0,0,0,0,0,0x2,0x50,0x38,0x5d30000,0x1a00,0xc0000,0x14,0xc280,0x3,0x1a,0x4,0x402c1800,0,0,0,0,0x3,0x1a,0x2,0x402c1800,0,0,0,0,0x4,0x28,0x6000000,0,0x11800,0x10a010a,0x4800,0,0x4,0x28,0x6000000,0,0x17800,0x2010c2a,0x4800,0,0,0,0,0,0,0,0,0,0,0,0,0,0,0,0,0,0,0,0,0,0,0,0,0,0x2,0x50,0x38,0x5f30000,0x1e00,0xc0000,0x14,0xce80,0x3,0x1e,0x4,0x402c1800,0,0,0,0,0x3,0x1e,0x2,0x402c1800,0,0,0,0,0x4,0x28,0x6000000,0,0x12800,0x10a011e,0x4800,0,0x4,0x28,0x6000000,0,0x18800,0x2010c28,0x4800,0,0,0,0,0,0,0,0,0,0,0,0,0,0,0,0,0,0,0,0,0,0,0,0,0,0x5,0x4,0,0x2,0x20,0,0x6,0,0,0,0,0,0,0,0,0,0,0,0,0,0,0,0,0,0,0,0,0,0,0,0,0,0,0,0,0,0,0,0,0,0,0,0,0,0,0,0,0,0,0,0,0,0,0,0,0,0,0,0,0,0,0,0,0,0x5,0x4,0x1,0x2,0x20,0,0x6,0,0,0,0,0,0,0,0,0,0,0,0,0,0,0,0,0,0,0,0,0,0,0,0,0,0,0,0,0,0,0,0,0,0,0,0,0,0,0,0,0,0,0,0,0,0,0,0,0,0,0,0,0,0,0,0,0,0x2,0x50,0x38,0x5158000,0x200,0x160000,0x14,0x7b00,0x3,0x3,0x4,0x402c1800,0,0,0,0,0x3,0x3,0x2,0x402c1800,0,0,0,0,0x4,0x58,0x7010000,0x10100,0xd800,0x1080108,0x2200,0,0x4,0x28,0x6010000,0x100,0x13800,0x2010c32,0x4800,0,0,0,0,0,0,0,0,0,0,0,0,0,0,0,0,0,0,0,0,0,0,0,0,0,0x2,0x50,0x38,0x5458000,0x800,0x160000,0x14,0x8d00,0x3,0x9,0x4,0x402c1800,0,0,0,0,0x3,0x9,0x2,0x402c1800,0,0,0,0,0x4,0x58,0x7010000,0x10100,0xe800,0x1080118,0x2200,0,0x4,0x28,0x6010000,0x100,0x14800,0x2010c30,0x4800,0,0,0,0,0,0,0,0,0,0,0,0,0,0,0,0,0,0,0,0,0,0,0,0,0,0x2,0x50,0x38,0x5758000,0xe00,0x160000,0x14,0x9f00,0x3,0xf,0x4,0x402c1800,0,0,0,0,0x3,0xf,0x2,0x402c1800,0,0,0,0,0x4,0x58,0x7010000,0x10100,0xf800,0x1080128,0x2200,0,0x4,0x28,0x6010000,0x100,0x15800,0x2010c2e,0x4800,0,0,0,0,0,0,0,0,0,0,0,0,0,0,0,0,0,0,0,0,0,0,0,0,0,0x2,0x50,0x38,0x5a58000,0x1400,0x160000,0x14,0xb100,0x3,0x15,0x4,0x402c1800,0,0,0,0,0x3,0x15,0x2,0x402c1800,0,0,0,0,0x4,0x58,0x7010000,0x10100,0x10800,0x1080138,0x2200,0,0x4,0x28,0x6010000,0x100,0x16800,0x2010c2c,0x4800,0,0,0,0,0,0,0,0,0,0,0,0,0,0,0,0,0,0,0,0,0,0,0,0,0,0x2,0x50,0x38,0x5d58000,0x1a00,0x160000,0x14,0xc300,0x3,0x1b,0x4,0x402c1800,0,0,0,0,0x3,0x1b,0x2,0x402c1800,0,0,0,0,0x4,0x28,0x6010000,0x100,0x11800,0x10a010a,0x4800,0,0x4,0x28,0x6010000,0x100,0x17800,0x2010c2a,0x4800,0,0,0,0,0,0,0,0,0,0,0,0,0,0,0,0,0,0,0,0,0,0,0,0,0,0x2,0x50,0x38,0x5f58000,0x1e00,0x160000,0x14,0xcf00,0x3,0x1f,0x4,0x402c1800,0,0,0,0,0x3,0x1f,0x2,0x402c1800,0,0,0,0,0x4,0x28,0x6010000,0x100,0x12800,0x10a011e,0x4800,0,0x4,0x28,0x6010000,0x100,0x18800,0x2010c28,0x4800,0,0,0,0,0,0,0,0,0,0,0,0,0,0,0,0,0,0,0,0,0,0,0,0,0,0x5,0x8,0,0x2,0x20,0,0x6,0,0,0,0,0,0,0,0,0,0,0,0,0,0,0,0,0,0,0,0,0,0,0,0,0,0,0,0,0,0,0,0,0,0,0,0,0,0,0,0,0,0,0,0,0,0,0,0,0,0,0,0,0,0,0,0,0,0x5,0x8,0x1,0x2,0x20,0,0x6,0,0,0,0,0,0,0,0,0,0,0,0,0,0,0,0,0,0,0,0,0,0,0,0,0,0,0,0,0,0,0,0,0,0,0,0,0,0,0,0,0,0,0,0,0,0,0,0,0,0,0,0,0,0,0,0,0,0x1,0x40,0x30,0x3200000,0x400,0,0x18,0x80007c00,0x3,0x4,0x7,0x402c1800,0,0,0,0,0x4,0x58,0x7020000,0x10200,0xd800,0x1080108,0x2200,0,0x4,0x28,0x6020000,0x200,0x13800,0x2010c32,0x4800,0,0,0,0,0,0,0,0,0,0,0,0,0,0,0,0,0,0,0,0,0,0,0,0,0,0,0,0,0,0,0,0,0,0x1,0x40,0x30,0x3500000,0xa00,0,0x18,0x80008e00,0x3,0xa,0x7,0x402c1800,0,0,0,0,0x4,0x58,0x7020000,0x10200,0xe800,0x1080118,0x2200,0,0x4,0x28,0x6020000,0x200,0x14800,0x2010c30,0x4800,0,0,0,0,0,0,0,0,0,0,0,0,0,0,0,0,0,0,0,0,0,0,0,0,0,0,0,0,0,0,0,0,0,0x1,0x40,0x30,0x3800000,0x1000,0,0x18,0x8000a000,0x3,0x10,0x7,0x402c1800,0,0,0,0,0x4,0x58,0x7020000,0x10200,0xf800,0x1080128,0x2200,0,0x4,0x28,0x6020000,0x200,0x15800,0x2010c2e,0x4800,0,0,0,0,0,0,0,0,0,0,0,0,0,0,0,0,0,0,0,0,0,0,0,0,0,0,0,0,0,0,0,0,0,0x1,0x40,0x30,0x3b00000,0x1600,0,0x18,0x8000b200,0x3,0x16,0x7,0x402c1800,0,0,0,0,0x4,0x58,0x7020000,0x10200,0x10800,0x1080138,0x2200,0,0x4,0x28,0x6020000,0x200,0x16800,0x2010c2c,0x4800,0,0,0,0,0,0,0,0,0,0,0,0,0,0,0,0,0,0,0,0,0,0,0,0,0,0,0,0,0,0,0,0,0,0x1,0x10,0,0x600000,0xc00,0,0x40,0x80003400,0x4,0x28,0x6020000,0x200,0x11800,0x10a010a,0x4800,0,0x4,0x28,0x6020000,0x200,0x17800,0x2010c2a,0x4800,0,0,0,0,0,0,0,0,0,0,0,0,0,0,0,0,0,0,0,0,0,0,0,0,0,0,0,0,0,0,0,0,0,0,0,0,0,0,0,0,0,0x2,0x20,0,0x2b00000,0x1600,0,0x40,0x584,0x4,0x28,0x6020000,0x200,0x12800,0x10a011e,0x4800,0,0x4,0x28,0x6020000,0x200,0x18800,0x2010c28,0x4800,0,0,0,0,0,0,0,0,0,0,0,0,0,0,0,0,0,0,0,0,0,0,0,0,0,0,0,0,0,0,0,0,0,0,0,0,0,0,0,0,0,0x5,0xc,0,0x2,0x20,0,0x6,0,0,0,0,0,0,0,0,0,0,0,0,0,0,0,0,0,0,0,0,0,0,0,0,0,0,0,0,0,0,0,0,0,0,0,0,0,0,0,0,0,0,0,0,0,0,0,0,0,0,0,0,0,0,0,0,0,0x5,0xc,0x1,0x2,0x20,0,0x6,0,0,0,0,0,0,0,0,0,0,0,0,0,0,0,0,0,0,0,0,0,0,0,0,0,0,0,0,0,0,0,0,0,0,0,0,0,0,0,0,0,0,0,0,0,0,0,0,0,0,0,0,0,0,0,0,0,0x1,0x40,0x30,0x3230000,0x400,0xc0000,0x14,0x80007c80,0x3,0x4,0x6,0x402c1800,0,0,0,0,0x3,0x4,0,0x402c1800,0,0,0,0,0x4,0x58,0x7030000,0x10300,0xd800,0x1080108,0x2200,0,0x4,0x28,0x6030000,0x300,0x13800,0x2010c32,0x4800,0,0,0,0,0,0,0,0,0,0,0,0,0,0,0,0,0,0,0,0,0,0,0,0,0,0x1,0x40,0x30,0x3530000,0xa00,0xc0000,0x14,0x80008e80,0x3,0xa,0x6,0x402c1800,0,0,0,0,0x3,0xa,0,0x402c1800,0,0,0,0,0x4,0x58,0x7030000,0x10300,0xe800,0x1080118,0x2200,0,0x4,0x28,0x6030000,0x300,0x14800,0x2010c30,0x4800,0,0,0,0,0,0,0,0,0,0,0,0,0,0,0,0,0,0,0,0,0,0,0,0,0,0x1,0x40,0x30,0x3830000,0x1000,0xc0000,0x14,0x8000a080,0x3,0x10,0x6,0x402c1800,0,0,0,0,0x3,0x10,0,0x402c1800,0,0,0,0,0x4,0x58,0x7030000,0x10300,0xf800,0x1080128,0x2200,0,0x4,0x28,0x6030000,0x300,0x15800,0x2010c2e,0x4800,0,0,0,0,0,0,0,0,0,0,0,0,0,0,0,0,0,0,0,0,0,0,0,0,0,0x1,0x40,0x30,0x3b30000,0x1600,0xc0000,0x14,0x8000b280,0x3,0x16,0x6,0x402c1800,0,0,0,0,0x3,0x16,0,0x402c1800,0,0,0,0,0x4,0x58,0x7030000,0x10300,0x10800,0x1080138,0x2200,0,0x4,0x28,0x6030000,0x300,0x16800,0x2010c2c,0x4800,0,0,0,0,0,0,0,0,0,0,0,0,0,0,0,0,0,0,0,0,0,0,0,0,0,0x1,0x18,0,0x1700000,0xe00,0,0x40,0x380,0x4,0x28,0x6030000,0x300,0x11800,0x10a010a,0x4800,0,0x4,0x28,0x6030000,0x300,0x17800,0x2010c2a,0x4800,0,0,0,0,0,0,0,0,0,0,0,0,0,0,0,0,0,0,0,0,0,0,0,0,0,0,0,0,0,0,0,0,0,0,0,0,0,0,0,0,0,0x1,0x10,0,0xb00000,0x1600,0,0x40,0x80003e00,0x4,0x28,0x6030000,0x300,0x12800,0x10a011e,0x4800,0,0x4,0x28,0x6030000,0x300,0x18800,0x2010c28,0x4800,0,0,0,0,0,0,0,0,0,0,0,0,0,0,0,0,0,0,0,0,0,0,0,0,0,0,0,0,0,0,0,0,0,0,0,0,0,0,0,0,0,0x5,0x10,0,0x2,0x20,0,0x6,0,0,0,0,0,0,0,0,0,0,0,0,0,0,0,0,0,0,0,0,0,0,0,0,0,0,0,0,0,0,0,0,0,0,0,0,0,0,0,0,0,0,0,0,0,0,0,0,0,0,0,0,0,0,0,0,0,0x5,0x10,0x1,0x2,0x20,0,0x6,0,0,0,0,0,0,0,0,0,0,0,0,0,0,0,0,0,0,0,0,0,0,0,0,0,0,0,0,0,0,0,0,0,0,0,0,0,0,0,0,0,0,0,0,0,0,0,0,0,0,0,0,0,0,0,0,0,0x1,0x40,0x30,0x3258000,0x400,0x160000,0x14,0x80007d00,0x3,0x4,0x5,0x402c1800,0,0,0,0,0x3,0x4,0x1,0x402c1800,0,0,0,0,0x4,0x58,0x7000000,0x10001,0xda00,0x1080108,0x2200,0,0x4,0x28,0x6000000,0x1,0x13a00,0x2010c32,0x4800,0,0,0,0,0,0,0,0,0,0,0,0,0,0,0,0,0,0,0,0,0,0,0,0,0,0x1,0x40,0x30,0x3558000,0xa00,0x160000,0x14,0x80008f00,0x3,0xa,0x5,0x402c1800,0,0,0,0,0x3,0xa,0x1,0x402c1800,0,0,0,0,0x4,0x58,0x7000000,0x10001,0xea00,0x1080118,0x2200,0,0x4,0x28,0x6000000,0x1,0x14a00,0x2010c30,0x4800,0,0,0,0,0,0,0,0,0,0,0,0,0,0,0,0,0,0,0,0,0,0,0,0,0,0x1,0x40,0x30,0x3858000,0x1000,0x160000,0x14,0x8000a100,0x3,0x10,0x5,0x402c1800,0,0,0,0,0x3,0x10,0x1,0x402c1800,0,0,0,0,0x4,0x58,0x7000000,0x10001,0xfa00,0x1080128,0x2200,0,0x4,0x28,0x6000000,0x1,0x15a00,0x2010c2e,0x4800,0,0,0,0,0,0,0,0,0,0,0,0,0,0,0,0,0,0,0,0,0,0,0,0,0,0x1,0x40,0x30,0x3b58000,0x1600,0x160000,0x14,0x8000b300,0x3,0x16,0x5,0x402c1800,0,0,0,0,0x3,0x16,0x1,0x402c1800,0,0,0,0,0x4,0x58,0x7000000,0x10001,0x10a00,0x1080138,0x2200,0,0x4,0x28,0x6000000,0x1,0x16a00,0x2010c2c,0x4800,0,0,0,0,0,0,0,0,0,0,0,0,0,0,0,0,0,0,0,0,0,0,0,0,0,0x2,0x20,0,0x2700000,0xe00,0,0x40,0x384,0x4,0x28,0x6000000,0x1,0x11a00,0x10a010a,0x4800,0,0x4,0x28,0x6000000,0x1,0x17a00,0x2010c2a,0x4800,0,0,0,0,0,0,0,0,0,0,0,0,0,0,0,0,0,0,0,0,0,0,0,0,0,0,0,0,0,0,0,0,0,0,0,0,0,0,0,0,0,0x1,0x18,0,0x1c00000,0x1800,0,0x40,0x600,0x4,0x28,0x6000000,0x1,0x12a00,0x10a011e,0x4800,0,0x4,0x28,0x6000000,0x1,0x18a00,0x2010c28,0x4800,0,0,0,0,0,0,0,0,0,0,0,0,0,0,0,0,0,0,0,0,0,0,0,0,0,0,0,0,0,0,0,0,0,0,0,0,0,0,0,0,0,0x5,0x14,0,0x2,0x20,0,0x6,0,0,0,0,0,0,0,0,0,0,0,0,0,0,0,0,0,0,0,0,0,0,0,0,0,0,0,0,0,0,0,0,0,0,0,0,0,0,0,0,0,0,0,0,0,0,0,0,0,0,0,0,0,0,0,0,0,0x5,0x14,0x1,0x2,0x20,0,0x6,0,0,0,0,0,0,0,0,0,0,0,0,0,0,0,0,0,0,0,0,0,0,0,0,0,0,0,0,0,0,0,0,0,0,0,0,0,0,0,0,0,0,0,0,0,0,0,0,0,0,0,0,0,0,0,0,0,0x1,0x48,0x38,0x4200000,0x400,0,0x18,0x7e00,0x3,0x5,0x7,0x402c1800,0,0,0,0,0x4,0x58,0x7010000,0x10101,0xda00,0x1080108,0x2200,0,0x4,0x28,0x6010000,0x101,0x13a00,0x2010c32,0x4800,0,0,0,0,0,0,0,0,0,0,0,0,0,0,0,0,0,0,0,0,0,0,0,0,0,0,0,0,0,0,0,0,0,0x1,0x48,0x38,0x4500000,0xa00,0,0x18,0x9000,0x3,0xb,0x7,0x402c1800,0,0,0,0,0x4,0x58,0x7010000,0x10101,0xea00,0x1080118,0x2200,0,0x4,0x28,0x6010000,0x101,0x14a00,0x2010c30,0x4800,0,0,0,0,0,0,0,0,0,0,0,0,0,0,0,0,0,0,0,0,0,0,0,0,0,0,0,0,0,0,0,0,0,0x1,0x48,0x38,0x4800000,0x1000,0,0x18,0xa200,0x3,0x11,0x7,0x402c1800,0,0,0,0,0x4,0x58,0x7010000,0x10101,0xfa00,0x1080128,0x2200,0,0x4,0x28,0x6010000,0x101,0x15a00,0x2010c2e,0x4800,0,0,0,0,0,0,0,0,0,0,0,0,0,0,0,0,0,0,0,0,0,0,0,0,0,0,0,0,0,0,0,0,0,0x1,0x48,0x38,0x4b00000,0x1600,0,0x18,0xb400,0x3,0x17,0x7,0x402c1800,0,0,0,0,0x4,0x58,0x7010000,0x10101,0x10a00,0x1080138,0x2200,0,0x4,0x28,0x6010000,0x101,0x16a00,0x2010c2c,0x4800,0,0,0,0,0,0,0,0,0,0,0,0,0,0,0,0,0,0,0,0,0,0,0,0,0,0,0,0,0,0,0,0,0,0x1,0x10,0,0x700000,0xe00,0,0x40,0x80003600,0x4,0x28,0x6010000,0x101,0x11a00,0x10a010a,0x4800,0,0x4,0x28,0x6010000,0x101,0x17a00,0x2010c2a,0x4800,0,0,0,0,0,0,0,0,0,0,0,0,0,0,0,0,0,0,0,0,0,0,0,0,0,0,0,0,0,0,0,0,0,0,0,0,0,0,0,0,0,0x2,0x20,0,0x2c00000,0x1800,0,0x40,0x604,0x4,0x28,0x6010000,0x101,0x12a00,0x10a011e,0x4800,0,0x4,0x28,0x6010000,0x101,0x18a00,0x2010c28,0x4800,0,0,0,0,0,0,0,0,0,0,0,0,0,0,0,0,0,0,0,0,0,0,0,0,0,0,0,0,0,0,0,0,0,0,0,0,0,0,0,0,0,0x5,0x18,0,0x2,0x20,0,0x6,0,0,0,0,0,0,0,0,0,0,0,0,0,0,0,0,0,0,0,0,0,0,0,0,0,0,0,0,0,0,0,0,0,0,0,0,0,0,0,0,0,0,0,0,0,0,0,0,0,0,0,0,0,0,0,0,0,0x5,0x18,0x1,0x2,0x20,0,0x6,0,0,0,0,0,0,0,0,0,0,0,0,0,0,0,0,0,0,0,0,0,0,0,0,0,0,0,0,0,0,0,0,0,0,0,0,0,0,0,0,0,0,0,0,0,0,0,0,0,0,0,0,0,0,0,0,0,0x1,0x48,0x38,0x4230000,0x400,0xc0000,0x14,0x7e80,0x3,0x5,0x6,0x402c1800,0,0,0,0,0x3,0x5,0,0x402c1800,0,0,0,0,0x4,0x58,0x7020000,0x10201,0xda00,0x1080108,0x2200,0,0x4,0x28,0x6020000,0x201,0x13a00,0x2010c32,0x4800,0,0,0,0,0,0,0,0,0,0,0,0,0,0,0,0,0,0,0,0,0,0,0,0,0,0x1,0x48,0x38,0x4530000,0xa00,0xc0000,0x14,0x9080,0x3,0xb,0x6,0x402c1800,0,0,0,0,0x3,0xb,0,0x402c1800,0,0,0,0,0x4,0x58,0x7020000,0x10201,0xea00,0x1080118,0x2200,0,0x4,0x28,0x6020000,0x201,0x14a00,0x2010c30,0x4800,0,0,0,0,0,0,0,0,0,0,0,0,0,0,0,0,0,0,0,0,0,0,0,0,0,0x1,0x48,0x38,0x4830000,0x1000,0xc0000,0x14,0xa280,0x3,0x11,0x6,0x402c1800,0,0,0,0,0x3,0x11,0,0x402c1800,0,0,0,0,0x4,0x58,0x7020000,0x10201,0xfa00,0x1080128,0x2200,0,0x4,0x28,0x6020000,0x201,0x15a00,0x2010c2e,0x4800,0,0,0,0,0,0,0,0,0,0,0,0,0,0,0,0,0,0,0,0,0,0,0,0,0,0x1,0x48,0x38,0x4b30000,0x1600,0xc0000,0x14,0xb480,0x3,0x17,0x6,0x402c1800,0,0,0,0,0x3,0x17,0,0x402c1800,0,0,0,0,0x4,0x58,0x7020000,0x10201,0x10a00,0x1080138,0x2200,0,0x4,0x28,0x6020000,0x201,0x16a00,0x2010c2c,0x4800,0,0,0,0,0,0,0,0,0,0,0,0,0,0,0,0,0,0,0,0,0,0,0,0,0,0x1,0x18,0,0x1800000,0x1000,0,0x40,0x400,0x4,0x28,0x6020000,0x201,0x11a00,0x10a010a,0x4800,0,0x4,0x28,0x6020000,0x201,0x17a00,0x2010c2a,0x4800,0,0,0,0,0,0,0,0,0,0,0,0,0,0,0,0,0,0,0,0,0,0,0,0,0,0,0,0,0,0,0,0,0,0,0,0,0,0,0,0,0,0x1,0x10,0,0xc00000,0x1800,0,0x40,0x80004000,0x4,0x28,0x6020000,0x201,0x12a00,0x10a011e,0x4800,0,0x4,0x28,0x6020000,0x201,0x18a00,0x2010c28,0x4800,0,0,0,0,0,0,0,0,0,0,0,0,0,0,0,0,0,0,0,0,0,0,0,0,0,0,0,0,0,0,0,0,0,0,0,0,0,0,0,0,0,0x5,0x1c,0,0x2,0x20,0,0x6,0,0,0,0,0,0,0,0,0,0,0,0,0,0,0,0,0,0,0,0,0,0,0,0,0,0,0,0,0,0,0,0,0,0,0,0,0,0,0,0,0,0,0,0,0,0,0,0,0,0,0,0,0,0,0,0,0,0x5,0x1c,0x1,0x2,0x20,0,0x6,0,0,0,0,0,0,0,0,0,0,0,0,0,0,0,0,0,0,0,0,0,0,0,0,0,0,0,0,0,0,0,0,0,0,0,0,0,0,0,0,0,0,0,0,0,0,0,0,0,0,0,0,0,0,0,0,0,0x1,0x48,0x38,0x4258000,0x400,0x160000,0x14,0x7f00,0x3,0x5,0x5,0x402c1800,0,0,0,0,0x3,0x5,0x1,0x402c1800,0,0,0,0,0x4,0x58,0x7030000,0x10301,0xda00,0x1080108,0x2200,0,0x4,0x28,0x6030000,0x301,0x13a00,0x2010c32,0x4800,0,0,0,0,0,0,0,0,0,0,0,0,0,0,0,0,0,0,0,0,0,0,0,0,0,0x1,0x48,0x38,0x4558000,0xa00,0x160000,0x14,0x9100,0x3,0xb,0x5,0x402c1800,0,0,0,0,0x3,0xb,0x1,0x402c1800,0,0,0,0,0x4,0x58,0x7030000,0x10301,0xea00,0x1080118,0x2200,0,0x4,0x28,0x6030000,0x301,0x14a00,0x2010c30,0x4800,0,0,0,0,0,0,0,0,0,0,0,0,0,0,0,0,0,0,0,0,0,0,0,0,0,0x1,0x48,0x38,0x4858000,0x1000,0x160000,0x14,0xa300,0x3,0x11,0x5,0x402c1800,0,0,0,0,0x3,0x11,0x1,0x402c1800,0,0,0,0,0x4,0x58,0x7030000,0x10301,0xfa00,0x1080128,0x2200,0,0x4,0x28,0x6030000,0x301,0x15a00,0x2010c2e,0x4800,0,0,0,0,0,0,0,0,0,0,0,0,0,0,0,0,0,0,0,0,0,0,0,0,0,0x1,0x48,0x38,0x4b58000,0x1600,0x160000,0x14,0xb500,0x3,0x17,0x5,0x402c1800,0,0,0,0,0x3,0x17,0x1,0x402c1800,0,0,0,0,0x4,0x58,0x7030000,0x10301,0x10a00,0x1080138,0x2200,0,0x4,0x28,0x6030000,0x301,0x16a00,0x2010c2c,0x4800,0,0,0,0,0,0,0,0,0,0,0,0,0,0,0,0,0,0,0,0,0,0,0,0,0,0x2,0x20,0,0x2800000,0x1000,0,0x40,0x404,0x4,0x28,0x6030000,0x301,0x11a00,0x10a010a,0x4800,0,0x4,0x28,0x6030000,0x301,0x17a00,0x2010c2a,0x4800,0,0,0,0,0,0,0,0,0,0,0,0,0,0,0,0,0,0,0,0,0,0,0,0,0,0,0,0,0,0,0,0,0,0,0,0,0,0,0,0,0,0x1,0x18,0,0x1d00000,0x1a00,0,0x40,0x680,0x4,0x28,0x6030000,0x301,0x12a00,0x10a011e,0x4800,0,0x4,0x28,0x6030000,0x301,0x18a00,0x2010c28,0x4800,0,0,0,0,0,0,0,0,0,0,0,0,0,0,0,0,0,0,0,0,0,0,0,0,0,0,0,0,0,0,0,0,0,0,0,0,0,0,0,0,0,0,0,0,0,0,0,0,0,0,0,0,0,0,0,0,0,0,0,0,0,0,0,0,0,0,0,0,0,0,0,0,0,0,0,0,0,0,0,0,0,0,0,0,0,0,0,0,0,0,0,0,0,0,0,0,0,0,0,0,0,0,0,0,0,0,0,0,0,0,0,0,0,0,0,0,0,0,0,0,0,0,0,0,0,0,0,0,0,0,0,0,0,0,0,0,0,0,0,0,0,0,0,0,0,0,0,0,0,0,0,0,0,0,0,0,0,0,0,0,0,0,0,0,0,0,0,0,0,0x2,0x50,0x38,0x5200000,0x400,0,0x18,0x8000,0x3,0x4,0x3,0x402c1800,0,0,0,0,0x3,0x5,0x3,0x402c1800,0,0,0,0,0x4,0x58,0x7000000,0x10002,0xdc00,0x1080108,0x2200,0,0x4,0x28,0x6000000,0x2,0x13c00,0x2010c32,0x4800,0,0,0,0,0,0,0,0,0,0,0,0,0,0,0,0,0,0,0,0,0,0,0,0,0,0x2,0x50,0x38,0x5500000,0xa00,0,0x18,0x9200,0x3,0xa,0x3,0x402c1800,0,0,0,0,0x3,0xb,0x3,0x402c1800,0,0,0,0,0x4,0x58,0x7000000,0x10002,0xec00,0x1080118,0x2200,0,0x4,0x28,0x6000000,0x2,0x14c00,0x2010c30,0x4800,0,0,0,0,0,0,0,0,0,0,0,0,0,0,0,0,0,0,0,0,0,0,0,0,0,0x2,0x50,0x38,0x5800000,0x1000,0,0x18,0xa400,0x3,0x10,0x3,0x402c1800,0,0,0,0,0x3,0x11,0x3,0x402c1800,0,0,0,0,0x4,0x58,0x7000000,0x10002,0xfc00,0x1080128,0x2200,0,0x4,0x28,0x6000000,0x2,0x15c00,0x2010c2e,0x4800,0,0,0,0,0,0,0,0,0,0,0,0,0,0,0,0,0,0,0,0,0,0,0,0,0,0x2,0x50,0x38,0x5b00000,0x1600,0,0x18,0xb600,0x3,0x16,0x3,0x402c1800,0,0,0,0,0x3,0x17,0x3,0x402c1800,0,0,0,0,0x4,0x58,0x7000000,0x10002,0x10c00,0x1080138,0x2200,0,0x4,0x28,0x6000000,0x2,0x16c00,0x2010c2c,0x4800,0,0,0,0,0,0,0,0,0,0,0,0,0,0,0,0,0,0,0,0,0,0,0,0,0,0x1,0x10,0,0x800000,0x1000,0,0x40,0x80003800,0x4,0x28,0x6000000,0x2,0x11c00,0x10a010a,0x4800,0,0x4,0x28,0x6000000,0x2,0x17c00,0x2010c2a,0x4800,0,0,0,0,0,0,0,0,0,0,0,0,0,0,0,0,0,0,0,0,0,0,0,0,0,0,0,0,0,0,0,0,0,0,0,0,0,0,0,0,0,0x2,0x20,0,0x2d00000,0x1a00,0,0x40,0x684,0x4,0x28,0x6000000,0x2,0x12c00,0x10a011e,0x4800,0,0x4,0x28,0x6000000,0x2,0x18c00,0x2010c28,0x4800,0,0,0,0,0,0,0,0,0,0,0,0,0,0,0,0,0,0,0,0,0,0,0,0,0,0,0,0,0,0,0,0,0,0,0,0,0,0,0,0,0,0,0,0,0,0,0,0,0,0,0,0,0,0,0,0,0,0,0,0,0,0,0,0,0,0,0,0,0,0,0,0,0,0,0,0,0,0,0,0,0,0,0,0,0,0,0,0,0,0,0,0,0,0,0,0,0,0,0,0,0,0,0,0,0,0,0,0,0,0,0,0,0,0,0,0,0,0,0,0,0,0,0,0,0,0,0,0,0,0,0,0,0,0,0,0,0,0,0,0,0,0,0,0,0,0,0,0,0,0,0,0,0,0,0,0,0,0,0,0,0,0,0,0,0,0,0,0,0,0x2,0x50,0x38,0x5230000,0x400,0xc0000,0x14,0x8080,0x3,0x4,0x4,0x402c1800,0,0,0,0,0x3,0x4,0x2,0x402c1800,0,0,0,0,0x4,0x58,0x7010000,0x10102,0xdc00,0x1080108,0x2200,0,0x4,0x28,0x6010000,0x102,0x13c00,0x2010c32,0x4800,0,0,0,0,0,0,0,0,0,0,0,0,0,0,0,0,0,0,0,0,0,0,0,0,0,0x2,0x50,0x38,0x5530000,0xa00,0xc0000,0x14,0x9280,0x3,0xa,0x4,0x402c1800,0,0,0,0,0x3,0xa,0x2,0x402c1800,0,0,0,0,0x4,0x58,0x7010000,0x10102,0xec00,0x1080118,0x2200,0,0x4,0x28,0x6010000,0x102,0x14c00,0x2010c30,0x4800,0,0,0,0,0,0,0,0,0,0,0,0,0,0,0,0,0,0,0,0,0,0,0,0,0,0x2,0x50,0x38,0x5830000,0x1000,0xc0000,0x14,0xa480,0x3,0x10,0x4,0x402c1800,0,0,0,0,0x3,0x10,0x2,0x402c1800,0,0,0,0,0x4,0x58,0x7010000,0x10102,0xfc00,0x1080128,0x2200,0,0x4,0x28,0x6010000,0x102,0x15c00,0x2010c2e,0x4800,0,0,0,0,0,0,0,0,0,0,0,0,0,0,0,0,0,0,0,0,0,0,0,0,0,0x2,0x50,0x38,0x5b30000,0x1600,0xc0000,0x14,0xb680,0x3,0x16,0x4,0x402c1800,0,0,0,0,0x3,0x16,0x2,0x402c1800,0,0,0,0,0x4,0x58,0x7010000,0x10102,0x10c00,0x1080138,0x2200,0,0x4,0x28,0x6010000,0x102,0x16c00,0x2010c2c,0x4800,0,0,0,0,0,0,0,0,0,0,0,0,0,0,0,0,0,0,0,0,0,0,0,0,0,0x1,0x18,0,0x1900000,0x1200,0,0x40,0x480,0x4,0x28,0x6010000,0x102,0x11c00,0x10a010a,0x4800,0,0x4,0x28,0x6010000,0x102,0x17c00,0x2010c2a,0x4800,0,0,0,0,0,0,0,0,0,0,0,0,0,0,0,0,0,0,0,0,0,0,0,0,0,0,0,0,0,0,0,0,0,0,0,0,0,0,0,0,0,0x1,0x10,0,0xd00000,0x1a00,0,0x40,0x80004200,0x4,0x28,0x6010000,0x102,0x12c00,0x10a011e,0x4800,0,0x4,0x28,0x6010000,0x102,0x18c00,0x2010c28,0x4800,0,0,0,0,0,0,0,0,0,0,0,0,0,0,0,0,0,0,0,0,0,0,0,0,0,0,0,0,0,0,0,0,0,0,0,0,0,0,0,0,0,0,0,0,0,0,0,0,0,0,0,0,0,0,0,0,0,0,0,0,0,0,0,0,0,0,0,0,0,0,0,0,0,0,0,0,0,0,0,0,0,0,0,0,0,0,0,0,0,0,0,0,0,0,0,0,0,0,0,0,0,0,0,0,0,0,0,0,0,0,0,0,0,0,0,0,0,0,0,0,0,0,0,0,0,0,0,0,0,0,0,0,0,0,0,0,0,0,0,0,0,0,0,0,0,0,0,0,0,0,0,0,0,0,0,0,0,0,0,0,0,0,0,0,0,0,0,0,0,0x2,0x50,0x38,0x5258000,0x400,0x160000,0x14,0x8100,0x3,0x5,0x4,0x402c1800,0,0,0,0,0x3,0x5,0x2,0x402c1800,0,0,0,0,0x4,0x58,0x7020000,0x10202,0xdc00,0x1080108,0x2200,0,0x4,0x28,0x6020000,0x202,0x13c00,0x2010c32,0x4800,0,0,0,0,0,0,0,0,0,0,0,0,0,0,0,0,0,0,0,0,0,0,0,0,0,0x2,0x50,0x38,0x5558000,0xa00,0x160000,0x14,0x9300,0x3,0xb,0x4,0x402c1800,0,0,0,0,0x3,0xb,0x2,0x402c1800,0,0,0,0,0x4,0x58,0x7020000,0x10202,0xec00,0x1080118,0x2200,0,0x4,0x28,0x6020000,0x202,0x14c00,0x2010c30,0x4800,0,0,0,0,0,0,0,0,0,0,0,0,0,0,0,0,0,0,0,0,0,0,0,0,0,0x2,0x50,0x38,0x5858000,0x1000,0x160000,0x14,0xa500,0x3,0x11,0x4,0x402c1800,0,0,0,0,0x3,0x11,0x2,0x402c1800,0,0,0,0,0x4,0x58,0x7020000,0x10202,0xfc00,0x1080128,0x2200,0,0x4,0x28,0x6020000,0x202,0x15c00,0x2010c2e,0x4800,0,0,0,0,0,0,0,0,0,0,0,0,0,0,0,0,0,0,0,0,0,0,0,0,0,0x2,0x50,0x38,0x5b58000,0x1600,0x160000,0x14,0xb700,0x3,0x17,0x4,0x402c1800,0,0,0,0,0x3,0x17,0x2,0x402c1800,0,0,0,0,0x4,0x58,0x7020000,0x10202,0x10c00,0x1080138,0x2200,0,0x4,0x28,0x6020000,0x202,0x16c00,0x2010c2c,0x4800,0,0,0,0,0,0,0,0,0,0,0,0,0,0,0,0,0,0,0,0,0,0,0,0,0,0x2,0x20,0,0x2900000,0x1200,0,0x40,0x484,0x4,0x28,0x6020000,0x202,0x11c00,0x10a010a,0x4800,0,0x4,0x28,0x6020000,0x202,0x17c00,0x2010c2a,0x4800,0,0,0,0,0,0,0,0,0,0,0,0,0,0,0,0,0,0,0,0,0,0,0,0,0,0,0,0,0,0,0,0,0,0,0,0,0,0,0,0,0,0x1,0x18,0,0x1e00000,0x1c00,0,0x40,0x700,0x4,0x28,0x6020000,0x202,0x12c00,0x10a011e,0x4800,0,0x4,0x28,0x6020000,0x202,0x18c00,0x2010c28,0x4800,0,0,0,0,0,0,0,0,0,0,0,0,0,0,0,0,0,0,0,0,0,0,0,0,0,0,0,0,0,0,0,0,0,0,0,0,0,0,0,0,0,0,0,0,0,0,0,0,0,0,0,0,0,0,0,0,0,0,0,0,0,0,0,0,0,0,0,0,0,0,0,0,0,0,0,0,0,0,0,0,0,0,0,0,0,0,0,0,0,0,0,0,0,0,0,0,0,0,0,0,0,0,0,0,0,0,0,0,0,0,0,0,0,0,0,0,0,0,0,0,0,0,0,0,0,0,0,0,0,0,0,0,0,0,0,0,0,0,0,0,0,0,0,0,0,0,0,0,0,0,0,0,0,0,0,0,0,0,0,0,0,0,0,0,0,0,0,0,0,0x1,0x18,0,0x1000000,0,0,0x40,0,0x4,0x58,0x7030000,0x10302,0xdc00,0x1080108,0x2200,0,0x4,0x28,0x6030000,0x302,0x13c00,0x2010c32,0x4800,0,0,0,0,0,0,0,0,0,0,0,0,0,0,0,0,0,0,0,0,0,0,0,0,0,0,0,0,0,0,0,0,0,0,0,0,0,0,0,0,0,0x1,0x10,0,0x100000,0x200,0,0x40,0x80002a00,0x4,0x58,0x7030000,0x10302,0xec00,0x1080118,0x2200,0,0x4,0x28,0x6030000,0x302,0x14c00,0x2010c30,0x4800,0,0,0,0,0,0,0,0,0,0,0,0,0,0,0,0,0,0,0,0,0,0,0,0,0,0,0,0,0,0,0,0,0,0,0,0,0,0,0,0,0,0x2,0x20,0,0x2300000,0x600,0,0x40,0x184,0x4,0x58,0x7030000,0x10302,0xfc00,0x1080128,0x2200,0,0x4,0x28,0x6030000,0x302,0x15c00,0x2010c2e,0x4800,0,0,0,0,0,0,0,0,0,0,0,0,0,0,0,0,0,0,0,0,0,0,0,0,0,0,0,0,0,0,0,0,0,0,0,0,0,0,0,0,0,0x1,0x18,0,0x1500000,0xa00,0,0x40,0x280,0x4,0x58,0x7030000,0x10302,0x10c00,0x1080138,0x2200,0,0x4,0x28,0x6030000,0x302,0x16c00,0x2010c2c,0x4800,0,0,0,0,0,0,0,0,0,0,0,0,0,0,0,0,0,0,0,0,0,0,0,0,0,0,0,0,0,0,0,0,0,0,0,0,0,0,0,0,0,0x1,0x10,0,0x900000,0x1200,0,0x40,0x80003a00,0x4,0x28,0x6030000,0x302,0x11c00,0x10a010a,0x4800,0,0x4,0x28,0x6030000,0x302,0x17c00,0x2010c2a,0x4800,0,0,0,0,0,0,0,0,0,0,0,0,0,0,0,0,0,0,0,0,0,0,0,0,0,0,0,0,0,0,0,0,0,0,0,0,0,0,0,0,0,0x2,0x20,0,0x2e00000,0x1c00,0,0x40,0x704,0x4,0x28,0x6030000,0x302,0x12c00,0x10a011e,0x4800,0,0x4,0x28,0x6030000,0x302,0x18c00,0x2010c28,0x4800,0,0,0,0,0,0,0,0,0,0,0,0,0,0,0,0,0,0,0,0,0,0,0,0,0,0,0,0,0,0,0,0,0,0,0,0,0,0,0,0,0,0,0,0,0,0,0,0,0,0,0,0,0,0,0,0,0,0,0,0,0,0,0,0,0,0,0,0,0,0,0,0,0,0,0,0,0,0,0,0,0,0,0,0,0,0,0,0,0,0,0,0,0,0,0,0,0,0,0,0,0,0,0,0,0,0,0,0,0,0,0,0,0,0,0,0,0,0,0,0,0,0,0,0,0,0,0,0,0,0,0,0,0,0,0,0,0,0,0,0,0,0,0,0,0,0,0,0,0,0,0,0,0,0,0,0,0,0,0,0,0,0,0,0,0,0,0,0,0,0x2,0x20,0,0x2000000,0,0,0x40,0x4,0x4,0x58,0x7000000,0x10003,0xde00,0x1080108,0x2200,0,0x4,0x28,0x6000000,0x3,0x13e00,0x2010c32,0x4800,0,0,0,0,0,0,0,0,0,0,0,0,0,0,0,0,0,0,0,0,0,0,0,0,0,0,0,0,0,0,0,0,0,0,0,0,0,0,0,0,0,0x1,0x18,0,0x1200000,0x400,0,0x40,0x100,0x4,0x58,0x7000000,0x10003,0xee00,0x1080118,0x2200,0,0x4,0x28,0x6000000,0x3,0x14e00,0x2010c30,0x4800,0,0,0,0,0,0,0,0,0,0,0,0,0,0,0,0,0,0,0,0,0,0,0,0,0,0,0,0,0,0,0,0,0,0,0,0,0,0,0,0,0,0x1,0x10,0,0x300000,0x600,0,0x40,0x80002e00,0x4,0x58,0x7000000,0x10003,0xfe00,0x1080128,0x2200,0,0x4,0x28,0x6000000,0x3,0x15e00,0x2010c2e,0x4800,0,0,0,0,0,0,0,0,0,0,0,0,0,0,0,0,0,0,0,0,0,0,0,0,0,0,0,0,0,0,0,0,0,0,0,0,0,0,0,0,0,0x2,0x20,0,0x2500000,0xa00,0,0x40,0x284,0x4,0x58,0x7000000,0x10003,0x10e00,0x1080138,0x2200,0,0x4,0x28,0x6000000,0x3,0x16e00,0x2010c2c,0x4800,0,0,0,0,0,0,0,0,0,0,0,0,0,0,0,0,0,0,0,0,0,0,0,0,0,0,0,0,0,0,0,0,0,0,0,0,0,0,0,0,0,0x1,0x18,0,0x1a00000,0x1400,0,0x40,0x500,0x4,0x28,0x6000000,0x3,0x11e00,0x10a010a,0x4800,0,0x4,0x28,0x6000000,0x3,0x17e00,0x2010c2a,0x4800,0,0,0,0,0,0,0,0,0,0,0,0,0,0,0,0,0,0,0,0,0,0,0,0,0,0,0,0,0,0,0,0,0,0,0,0,0,0,0,0,0,0x1,0x10,0,0xe00000,0x1c00,0,0x40,0x80004400,0x4,0x28,0x6000000,0x3,0x12e00,0x10a011e,0x4800,0,0x4,0x28,0x6000000,0x3,0x18e00,0x2010c28,0x4800,0,0,0,0,0,0,0,0,0,0,0,0,0,0,0,0,0,0,0,0,0,0,0,0,0,0,0,0,0,0,0,0,0,0,0,0,0,0,0,0,0,0,0,0,0,0,0,0,0,0,0,0,0,0,0,0,0,0,0,0,0,0,0,0,0,0,0,0,0,0,0,0,0,0,0,0,0,0,0,0,0,0,0,0,0,0,0,0,0,0,0,0,0,0,0,0,0,0,0,0,0,0,0,0,0,0,0,0,0,0,0,0,0,0,0,0,0,0,0,0,0,0,0,0,0,0,0,0,0,0,0,0,0,0,0,0,0,0,0,0,0,0,0,0,0,0,0,0,0,0,0,0,0,0,0,0,0,0,0,0,0,0,0,0,0,0,0,0,0,0x1,0x10,0,0,0,0,0x40,0x80002800,0x4,0x58,0x7010000,0x10103,0xde00,0x1080108,0x2200,0,0x4,0x28,0x6010000,0x103,0x13e00,0x2010c32,0x4800,0,0,0,0,0,0,0,0,0,0,0,0,0,0,0,0,0,0,0,0,0,0,0,0,0,0,0,0,0,0,0,0,0,0,0,0,0,0,0,0,0,0x2,0x20,0,0x2200000,0x400,0,0x40,0x104,0x4,0x58,0x7010000,0x10103,0xee00,0x1080118,0x2200,0,0x4,0x28,0x6010000,0x103,0x14e00,0x2010c30,0x4800,0,0,0,0,0,0,0,0,0,0,0,0,0,0,0,0,0,0,0,0,0,0,0,0,0,0,0,0,0,0,0,0,0,0,0,0,0,0,0,0,0,0x1,0x18,0,0x1400000,0x800,0,0x40,0x200,0x4,0x58,0x7010000,0x10103,0xfe00,0x1080128,0x2200,0,0x4,0x28,0x6010000,0x103,0x15e00,0x2010c2e,0x4800,0,0,0,0,0,0,0,0,0,0,0,0,0,0,0,0,0,0,0,0,0,0,0,0,0,0,0,0,0,0,0,0,0,0,0,0,0,0,0,0,0,0x1,0x10,0,0x500000,0xa00,0,0x40,0x80003200,0x4,0x58,0x7010000,0x10103,0x10e00,0x1080138,0x2200,0,0x4,0x28,0x6010000,0x103,0x16e00,0x2010c2c,0x4800,0,0,0,0,0,0,0,0,0,0,0,0,0,0,0,0,0,0,0,0,0,0,0,0,0,0,0,0,0,0,0,0,0,0,0,0,0,0,0,0,0,0x2,0x20,0,0x2a00000,0x1400,0,0x40,0x504,0x4,0x28,0x6010000,0x103,0x11e00,0x10a010a,0x4800,0,0x4,0x28,0x6010000,0x103,0x17e00,0x2010c2a,0x4800,0,0,0,0,0,0,0,0,0,0,0,0,0,0,0,0,0,0,0,0,0,0,0,0,0,0,0,0,0,0,0,0,0,0,0,0,0,0,0,0,0,0x1,0x18,0,0x1f00000,0x1e00,0,0x40,0x780,0x4,0x28,0x6010000,0x103,0x12e00,0x10a011e,0x4800,0,0x4,0x28,0x6010000,0x103,0x18e00,0x2010c28,0x4800,0,0,0,0,0,0,0,0,0,0,0,0,0,0,0,0,0,0,0,0,0,0,0,0,0,0,0,0,0,0,0,0,0,0,0,0,0,0,0,0,0,0,0,0,0,0,0,0,0,0,0,0,0,0,0,0,0,0,0,0,0,0,0,0,0,0,0,0,0,0,0,0,0,0,0,0,0,0,0,0,0,0,0,0,0,0,0,0,0,0,0,0,0,0,0,0,0,0,0,0,0,0,0,0,0,0,0,0,0,0,0,0,0,0,0,0,0,0,0,0,0,0,0,0,0,0,0,0,0,0,0,0,0,0,0,0,0,0,0,0,0,0,0,0,0,0,0,0,0,0,0,0,0,0,0,0,0,0,0,0,0,0,0,0,0,0,0,0,0,0x1,0x18,0,0x1100000,0x200,0,0x40,0x80,0x4,0x58,0x7020000,0x10203,0xde00,0x1080108,0x2200,0,0x4,0x28,0x6020000,0x203,0x13e00,0x2010c32,0x4800,0,0,0,0,0,0,0,0,0,0,0,0,0,0,0,0,0,0,0,0,0,0,0,0,0,0,0,0,0,0,0,0,0,0,0,0,0,0,0,0,0,0x1,0x10,0,0x200000,0x400,0,0x40,0x80002c00,0x4,0x58,0x7020000,0x10203,0xee00,0x1080118,0x2200,0,0x4,0x28,0x6020000,0x203,0x14e00,0x2010c30,0x4800,0,0,0,0,0,0,0,0,0,0,0,0,0,0,0,0,0,0,0,0,0,0,0,0,0,0,0,0,0,0,0,0,0,0,0,0,0,0,0,0,0,0x2,0x20,0,0x2400000,0x800,0,0x40,0x204,0x4,0x58,0x7020000,0x10203,0xfe00,0x1080128,0x2200,0,0x4,0x28,0x6020000,0x203,0x15e00,0x2010c2e,0x4800,0,0,0,0,0,0,0,0,0,0,0,0,0,0,0,0,0,0,0,0,0,0,0,0,0,0,0,0,0,0,0,0,0,0,0,0,0,0,0,0,0,0x1,0x18,0,0x1600000,0xc00,0,0x40,0x300,0x4,0x58,0x7020000,0x10203,0x10e00,0x1080138,0x2200,0,0x4,0x28,0x6020000,0x203,0x16e00,0x2010c2c,0x4800,0,0,0,0,0,0,0,0,0,0,0,0,0,0,0,0,0,0,0,0,0,0,0,0,0,0,0,0,0,0,0,0,0,0,0,0,0,0,0,0,0,0x1,0x10,0,0xa00000,0x1400,0,0x40,0x80003c00,0x4,0x28,0x6020000,0x203,0x11e00,0x10a010a,0x4800,0,0x4,0x28,0x6020000,0x203,0x17e00,0x2010c2a,0x4800,0,0,0,0,0,0,0,0,0,0,0,0,0,0,0,0,0,0,0,0,0,0,0,0,0,0,0,0,0,0,0,0,0,0,0,0,0,0,0,0,0,0x2,0x20,0,0x2f00000,0x1e00,0,0x40,0x784,0x4,0x28,0x6020000,0x203,0x12e00,0x10a011e,0x4800,0,0x4,0x28,0x6020000,0x203,0x18e00,0x2010c28,0x4800,0,0,0,0,0,0,0,0,0,0,0,0,0,0,0,0,0,0,0,0,0,0,0,0,0,0,0,0,0,0,0,0,0,0,0,0,0,0,0,0,0,0,0,0,0,0,0,0,0,0,0,0,0,0,0,0,0,0,0,0,0,0,0,0,0,0,0,0,0,0,0,0,0,0,0,0,0,0,0,0,0,0,0,0,0,0,0,0,0,0,0,0,0,0,0,0,0,0,0,0,0,0,0,0,0,0x6,0,0,0,0,0,0,0,0,0,0,0,0,0,0,0,0,0,0,0,0,0,0,0,0,0,0,0,0,0,0,0,0,0,0,0,0,0,0,0,0,0,0,0,0,0,0,0,0,0,0,0,0,0,0,0,0,0,0,0,0,0,0,0,0x2,0x20,0,0x2100000,0x200,0,0x40,0x84,0x4,0x58,0x7030000,0x10303,0xde00,0x1080108,0x2200,0,0x4,0x28,0x6030000,0x303,0x13e00,0x2010c32,0x4800,0,0,0,0,0,0,0,0,0,0,0,0,0,0,0,0,0,0,0,0,0,0,0,0,0,0,0,0,0,0,0,0,0,0,0,0,0,0,0,0,0,0x1,0x18,0,0x1300000,0x600,0,0x40,0x180,0x4,0x58,0x7030000,0x10303,0xee00,0x1080118,0x2200,0,0x4,0x28,0x6030000,0x303,0x14e00,0x2010c30,0x4800,0,0,0,0,0,0,0,0,0,0,0,0,0,0,0,0,0,0,0,0,0,0,0,0,0,0,0,0,0,0,0,0,0,0,0,0,0,0,0,0,0,0x1,0x10,0,0x400000,0x800,0,0x40,0x80003000,0x4,0x58,0x7030000,0x10303,0xfe00,0x1080128,0x2200,0,0x4,0x28,0x6030000,0x303,0x15e00,0x2010c2e,0x4800,0,0,0,0,0,0,0,0,0,0,0,0,0,0,0,0,0,0,0,0,0,0,0,0,0,0,0,0,0,0,0,0,0,0,0,0,0,0,0,0,0,0x2,0x20,0,0x2600000,0xc00,0,0x40,0x304,0x4,0x58,0x7030000,0x10303,0x10e00,0x1080138,0x2200,0,0x4,0x28,0x6030000,0x303,0x16e00,0x2010c2c,0x4800,0,0,0,0,0,0,0,0,0,0,0,0,0,0,0,0,0,0,0,0,0,0,0,0,0,0,0,0,0,0,0,0,0,0,0,0,0,0,0,0,0,0x1,0x18,0,0x1b00000,0x1600,0,0x40,0x580,0x4,0x28,0x6030000,0x303,0x11e00,0x10a010a,0x4800,0,0x4,0x28,0x6030000,0x303,0x17e00,0x2010c2a,0x4800,0,0,0,0,0,0,0,0,0,0,0,0,0,0,0,0,0,0,0,0,0,0,0,0,0,0,0,0,0,0,0,0,0,0,0,0,0,0,0,0,0,0x1,0x10,0,0xf00000,0x1e00,0,0x40,0x80004600,0x4,0x28,0x6030000,0x303,0x12e00,0x10a011e,0x4800,0,0x4,0x28,0x6030000,0x303,0x18e00,0x2010c28,0x4800,0,0,0,0,0,0,0,0,0,0,0,0,0,0,0,0,0,0,0,0,0,0,0,0,0,0,0,0,0,0,0,0,0,0,0,0,0,0,0,0,0};
__global__ void __launch_bounds__(256, 1) mega(Params p) {
  __shared__ __attribute__((aligned(16))) char lds[163840];
  auto ka = __builtin_amdgcn_kernarg_segment_ptr();
  unsigned ldsb = (unsigned)(size_t)(__attribute__((address_space(3))) char*)lds;
  asm volatile("s_mov_b64 s[6:7], %4\ns_mov_b64 s[0:1], %0\ns_mov_b32 s2, %1\n"
"s_and_b32 s3, s2, 7\n"
"s_cmp_lt_u32 s3, 2\n"
"s_cbranch_scc0 L_bulk\n"
"s_cmp_lt_u32 s2, 128\n"
"s_cbranch_scc0 L_bulk\n"
"s_lshr_b32 s4, s2, 3\n"
"s_mul_i32 s4, s4, 2\n"
"s_add_u32 s4, s4, s3\n"
"s_load_dwordx4 s[60:63], s[0:1], 0\n"
"s_load_dwordx8 s[64:71], s[0:1], 16\n"
"s_load_dwordx4 s[72:75], s[0:1], 104\n"
"s_load_dwordx4 s[76:79], s[0:1], 120\n"
"v_lshrrev_b32 v1, 6, v0\n"
"v_and_b32 v107, 63, v0\n"
"s_nop 0\n"
"v_readfirstlane_b32 s5, v1\n"
"s_waitcnt lgkmcnt(0)\n"
"s_lshl_b32 s49, s4, 8\n"
"s_lshl_b32 s50, s4, 17\n"
"s_add_u32 s28, s64, 0x0\n"
"s_addc_u32 s29, s65, 0\n"
"s_add_u32 s28, s28, s49\n"
"s_addc_u32 s29, s29, 0\n"
"s_and_b32 s29, s29, 0xffff\n"
"s_mov_b32 s30, 0x400000\n"
"s_mov_b32 s31, 0x20000\n"
"s_sub_u32 s30, s30, s49\n"
"s_add_u32 s80, s66, 0x0\n"
"s_addc_u32 s81, s67, 0\n"
"s_add_u32 s80, s80, s49\n"
"s_addc_u32 s81, s81, 0\n"
"s_and_b32 s81, s81, 0xffff\n"
"s_mov_b32 s82, 0x400000\n"
"s_mov_b32 s83, 0x20000\n"
"s_sub_u32 s82, s82, s49\n"
"s_add_u32 s84, s68, 0x0\n"
"s_addc_u32 s85, s69, 0\n"
"s_add_u32 s84, s84, s49\n"
"s_addc_u32 s85, s85, 0\n"
"s_and_b32 s85, s85, 0xffff\n"
"s_mov_b32 s86, 0x400000\n"
"s_mov_b32 s87, 0x20000\n"
"s_sub_u32 s86, s86, s49\n"
"s_add_u32 s88, s70, 0x0\n"
"s_addc_u32 s89, s71, 0\n"
"s_add_u32 s88, s88, s50\n"
"s_addc_u32 s89, s89, 0\n"
"s_and_b32 s89, s89, 0xffff\n"
"s_mov_b32 s90, 0x20000\n"
"s_mov_b32 s91, 0x20000\n"
"v_mov_b32 v96, 0x0\n"
"v_bfe_i32 v126, v107, 0, 1\n"
"v_and_b32 v126, 0x10, v126\n"
"v_xor_b32 v96, v96, v126\n"
"v_bfe_i32 v126, v107, 1, 1\n"
"v_and_b32 v126, 0x20, v126\n"
"v_xor_b32 v96, v96, v126\n"
"v_bfe_i32 v126, v107, 2, 1\n"
"v_and_b32 v126, 0x40, v126\n"
"v_xor_b32 v96, v96, v126\n"
"v_bfe_i32 v126, v107, 3, 1\n"
"v_and_b32 v126, 0x80, v126\n"
"v_xor_b32 v96, v96, v126\n"
"v_bfe_i32 v126, v107, 4, 1\n"
"v_and_b32 v126, 0x2000, v126\n"
"v_xor_b32 v96, v96, v126\n"
"v_bfe_i32 v126, v107, 5, 1\n"
"v_and_b32 v126, 0x4080, v126\n"
"v_xor_b32 v96, v96, v126\n"
"s_lshl_b32 s3, s5, 5\n"
"v_xor_b32 v96, s3, v96\n"
"s_lshl_b32 s3, s5, 17\n"
"v_add_u32 v96, s3, v96\n"
"v_mov_b32 v112, 0x0\n"
"v_bfe_i32 v126, v107, 0, 1\n"
"v_and_b32 v126, 0x10, v126\n"
"v_add_u32 v112, v112, v126\n"
"v_bfe_i32 v126, v107, 1, 1\n"
"v_and_b32 v126, 0x20, v126\n"
"v_add_u32 v112, v112, v126\n"
"v_bfe_i32 v126, v107, 2, 1\n"
"v_and_b32 v126, 0x40, v126\n"
"v_add_u32 v112, v112, v126\n"
"v_bfe_i32 v126, v107, 3, 1\n"
"v_and_b32 v126, 0x80, v126\n"
"v_add_u32 v112, v112, v126\n"
"v_bfe_i32 v126, v107, 4, 1\n"
"v_and_b32 v126, 0x100, v126\n"
"v_add_u32 v112, v112, v126\n"
"v_bfe_i32 v126, v107, 5, 1\n"
"v_and_b32 v126, 0x200, v126\n"
"v_add_u32 v112, v112, v126\n"
"s_mul_i32 s3, s5, 0x4000\n"
"v_add_u32 v112, s3, v112\n"
"v_add_u32 v113, 0x10000, v112\n"
"s_lshl_b32 s92, s5, 12\n"
"s_and_b32 s3, s5, 1\n"
"s_lshl_b32 s3, s3, 6\n"
"v_lshlrev_b32 v100, 4, v107\n"
"v_xor_b32 v100, s3, v100\n"
"s_add_u32 m0, s92, 0x0\n"
"s_mov_b32 s3, 0x0\n"
"buffer_load_dwordx4 v96, s[28:31], s3 offen lds\n"
"s_add_u32 m0, s92, 0x400\n"
"s_mov_b32 s3, 0x8000\n"
"buffer_load_dwordx4 v96, s[28:31], s3 offen lds\n"
"s_add_u32 m0, s92, 0x800\n"
"s_mov_b32 s3, 0x10000\n"
"buffer_load_dwordx4 v96, s[28:31], s3 offen lds\n"
"s_add_u32 m0, s92, 0xc00\n"
"s_mov_b32 s3, 0x18000\n"
"buffer_load_dwordx4 v96, s[28:31], s3 offen lds\n"
"s_add_u32 m0, s92, 0x4000\n"
"s_mov_b32 s3, 0x80000\n"
"buffer_load_dwordx4 v96, s[28:31], s3 offen lds\n"
"s_add_u32 m0, s92, 0x4400\n"
"s_mov_b32 s3, 0x88000\n"
"buffer_load_dwordx4 v96, s[28:31], s3 offen lds\n"
"s_add_u32 m0, s92, 0x4800\n"
"s_mov_b32 s3, 0x90000\n"
"buffer_load_dwordx4 v96, s[28:31], s3 offen lds\n"
"s_add_u32 m0, s92, 0x4c00\n"
"s_mov_b32 s3, 0x98000\n"
"buffer_load_dwordx4 v96, s[28:31], s3 offen lds\n"
"s_add_u32 m0, s92, 0x8000\n"
"s_mov_b32 s3, 0x100000\n"
"buffer_load_dwordx4 v96, s[28:31], s3 offen lds\n"
"s_add_u32 m0, s92, 0x8400\n"
"s_mov_b32 s3, 0x108000\n"
"buffer_load_dwordx4 v96, s[28:31], s3 offen lds\n"
"s_add_u32 m0, s92, 0x8800\n"
"s_mov_b32 s3, 0x110000\n"
"buffer_load_dwordx4 v96, s[28:31], s3 offen lds\n"
"s_add_u32 m0, s92, 0x8c00\n"
"s_mov_b32 s3, 0x118000\n"
"buffer_load_dwordx4 v96, s[28:31], s3 offen lds\n"
"s_add_u32 m0, s92, 0xc000\n"
"s_mov_b32 s3, 0x180000\n"
"buffer_load_dwordx4 v96, s[28:31], s3 offen lds\n"
"s_add_u32 m0, s92, 0xc400\n"
"s_mov_b32 s3, 0x188000\n"
"buffer_load_dwordx4 v96, s[28:31], s3 offen lds\n"
"s_add_u32 m0, s92, 0xc800\n"
"s_mov_b32 s3, 0x190000\n"
"buffer_load_dwordx4 v96, s[28:31], s3 offen lds\n"
"s_add_u32 m0, s92, 0xcc00\n"
"s_mov_b32 s3, 0x198000\n"
"buffer_load_dwordx4 v96, s[28:31], s3 offen lds\n"
"s_add_u32 m0, s92, 0x10000\n"
"s_mov_b32 s3, 0x200000\n"
"buffer_load_dwordx4 v96, s[28:31], s3 offen lds\n"
"s_add_u32 m0, s92, 0x10400\n"
"s_mov_b32 s3, 0x208000\n"
"buffer_load_dwordx4 v96, s[28:31], s3 offen lds\n"
"s_add_u32 m0, s92, 0x10800\n"
"s_mov_b32 s3, 0x210000\n"
"buffer_load_dwordx4 v96, s[28:31], s3 offen lds\n"
"s_add_u32 m0, s92, 0x10c00\n"
"s_mov_b32 s3, 0x218000\n"
"buffer_load_dwordx4 v96, s[28:31], s3 offen lds\n"
"s_add_u32 m0, s92, 0x14000\n"
"s_mov_b32 s3, 0x280000\n"
"buffer_load_dwordx4 v96, s[28:31], s3 offen lds\n"
"s_add_u32 m0, s92, 0x14400\n"
"s_mov_b32 s3, 0x288000\n"
"buffer_load_dwordx4 v96, s[28:31], s3 offen lds\n"
"s_add_u32 m0, s92, 0x14800\n"
"s_mov_b32 s3, 0x290000\n"
"buffer_load_dwordx4 v96, s[28:31], s3 offen lds\n"
"s_add_u32 m0, s92, 0x14c00\n"
"s_mov_b32 s3, 0x298000\n"
"buffer_load_dwordx4 v96, s[28:31], s3 offen lds\n"
"s_add_u32 m0, s92, 0x18000\n"
"s_mov_b32 s3, 0x300000\n"
"buffer_load_dwordx4 v96, s[28:31], s3 offen lds\n"
"s_add_u32 m0, s92, 0x18400\n"
"s_mov_b32 s3, 0x308000\n"
"buffer_load_dwordx4 v96, s[28:31], s3 offen lds\n"
"s_add_u32 m0, s92, 0x18800\n"
"s_mov_b32 s3, 0x310000\n"
"buffer_load_dwordx4 v96, s[28:31], s3 offen lds\n"
"s_add_u32 m0, s92, 0x18c00\n"
"s_mov_b32 s3, 0x318000\n"
"buffer_load_dwordx4 v96, s[28:31], s3 offen lds\n"
"v_mov_b32 v97, 0x0\n"
"v_bfe_i32 v126, v107, 0, 1\n"
"v_and_b32 v126, 0x8, v126\n"
"v_add_u32 v97, v97, v126\n"
"v_bfe_i32 v126, v107, 1, 1\n"
"v_and_b32 v126, 0x10, v126\n"
"v_add_u32 v97, v97, v126\n"
"v_bfe_i32 v126, v107, 2, 1\n"
"v_and_b32 v126, 0x1020, v126\n"
"v_add_u32 v97, v97, v126\n"
"v_bfe_i32 v126, v107, 3, 1\n"
"v_and_b32 v126, 0x2040, v126\n"
"v_add_u32 v97, v97, v126\n"
"v_bfe_i32 v126, v107, 4, 1\n"
"v_and_b32 v126, 0x280, v126\n"
"v_add_u32 v97, v97, v126\n"
"v_bfe_i32 v126, v107, 5, 1\n"
"v_and_b32 v126, 0x400, v126\n"
"v_add_u32 v97, v97, v126\n"
"s_lshr_b32 s93, s5, 1\n"
"s_lshl_b32 s93, s93, 7\n"
"s_and_b32 s3, s5, 1\n"
"s_lshl_b32 s3, s3, 5\n"
"s_or_b32 s93, s93, s3\n"
"s_lshl_b32 s94, s5, 6\n"
"v_mov_b32 v114, 0x0\n"
"v_bfe_i32 v126, v107, 0, 1\n"
"v_and_b32 v126, 0x10, v126\n"
"v_add_u32 v114, v114, v126\n"
"v_bfe_i32 v126, v107, 1, 1\n"
"v_and_b32 v126, 0x20, v126\n"
"v_add_u32 v114, v114, v126\n"
"v_bfe_i32 v126, v107, 2, 1\n"
"v_and_b32 v126, 0x100, v126\n"
"v_add_u32 v114, v114, v126\n"
"v_bfe_i32 v126, v107, 3, 1\n"
"v_and_b32 v126, 0x200, v126\n"
"v_add_u32 v114, v114, v126\n"
"v_bfe_i32 v126, v107, 4, 1\n"
"v_and_b32 v126, 0x40, v126\n"
"v_add_u32 v114, v114, v126\n"
"v_bfe_i32 v126, v107, 5, 1\n"
"v_and_b32 v126, 0x80, v126\n"
"v_add_u32 v114, v114, v126\n"
"v_mov_b32 v115, 0x0\n"
"v_bfe_i32 v126, v107, 0, 1\n"
"v_and_b32 v126, 0x10, v126\n"
"v_add_u32 v115, v115, v126\n"
"v_bfe_i32 v126, v107, 1, 1\n"
"v_and_b32 v126, 0x20, v126\n"
"v_add_u32 v115, v115, v126\n"
"v_bfe_i32 v126, v107, 2, 1\n"
"v_and_b32 v126, 0x4000, v126\n"
"v_add_u32 v115, v115, v126\n"
"v_bfe_i32 v126, v107, 3, 1\n"
"v_and_b32 v126, 0x8000, v126\n"
"v_add_u32 v115, v115, v126\n"
"v_bfe_i32 v126, v107, 4, 1\n"
"v_and_b32 v126, 0x10000, v126\n"
"v_add_u32 v115, v115, v126\n"
"v_bfe_i32 v126, v107, 5, 1\n"
"v_and_b32 v126, 0x20000, v126\n"
"v_add_u32 v115, v115, v126\n"
"v_mov_b32 v116, 0x0\n"
"v_bfe_i32 v126, v107, 0, 1\n"
"v_and_b32 v126, 0x4, v126\n"
"v_add_u32 v116, v116, v126\n"
"v_bfe_i32 v126, v107, 1, 1\n"
"v_and_b32 v126, 0x8, v126\n"
"v_add_u32 v116, v116, v126\n"
"v_bfe_i32 v126, v107, 4, 1\n"
"v_and_b32 v126, 0x20, v126\n"
"v_add_u32 v116, v116, v126\n"
"v_bfe_i32 v126, v107, 5, 1\n"
"v_and_b32 v126, 0x10, v126\n"
"v_add_u32 v116, v116, v126\n"
"v_mov_b32 v117, 0x26000\n"
"v_bfe_i32 v126, v107, 0, 1\n"
"v_and_b32 v126, 0x4, v126\n"
"v_add_u32 v117, v117, v126\n"
"v_bfe_i32 v126, v107, 1, 1\n"
"v_and_b32 v126, 0x8, v126\n"
"v_add_u32 v117, v117, v126\n"
"v_bfe_i32 v126, v107, 2, 1\n"
"v_and_b32 v126, 0x10, v126\n"
"v_add_u32 v117, v117, v126\n"
"v_bfe_i32 v126, v107, 3, 1\n"
"v_and_b32 v126, 0x20, v126\n"
"v_add_u32 v117, v117, v126\n"
"v_bfe_i32 v126, v107, 4, 1\n"
"v_and_b32 v126, 0x480, v126\n"
"v_add_u32 v117, v117, v126\n"
"v_bfe_i32 v126, v107, 5, 1\n"
"v_and_b32 v126, 0x900, v126\n"
"v_add_u32 v117, v117, v126\n"
"s_mul_i32 s3, s5, 0x40\n"
"v_add_u32 v117, s3, v117\n"
"v_mov_b32 v118, 0x26000\n"
"v_bfe_i32 v126, v107, 0, 1\n"
"v_and_b32 v126, 0x240, v126\n"
"v_add_u32 v118, v118, v126\n"
"v_bfe_i32 v126, v107, 2, 1\n"
"v_and_b32 v126, 0x480, v126\n"
"v_add_u32 v118, v118, v126\n"
"v_bfe_i32 v126, v107, 3, 1\n"
"v_and_b32 v126, 0x900, v126\n"
"v_add_u32 v118, v118, v126\n"
"v_bfe_i32 v126, v107, 4, 1\n"
"v_and_b32 v126, 0x10, v126\n"
"v_add_u32 v118, v118, v126\n"
"v_bfe_i32 v126, v107, 5, 1\n"
"v_and_b32 v126, 0x20, v126\n"
"v_add_u32 v118, v118, v126\n"
"v_mov_b32 v119, 0x27200\n"
"v_bfe_i32 v126, v107, 0, 1\n"
"v_and_b32 v126, 0x140, v126\n"
"v_add_u32 v119, v119, v126\n"
"v_bfe_i32 v126, v107, 2, 1\n"
"v_and_b32 v126, 0x280, v126\n"
"v_add_u32 v119, v119, v126\n"
"v_bfe_i32 v126, v107, 3, 1\n"
"v_and_b32 v126, 0x500, v126\n"
"v_add_u32 v119, v119, v126\n"
"v_bfe_i32 v126, v107, 4, 1\n"
"v_and_b32 v126, 0x10, v126\n"
"v_add_u32 v119, v119, v126\n"
"v_bfe_i32 v126, v107, 5, 1\n"
"v_and_b32 v126, 0x20, v126\n"
"v_add_u32 v119, v119, v126\n"
"s_mul_i32 s3, s5, 0x40\n"
"v_add_u32 v119, s3, v119\n"
"v_mov_b32 v120, 0x27200\n"
"v_bfe_i32 v126, v107, 0, 1\n"
"v_and_b32 v126, 0x140, v126\n"
"v_add_u32 v120, v120, v126\n"
"v_bfe_i32 v126, v107, 1, 1\n"
"v_and_b32 v126, 0x10, v126\n"
"v_add_u32 v120, v120, v126\n"
"v_bfe_i32 v126, v107, 2, 1\n"
"v_and_b32 v126, 0x280, v126\n"
"v_add_u32 v120, v120, v126\n"
"v_bfe_i32 v126, v107, 3, 1\n"
"v_and_b32 v126, 0x500, v126\n"
"v_add_u32 v120, v120, v126\n"
"v_bfe_i32 v126, v107, 4, 1\n"
"v_and_b32 v126, 0x20, v126\n"
"v_add_u32 v120, v120, v126\n"
"v_bfe_i32 v126, v107, 5, 1\n"
"v_and_b32 v126, 0x40, v126\n"
"v_add_u32 v120, v120, v126\n"
"v_mov_b32 v121, 0x20000\n"
"v_bfe_i32 v126, v107, 0, 1\n"
"v_and_b32 v126, 0x10, v126\n"
"v_add_u32 v121, v121, v126\n"
"v_bfe_i32 v126, v107, 1, 1\n"
"v_and_b32 v126, 0x20, v126\n"
"v_add_u32 v121, v121, v126\n"
"v_bfe_i32 v126, v107, 2, 1\n"
"v_and_b32 v126, 0x40, v126\n"
"v_add_u32 v121, v121, v126\n"
"v_bfe_i32 v126, v107, 3, 1\n"
"v_and_b32 v126, 0x100, v126\n"
"v_add_u32 v121, v121, v126\n"
"v_bfe_i32 v126, v107, 4, 1\n"
"v_and_b32 v126, 0x200, v126\n"
"v_add_u32 v121, v121, v126\n"
"v_bfe_i32 v126, v107, 5, 1\n"
"v_and_b32 v126, 0x400, v126\n"
"v_add_u32 v121, v121, v126\n"
"v_mov_b32 v122, 0x22000\n"
"v_bfe_i32 v126, v107, 0, 1\n"
"v_and_b32 v126, 0x10, v126\n"
"v_add_u32 v122, v122, v126\n"
"v_bfe_i32 v126, v107, 1, 1\n"
"v_and_b32 v126, 0x20, v126\n"
"v_add_u32 v122, v122, v126\n"
"v_bfe_i32 v126, v107, 2, 1\n"
"v_and_b32 v126, 0x40, v126\n"
"v_add_u32 v122, v122, v126\n"
"v_bfe_i32 v126, v107, 3, 1\n"
"v_and_b32 v126, 0x80, v126\n"
"v_add_u32 v122, v122, v126\n"
"v_bfe_i32 v126, v107, 4, 1\n"
"v_and_b32 v126, 0x200, v126\n"
"v_add_u32 v122, v122, v126\n"
"v_bfe_i32 v126, v107, 5, 1\n"
"v_and_b32 v126, 0x400, v126\n"
"v_add_u32 v122, v122, v126\n"
"s_mul_i32 s3, s5, 0x800\n"
"v_add_u32 v122, s3, v122\n"
"v_mov_b32 v123, 0x0\n"
"v_bfe_i32 v126, v107, 0, 1\n"
"v_and_b32 v126, 0x8, v126\n"
"v_add_u32 v123, v123, v126\n"
"v_bfe_i32 v126, v107, 1, 1\n"
"v_and_b32 v126, 0x10, v126\n"
"v_add_u32 v123, v123, v126\n"
"v_bfe_i32 v126, v107, 2, 1\n"
"v_and_b32 v126, 0x80, v126\n"
"v_add_u32 v123, v123, v126\n"
"v_bfe_i32 v126, v107, 3, 1\n"
"v_and_b32 v126, 0x100, v126\n"
"v_add_u32 v123, v123, v126\n"
"v_bfe_i32 v126, v107, 4, 1\n"
"v_and_b32 v126, 0x20, v126\n"
"v_add_u32 v123, v123, v126\n"
"v_bfe_i32 v126, v107, 5, 1\n"
"v_and_b32 v126, 0x40, v126\n"
"v_add_u32 v123, v123, v126\n"
"s_mul_i32 s3, s5, 0x200\n"
"v_add_u32 v123, s3, v123\n"
"v_mov_b32 v124, 0x800\n"
"v_bfe_i32 v126, v107, 0, 1\n"
"v_and_b32 v126, 0x20, v126\n"
"v_add_u32 v124, v124, v126\n"
"v_bfe_i32 v126, v107, 1, 1\n"
"v_and_b32 v126, 0x40, v126\n"
"v_add_u32 v124, v124, v126\n"
"v_bfe_i32 v126, v107, 2, 1\n"
"v_and_b32 v126, 0x80, v126\n"
"v_add_u32 v124, v124, v126\n"
"v_bfe_i32 v126, v107, 3, 1\n"
"v_and_b32 v126, 0x100, v126\n"
"v_add_u32 v124, v124, v126\n"
"v_bfe_i32 v126, v107, 4, 1\n"
"v_and_b32 v126, 0x8, v126\n"
"v_add_u32 v124, v124, v126\n"
"v_bfe_i32 v126, v107, 5, 1\n"
"v_and_b32 v126, 0x10, v126\n"
"v_add_u32 v124, v124, v126\n"
"s_mul_i32 s3, s5, 0x200\n"
"v_add_u32 v124, s3, v124\n"
"v_mov_b32 v125, 0x4444\n"
"v_bfe_i32 v126, v107, 1, 1\n"
"v_and_b32 v126, 0xaaaa, v126\n"
"v_add_u32 v125, v125, v126\n"
"v_mov_b32 v102, 0x3f80\n"
"v_bfe_i32 v126, v107, 3, 1\n"
"v_and_b32 v126, 0x3f7fc080, v126\n"
"v_add_u32 v102, v102, v126\n"
"v_mov_b32 v103, 0x0\n"
"v_bfe_i32 v126, v107, 0, 1\n"
"v_and_b32 v126, 0x1, v126\n"
"v_add_u32 v103, v103, v126\n"
"v_bfe_i32 v126, v107, 1, 1\n"
"v_and_b32 v126, 0x4, v126\n"
"v_add_u32 v103, v103, v126\n"
"v_bfe_i32 v126, v107, 2, 1\n"
"v_and_b32 v126, 0x2, v126\n"
"v_add_u32 v103, v103, v126\n"
"v_cmp_eq_u32 vcc, 0, v103\n"
"s_nop 1\n"
"v_cndmask_b32 v108, 0, v102, vcc\n"
"v_cmp_eq_u32 vcc, 1, v103\n"
"s_nop 1\n"
"v_cndmask_b32 v109, 0, v102, vcc\n"
"v_cmp_eq_u32 vcc, 2, v103\n"
"s_nop 1\n"
"v_cndmask_b32 v110, 0, v102, vcc\n"
"v_cmp_eq_u32 vcc, 3, v103\n"
"s_nop 1\n"
"v_cndmask_b32 v111, 0, v102, vcc\n"
"s_add_u32 s24, s74, 0x180000\n"
"s_addc_u32 s25, s75, 0\n"
"s_and_b32 s25, s25, 0xffff\n"
"s_mov_b32 s26, 0x1c000\n"
"s_mov_b32 s27, 0x20000\n"
"s_lshl_b32 s49, s4, 19\n"
"s_lshl_b32 s50, s5, 14\n"
"s_sub_u32 s49, s49, s50\n"
"s_add_u32 s49, s49, 0x10000\n"
"s_add_u32 s12, s72, 0xff0000\n"
"s_addc_u32 s13, s73, 0\n"
"s_add_u32 s12, s12, s49\n"
"s_addc_u32 s13, s13, 0\n"
"s_and_b32 s13, s13, 0xffff\n"
"s_mov_b32 s14, 0x80000\n"
"s_mov_b32 s15, 0x20000\n"
"s_add_u32 s16, s72, 0x1ff0000\n"
"s_addc_u32 s17, s73, 0\n"
"s_add_u32 s16, s16, s49\n"
"s_addc_u32 s17, s17, 0\n"
"s_and_b32 s17, s17, 0xffff\n"
"s_mov_b32 s18, 0x80000\n"
"s_mov_b32 s19, 0x20000\n"
"s_add_u32 s14, s14, s50\n"
"s_add_u32 s18, s18, s50\n"
"s_lshl_b32 s49, s4, 12\n"
"s_add_u32 s20, s72, 0x8000000\n"
"s_addc_u32 s21, s73, 0\n"
"s_add_u32 s20, s20, s49\n"
"s_addc_u32 s21, s21, 0\n"
"s_and_b32 s21, s21, 0xffff\n"
"s_mov_b32 s22, 0x800000\n"
"s_mov_b32 s23, 0x20000\n"
"s_sub_u32 s22, s22, s49\n"
"s_mov_b32 s32, 0\n"
"s_mov_b32 s33, 0\n"
"s_sub_u32 s34, 0x80000, s50\n"
"s_add_u32 s35, s34, 0x1000\n"
"s_lshl_b32 s36, s4, 8\n"
"s_lshl_b32 s3, s5, 6\n"
"s_add_u32 s36, s36, s3\n"
"s_add_u32 s37, s36, 0x40000\n"
"s_add_u32 s38, s36, 0x80000\n"
"s_lshl_b32 s39, s4, 14\n"
"s_lshl_b32 s3, s5, 11\n"
"s_add_u32 s39, s39, s3\n"
"s_add_u32 s40, s39, 0x400\n"
"s_lshl_b32 s41, s5, 11\n"
"s_add_u32 s42, s41, 0x20000\n"
"s_add_u32 s95, s41, 0x22000\n"
"s_mov_b32 s6, 0x3f803f80\n"
"s_mov_b32 s7, 0x3f803f80\n"
"s_mov_b32 s44, 0\n"
"s_mov_b32 s45, 8\n"
"s_mov_b32 s46, 17\n"
"s_mov_b32 s47, 0x40000\n"
"s_mov_b32 s48, -1\n"
"s_lshl_b32 s57, s4, 7\n"
"s_lshl_b32 s3, s5, 2\n"
"s_add_u32 s57, s57, s3\n"
"s_add_u32 s57, s57, 0x1000\n"
"s_lshr_b32 s51, s4, 1\n"
"s_lshl_b32 s51, s51, 7\n"
"s_mov_b32 s52, 0x33333333\n"
"s_mov_b32 s53, 0x33333333\n"
"s_mov_b32 s54, 0x000f000f\n"
"s_mov_b32 s55, 0x000f000f\n"
"s_mov_b32 s56, 0x3e0293ee\n"
"s_mov_b32 s58, 0\n"
"s_mov_b32 s59, 0\n"
"s_waitcnt vmcnt(24) lgkmcnt(0)\n"
"s_barrier\n"
"s_add_u32 m0, s92, 0x1c000\n"
"s_mov_b32 s3, 0x380000\n"
"buffer_load_dwordx4 v96, s[28:31], s3 offen lds\n"
"s_add_u32 m0, s92, 0x1c400\n"
"s_mov_b32 s3, 0x388000\n"
"buffer_load_dwordx4 v96, s[28:31], s3 offen lds\n"
"s_add_u32 m0, s92, 0x1c800\n"
"s_mov_b32 s3, 0x390000\n"
"buffer_load_dwordx4 v96, s[28:31], s3 offen lds\n"
"s_add_u32 m0, s92, 0x1cc00\n"
"s_mov_b32 s3, 0x398000\n"
"buffer_load_dwordx4 v96, s[28:31], s3 offen lds\n"
"s_xor_b32 s3, s93, 64\n"
"v_xor_b32 v98, s93, v97\n"
"v_xor_b32 v99, s3, v97\n"
"ds_read_b64_tr_b16 a[0:1], v98 offset:0\n"
"ds_read_b64_tr_b16 a[2:3], v98 offset:256\n"
"ds_read_b64_tr_b16 a[4:5], v98 offset:2048\n"
"ds_read_b64_tr_b16 a[6:7], v98 offset:2304\n"
"ds_read_b64_tr_b16 a[64:65], v99 offset:0\n"
"ds_read_b64_tr_b16 a[66:67], v99 offset:256\n"
"ds_read_b64_tr_b16 a[68:69], v99 offset:2048\n"
"ds_read_b64_tr_b16 a[70:71], v99 offset:2304\n"
"s_waitcnt vmcnt(24) lgkmcnt(0)\n"
"s_barrier\n"
"s_add_u32 m0, s92, 0x0\n"
"s_mov_b32 s3, 0x0\n"
"buffer_load_dwordx4 v96, s[80:83], s3 offen lds\n"
"s_add_u32 m0, s92, 0x400\n"
"s_mov_b32 s3, 0x8000\n"
"buffer_load_dwordx4 v96, s[80:83], s3 offen lds\n"
"s_add_u32 m0, s92, 0x800\n"
"s_mov_b32 s3, 0x10000\n"
"buffer_load_dwordx4 v96, s[80:83], s3 offen lds\n"
"s_add_u32 m0, s92, 0xc00\n"
"s_mov_b32 s3, 0x18000\n"
"buffer_load_dwordx4 v96, s[80:83], s3 offen lds\n"
"s_xor_b32 s3, s93, 64\n"
"v_xor_b32 v98, s93, v97\n"
"v_xor_b32 v99, s3, v97\n"
"ds_read_b64_tr_b16 a[8:9], v98 offset:16384\n"
"ds_read_b64_tr_b16 a[10:11], v98 offset:16640\n"
"ds_read_b64_tr_b16 a[12:13], v98 offset:18432\n"
"ds_read_b64_tr_b16 a[14:15], v98 offset:18688\n"
"ds_read_b64_tr_b16 a[72:73], v99 offset:16384\n"
"ds_read_b64_tr_b16 a[74:75], v99 offset:16640\n"
"ds_read_b64_tr_b16 a[76:77], v99 offset:18432\n"
"ds_read_b64_tr_b16 a[78:79], v99 offset:18688\n"
"s_waitcnt vmcnt(24) lgkmcnt(0)\n"
"s_barrier\n"
"s_add_u32 m0, s92, 0x4000\n"
"s_mov_b32 s3, 0x80000\n"
"buffer_load_dwordx4 v96, s[80:83], s3 offen lds\n"
"s_add_u32 m0, s92, 0x4400\n"
"s_mov_b32 s3, 0x88000\n"
"buffer_load_dwordx4 v96, s[80:83], s3 offen lds\n"
"s_add_u32 m0, s92, 0x4800\n"
"s_mov_b32 s3, 0x90000\n"
"buffer_load_dwordx4 v96, s[80:83], s3 offen lds\n"
"s_add_u32 m0, s92, 0x4c00\n"
"s_mov_b32 s3, 0x98000\n"
"buffer_load_dwordx4 v96, s[80:83], s3 offen lds\n"
"s_xor_b32 s3, s93, 64\n"
"v_xor_b32 v98, s93, v97\n"
"v_xor_b32 v99, s3, v97\n"
"ds_read_b64_tr_b16 a[16:17], v98 offset:32768\n"
"ds_read_b64_tr_b16 a[18:19], v98 offset:33024\n"
"ds_read_b64_tr_b16 a[20:21], v98 offset:34816\n"
"ds_read_b64_tr_b16 a[22:23], v98 offset:35072\n"
"ds_read_b64_tr_b16 a[80:81], v99 offset:32768\n"
"ds_read_b64_tr_b16 a[82:83], v99 offset:33024\n"
"ds_read_b64_tr_b16 a[84:85], v99 offset:34816\n"
"ds_read_b64_tr_b16 a[86:87], v99 offset:35072\n"
"s_waitcnt vmcnt(24) lgkmcnt(0)\n"
"s_barrier\n"
"s_add_u32 m0, s92, 0x8000\n"
"s_mov_b32 s3, 0x100000\n"
"buffer_load_dwordx4 v96, s[80:83], s3 offen lds\n"
"s_add_u32 m0, s92, 0x8400\n"
"s_mov_b32 s3, 0x108000\n"
"buffer_load_dwordx4 v96, s[80:83], s3 offen lds\n"
"s_add_u32 m0, s92, 0x8800\n"
"s_mov_b32 s3, 0x110000\n"
"buffer_load_dwordx4 v96, s[80:83], s3 offen lds\n"
"s_add_u32 m0, s92, 0x8c00\n"
"s_mov_b32 s3, 0x118000\n"
"buffer_load_dwordx4 v96, s[80:83], s3 offen lds\n"
"s_xor_b32 s3, s93, 64\n"
"v_xor_b32 v98, s93, v97\n"
"v_xor_b32 v99, s3, v97\n"
"ds_read_b64_tr_b16 a[24:25], v98 offset:49152\n"
"ds_read_b64_tr_b16 a[26:27], v98 offset:49408\n"
"ds_read_b64_tr_b16 a[28:29], v98 offset:51200\n"
"ds_read_b64_tr_b16 a[30:31], v98 offset:51456\n"
"ds_read_b64_tr_b16 a[88:89], v99 offset:49152\n"
"ds_read_b64_tr_b16 a[90:91], v99 offset:49408\n"
"ds_read_b64_tr_b16 a[92:93], v99 offset:51200\n"
"ds_read_b64_tr_b16 a[94:95], v99 offset:51456\n"
"s_waitcnt vmcnt(24) lgkmcnt(0)\n"
"s_barrier\n"
"s_add_u32 m0, s92, 0xc000\n"
"s_mov_b32 s3, 0x180000\n"
"buffer_load_dwordx4 v96, s[80:83], s3 offen lds\n"
"s_add_u32 m0, s92, 0xc400\n"
"s_mov_b32 s3, 0x188000\n"
"buffer_load_dwordx4 v96, s[80:83], s3 offen lds\n"
"s_add_u32 m0, s92, 0xc800\n"
"s_mov_b32 s3, 0x190000\n"
"buffer_load_dwordx4 v96, s[80:83], s3 offen lds\n"
"s_add_u32 m0, s92, 0xcc00\n"
"s_mov_b32 s3, 0x198000\n"
"buffer_load_dwordx4 v96, s[80:83], s3 offen lds\n"
"s_xor_b32 s3, s93, 64\n"
"v_xor_b32 v98, s93, v97\n"
"v_xor_b32 v99, s3, v97\n"
"v_add_u32 v98, 0x10000, v98\n"
"v_add_u32 v99, 0x10000, v99\n"
"ds_read_b64_tr_b16 a[32:33], v98 offset:0\n"
"ds_read_b64_tr_b16 a[34:35], v98 offset:256\n"
"ds_read_b64_tr_b16 a[36:37], v98 offset:2048\n"
"ds_read_b64_tr_b16 a[38:39], v98 offset:2304\n"
"ds_read_b64_tr_b16 a[96:97], v99 offset:0\n"
"ds_read_b64_tr_b16 a[98:99], v99 offset:256\n"
"ds_read_b64_tr_b16 a[100:101], v99 offset:2048\n"
"ds_read_b64_tr_b16 a[102:103], v99 offset:2304\n"
"s_waitcnt vmcnt(24) lgkmcnt(0)\n"
"s_barrier\n"
"s_add_u32 m0, s92, 0x10000\n"
"s_mov_b32 s3, 0x200000\n"
"buffer_load_dwordx4 v96, s[80:83], s3 offen lds\n"
"s_add_u32 m0, s92, 0x10400\n"
"s_mov_b32 s3, 0x208000\n"
"buffer_load_dwordx4 v96, s[80:83], s3 offen lds\n"
"s_add_u32 m0, s92, 0x10800\n"
"s_mov_b32 s3, 0x210000\n"
"buffer_load_dwordx4 v96, s[80:83], s3 offen lds\n"
"s_add_u32 m0, s92, 0x10c00\n"
"s_mov_b32 s3, 0x218000\n"
"buffer_load_dwordx4 v96, s[80:83], s3 offen lds\n"
"s_xor_b32 s3, s93, 64\n"
"v_xor_b32 v98, s93, v97\n"
"v_xor_b32 v99, s3, v97\n"
"v_add_u32 v98, 0x10000, v98\n"
"v_add_u32 v99, 0x10000, v99\n"
"ds_read_b64_tr_b16 a[40:41], v98 offset:16384\n"
"ds_read_b64_tr_b16 a[42:43], v98 offset:16640\n"
"ds_read_b64_tr_b16 a[44:45], v98 offset:18432\n"
"ds_read_b64_tr_b16 a[46:47], v98 offset:18688\n"
"ds_read_b64_tr_b16 a[104:105], v99 offset:16384\n"
"ds_read_b64_tr_b16 a[106:107], v99 offset:16640\n"
"ds_read_b64_tr_b16 a[108:109], v99 offset:18432\n"
"ds_read_b64_tr_b16 a[110:111], v99 offset:18688\n"
"s_waitcnt vmcnt(24) lgkmcnt(0)\n"
"s_barrier\n"
"s_add_u32 m0, s92, 0x14000\n"
"s_mov_b32 s3, 0x280000\n"
"buffer_load_dwordx4 v96, s[80:83], s3 offen lds\n"
"s_add_u32 m0, s92, 0x14400\n"
"s_mov_b32 s3, 0x288000\n"
"buffer_load_dwordx4 v96, s[80:83], s3 offen lds\n"
"s_add_u32 m0, s92, 0x14800\n"
"s_mov_b32 s3, 0x290000\n"
"buffer_load_dwordx4 v96, s[80:83], s3 offen lds\n"
"s_add_u32 m0, s92, 0x14c00\n"
"s_mov_b32 s3, 0x298000\n"
"buffer_load_dwordx4 v96, s[80:83], s3 offen lds\n"
"s_xor_b32 s3, s93, 64\n"
"v_xor_b32 v98, s93, v97\n"
"v_xor_b32 v99, s3, v97\n"
"v_add_u32 v98, 0x10000, v98\n"
"v_add_u32 v99, 0x10000, v99\n"
"ds_read_b64_tr_b16 a[48:49], v98 offset:32768\n"
"ds_read_b64_tr_b16 a[50:51], v98 offset:33024\n"
"ds_read_b64_tr_b16 a[52:53], v98 offset:34816\n"
"ds_read_b64_tr_b16 a[54:55], v98 offset:35072\n"
"ds_read_b64_tr_b16 a[112:113], v99 offset:32768\n"
"ds_read_b64_tr_b16 a[114:115], v99 offset:33024\n"
"ds_read_b64_tr_b16 a[116:117], v99 offset:34816\n"
"ds_read_b64_tr_b16 a[118:119], v99 offset:35072\n"
"s_waitcnt vmcnt(24) lgkmcnt(0)\n"
"s_barrier\n"
"s_add_u32 m0, s92, 0x18000\n"
"s_mov_b32 s3, 0x300000\n"
"buffer_load_dwordx4 v96, s[80:83], s3 offen lds\n"
"s_add_u32 m0, s92, 0x18400\n"
"s_mov_b32 s3, 0x308000\n"
"buffer_load_dwordx4 v96, s[80:83], s3 offen lds\n"
"s_add_u32 m0, s92, 0x18800\n"
"s_mov_b32 s3, 0x310000\n"
"buffer_load_dwordx4 v96, s[80:83], s3 offen lds\n"
"s_add_u32 m0, s92, 0x18c00\n"
"s_mov_b32 s3, 0x318000\n"
"buffer_load_dwordx4 v96, s[80:83], s3 offen lds\n"
"s_xor_b32 s3, s93, 64\n"
"v_xor_b32 v98, s93, v97\n"
"v_xor_b32 v99, s3, v97\n"
"v_add_u32 v98, 0x10000, v98\n"
"v_add_u32 v99, 0x10000, v99\n"
"ds_read_b64_tr_b16 a[56:57], v98 offset:49152\n"
"ds_read_b64_tr_b16 a[58:59], v98 offset:49408\n"
"ds_read_b64_tr_b16 a[60:61], v98 offset:51200\n"
"ds_read_b64_tr_b16 a[62:63], v98 offset:51456\n"
"ds_read_b64_tr_b16 a[120:121], v99 offset:49152\n"
"ds_read_b64_tr_b16 a[122:123], v99 offset:49408\n"
"ds_read_b64_tr_b16 a[124:125], v99 offset:51200\n"
"ds_read_b64_tr_b16 a[126:127], v99 offset:51456\n"
"s_waitcnt vmcnt(24) lgkmcnt(0)\n"
"s_barrier\n"
"s_add_u32 m0, s92, 0x1c000\n"
"s_mov_b32 s3, 0x380000\n"
"buffer_load_dwordx4 v96, s[80:83], s3 offen lds\n"
"s_add_u32 m0, s92, 0x1c400\n"
"s_mov_b32 s3, 0x388000\n"
"buffer_load_dwordx4 v96, s[80:83], s3 offen lds\n"
"s_add_u32 m0, s92, 0x1c800\n"
"s_mov_b32 s3, 0x390000\n"
"buffer_load_dwordx4 v96, s[80:83], s3 offen lds\n"
"s_add_u32 m0, s92, 0x1cc00\n"
"s_mov_b32 s3, 0x398000\n"
"buffer_load_dwordx4 v96, s[80:83], s3 offen lds\n"
"s_xor_b32 s3, s93, 64\n"
"v_xor_b32 v98, s93, v97\n"
"v_xor_b32 v99, s3, v97\n"
"ds_read_b64_tr_b16 a[128:129], v98 offset:0\n"
"ds_read_b64_tr_b16 a[130:131], v98 offset:256\n"
"ds_read_b64_tr_b16 a[132:133], v98 offset:2048\n"
"ds_read_b64_tr_b16 a[134:135], v98 offset:2304\n"
"ds_read_b64_tr_b16 a[192:193], v99 offset:0\n"
"ds_read_b64_tr_b16 a[194:195], v99 offset:256\n"
"ds_read_b64_tr_b16 a[196:197], v99 offset:2048\n"
"ds_read_b64_tr_b16 a[198:199], v99 offset:2304\n"
"s_waitcnt vmcnt(24) lgkmcnt(0)\n"
"s_barrier\n"
"s_add_u32 m0, s92, 0x0\n"
"s_mov_b32 s3, 0x0\n"
"buffer_load_dwordx4 v96, s[84:87], s3 offen lds\n"
"s_add_u32 m0, s92, 0x400\n"
"s_mov_b32 s3, 0x8000\n"
"buffer_load_dwordx4 v96, s[84:87], s3 offen lds\n"
"s_add_u32 m0, s92, 0x800\n"
"s_mov_b32 s3, 0x10000\n"
"buffer_load_dwordx4 v96, s[84:87], s3 offen lds\n"
"s_add_u32 m0, s92, 0xc00\n"
"s_mov_b32 s3, 0x18000\n"
"buffer_load_dwordx4 v96, s[84:87], s3 offen lds\n"
"s_xor_b32 s3, s93, 64\n"
"v_xor_b32 v98, s93, v97\n"
"v_xor_b32 v99, s3, v97\n"
"ds_read_b64_tr_b16 a[136:137], v98 offset:16384\n"
"ds_read_b64_tr_b16 a[138:139], v98 offset:16640\n"
"ds_read_b64_tr_b16 a[140:141], v98 offset:18432\n"
"ds_read_b64_tr_b16 a[142:143], v98 offset:18688\n"
"ds_read_b64_tr_b16 a[200:201], v99 offset:16384\n"
"ds_read_b64_tr_b16 a[202:203], v99 offset:16640\n"
"ds_read_b64_tr_b16 a[204:205], v99 offset:18432\n"
"ds_read_b64_tr_b16 a[206:207], v99 offset:18688\n"
"s_waitcnt vmcnt(24) lgkmcnt(0)\n"
"s_barrier\n"
"s_add_u32 m0, s92, 0x4000\n"
"s_mov_b32 s3, 0x80000\n"
"buffer_load_dwordx4 v96, s[84:87], s3 offen lds\n"
"s_add_u32 m0, s92, 0x4400\n"
"s_mov_b32 s3, 0x88000\n"
"buffer_load_dwordx4 v96, s[84:87], s3 offen lds\n"
"s_add_u32 m0, s92, 0x4800\n"
"s_mov_b32 s3, 0x90000\n"
"buffer_load_dwordx4 v96, s[84:87], s3 offen lds\n"
"s_add_u32 m0, s92, 0x4c00\n"
"s_mov_b32 s3, 0x98000\n"
"buffer_load_dwordx4 v96, s[84:87], s3 offen lds\n"
"s_xor_b32 s3, s93, 64\n"
"v_xor_b32 v98, s93, v97\n"
"v_xor_b32 v99, s3, v97\n"
"ds_read_b64_tr_b16 a[144:145], v98 offset:32768\n"
"ds_read_b64_tr_b16 a[146:147], v98 offset:33024\n"
"ds_read_b64_tr_b16 a[148:149], v98 offset:34816\n"
"ds_read_b64_tr_b16 a[150:151], v98 offset:35072\n"
"ds_read_b64_tr_b16 a[208:209], v99 offset:32768\n"
"ds_read_b64_tr_b16 a[210:211], v99 offset:33024\n"
"ds_read_b64_tr_b16 a[212:213], v99 offset:34816\n"
"ds_read_b64_tr_b16 a[214:215], v99 offset:35072\n"
"s_waitcnt vmcnt(24) lgkmcnt(0)\n"
"s_barrier\n"
"s_add_u32 m0, s92, 0x8000\n"
"s_mov_b32 s3, 0x100000\n"
"buffer_load_dwordx4 v96, s[84:87], s3 offen lds\n"
"s_add_u32 m0, s92, 0x8400\n"
"s_mov_b32 s3, 0x108000\n"
"buffer_load_dwordx4 v96, s[84:87], s3 offen lds\n"
"s_add_u32 m0, s92, 0x8800\n"
"s_mov_b32 s3, 0x110000\n"
"buffer_load_dwordx4 v96, s[84:87], s3 offen lds\n"
"s_add_u32 m0, s92, 0x8c00\n"
"s_mov_b32 s3, 0x118000\n"
"buffer_load_dwordx4 v96, s[84:87], s3 offen lds\n"
"s_xor_b32 s3, s93, 64\n"
"v_xor_b32 v98, s93, v97\n"
"v_xor_b32 v99, s3, v97\n"
"ds_read_b64_tr_b16 a[152:153], v98 offset:49152\n"
"ds_read_b64_tr_b16 a[154:155], v98 offset:49408\n"
"ds_read_b64_tr_b16 a[156:157], v98 offset:51200\n"
"ds_read_b64_tr_b16 a[158:159], v98 offset:51456\n"
"ds_read_b64_tr_b16 a[216:217], v99 offset:49152\n"
"ds_read_b64_tr_b16 a[218:219], v99 offset:49408\n"
"ds_read_b64_tr_b16 a[220:221], v99 offset:51200\n"
"ds_read_b64_tr_b16 a[222:223], v99 offset:51456\n"
"s_waitcnt vmcnt(24) lgkmcnt(0)\n"
"s_barrier\n"
"s_add_u32 m0, s92, 0xc000\n"
"s_mov_b32 s3, 0x180000\n"
"buffer_load_dwordx4 v96, s[84:87], s3 offen lds\n"
"s_add_u32 m0, s92, 0xc400\n"
"s_mov_b32 s3, 0x188000\n"
"buffer_load_dwordx4 v96, s[84:87], s3 offen lds\n"
"s_add_u32 m0, s92, 0xc800\n"
"s_mov_b32 s3, 0x190000\n"
"buffer_load_dwordx4 v96, s[84:87], s3 offen lds\n"
"s_add_u32 m0, s92, 0xcc00\n"
"s_mov_b32 s3, 0x198000\n"
"buffer_load_dwordx4 v96, s[84:87], s3 offen lds\n"
"s_xor_b32 s3, s93, 64\n"
"v_xor_b32 v98, s93, v97\n"
"v_xor_b32 v99, s3, v97\n"
"v_add_u32 v98, 0x10000, v98\n"
"v_add_u32 v99, 0x10000, v99\n"
"ds_read_b64_tr_b16 a[160:161], v98 offset:0\n"
"ds_read_b64_tr_b16 a[162:163], v98 offset:256\n"
"ds_read_b64_tr_b16 a[164:165], v98 offset:2048\n"
"ds_read_b64_tr_b16 a[166:167], v98 offset:2304\n"
"ds_read_b64_tr_b16 a[224:225], v99 offset:0\n"
"ds_read_b64_tr_b16 a[226:227], v99 offset:256\n"
"ds_read_b64_tr_b16 a[228:229], v99 offset:2048\n"
"ds_read_b64_tr_b16 a[230:231], v99 offset:2304\n"
"s_waitcnt vmcnt(24) lgkmcnt(0)\n"
"s_barrier\n"
"s_add_u32 m0, s92, 0x10000\n"
"s_mov_b32 s3, 0x200000\n"
"buffer_load_dwordx4 v96, s[84:87], s3 offen lds\n"
"s_add_u32 m0, s92, 0x10400\n"
"s_mov_b32 s3, 0x208000\n"
"buffer_load_dwordx4 v96, s[84:87], s3 offen lds\n"
"s_add_u32 m0, s92, 0x10800\n"
"s_mov_b32 s3, 0x210000\n"
"buffer_load_dwordx4 v96, s[84:87], s3 offen lds\n"
"s_add_u32 m0, s92, 0x10c00\n"
"s_mov_b32 s3, 0x218000\n"
"buffer_load_dwordx4 v96, s[84:87], s3 offen lds\n"
"s_xor_b32 s3, s93, 64\n"
"v_xor_b32 v98, s93, v97\n"
"v_xor_b32 v99, s3, v97\n"
"v_add_u32 v98, 0x10000, v98\n"
"v_add_u32 v99, 0x10000, v99\n"
"ds_read_b64_tr_b16 a[168:169], v98 offset:16384\n"
"ds_read_b64_tr_b16 a[170:171], v98 offset:16640\n"
"ds_read_b64_tr_b16 a[172:173], v98 offset:18432\n"
"ds_read_b64_tr_b16 a[174:175], v98 offset:18688\n"
"ds_read_b64_tr_b16 a[232:233], v99 offset:16384\n"
"ds_read_b64_tr_b16 a[234:235], v99 offset:16640\n"
"ds_read_b64_tr_b16 a[236:237], v99 offset:18432\n"
"ds_read_b64_tr_b16 a[238:239], v99 offset:18688\n"
"s_waitcnt vmcnt(24) lgkmcnt(0)\n"
"s_barrier\n"
"s_add_u32 m0, s92, 0x14000\n"
"s_mov_b32 s3, 0x280000\n"
"buffer_load_dwordx4 v96, s[84:87], s3 offen lds\n"
"s_add_u32 m0, s92, 0x14400\n"
"s_mov_b32 s3, 0x288000\n"
"buffer_load_dwordx4 v96, s[84:87], s3 offen lds\n"
"s_add_u32 m0, s92, 0x14800\n"
"s_mov_b32 s3, 0x290000\n"
"buffer_load_dwordx4 v96, s[84:87], s3 offen lds\n"
"s_add_u32 m0, s92, 0x14c00\n"
"s_mov_b32 s3, 0x298000\n"
"buffer_load_dwordx4 v96, s[84:87], s3 offen lds\n"
"s_xor_b32 s3, s93, 64\n"
"v_xor_b32 v98, s93, v97\n"
"v_xor_b32 v99, s3, v97\n"
"v_add_u32 v98, 0x10000, v98\n"
"v_add_u32 v99, 0x10000, v99\n"
"ds_read_b64_tr_b16 a[176:177], v98 offset:32768\n"
"ds_read_b64_tr_b16 a[178:179], v98 offset:33024\n"
"ds_read_b64_tr_b16 a[180:181], v98 offset:34816\n"
"ds_read_b64_tr_b16 a[182:183], v98 offset:35072\n"
"ds_read_b64_tr_b16 a[240:241], v99 offset:32768\n"
"ds_read_b64_tr_b16 a[242:243], v99 offset:33024\n"
"ds_read_b64_tr_b16 a[244:245], v99 offset:34816\n"
"ds_read_b64_tr_b16 a[246:247], v99 offset:35072\n"
"s_waitcnt vmcnt(24) lgkmcnt(0)\n"
"s_barrier\n"
"s_add_u32 m0, s92, 0x18000\n"
"s_mov_b32 s3, 0x300000\n"
"buffer_load_dwordx4 v96, s[84:87], s3 offen lds\n"
"s_add_u32 m0, s92, 0x18400\n"
"s_mov_b32 s3, 0x308000\n"
"buffer_load_dwordx4 v96, s[84:87], s3 offen lds\n"
"s_add_u32 m0, s92, 0x18800\n"
"s_mov_b32 s3, 0x310000\n"
"buffer_load_dwordx4 v96, s[84:87], s3 offen lds\n"
"s_add_u32 m0, s92, 0x18c00\n"
"s_mov_b32 s3, 0x318000\n"
"buffer_load_dwordx4 v96, s[84:87], s3 offen lds\n"
"s_xor_b32 s3, s93, 64\n"
"v_xor_b32 v98, s93, v97\n"
"v_xor_b32 v99, s3, v97\n"
"v_add_u32 v98, 0x10000, v98\n"
"v_add_u32 v99, 0x10000, v99\n"
"ds_read_b64_tr_b16 a[184:185], v98 offset:49152\n"
"ds_read_b64_tr_b16 a[186:187], v98 offset:49408\n"
"ds_read_b64_tr_b16 a[188:189], v98 offset:51200\n"
"ds_read_b64_tr_b16 a[190:191], v98 offset:51456\n"
"ds_read_b64_tr_b16 a[248:249], v99 offset:49152\n"
"ds_read_b64_tr_b16 a[250:251], v99 offset:49408\n"
"ds_read_b64_tr_b16 a[252:253], v99 offset:51200\n"
"ds_read_b64_tr_b16 a[254:255], v99 offset:51456\n"
"s_waitcnt vmcnt(24) lgkmcnt(0)\n"
"s_barrier\n"
"s_add_u32 m0, s92, 0x1c000\n"
"s_mov_b32 s3, 0x380000\n"
"buffer_load_dwordx4 v96, s[84:87], s3 offen lds\n"
"s_add_u32 m0, s92, 0x1c400\n"
"s_mov_b32 s3, 0x388000\n"
"buffer_load_dwordx4 v96, s[84:87], s3 offen lds\n"
"s_add_u32 m0, s92, 0x1c800\n"
"s_mov_b32 s3, 0x390000\n"
"buffer_load_dwordx4 v96, s[84:87], s3 offen lds\n"
"s_add_u32 m0, s92, 0x1cc00\n"
"s_mov_b32 s3, 0x398000\n"
"buffer_load_dwordx4 v96, s[84:87], s3 offen lds\n"
"s_xor_b32 s3, s94, 32\n"
"v_xor_b32 v98, s94, v97\n"
"v_xor_b32 v99, s3, v97\n"
"ds_read_b64_tr_b16 v[128:129], v98 offset:0\n"
"ds_read_b64_tr_b16 v[130:131], v98 offset:256\n"
"ds_read_b64_tr_b16 v[132:133], v98 offset:2048\n"
"ds_read_b64_tr_b16 v[134:135], v98 offset:2304\n"
"ds_read_b64_tr_b16 v[192:193], v99 offset:0\n"
"ds_read_b64_tr_b16 v[194:195], v99 offset:256\n"
"ds_read_b64_tr_b16 v[196:197], v99 offset:2048\n"
"ds_read_b64_tr_b16 v[198:199], v99 offset:2304\n"
"s_waitcnt vmcnt(24) lgkmcnt(0)\n"
"s_barrier\n"
"s_add_u32 s3, s92, 0x0\n"
"s_mov_b32 m0, s3\n"
"v_xor_b32 v101, 0x0, v100\n"
"buffer_load_dwordx4 v101, s[88:91], s3 offen lds\n"
"s_add_u32 s3, s92, 0x400\n"
"s_mov_b32 m0, s3\n"
"v_xor_b32 v101, 0x20, v100\n"
"buffer_load_dwordx4 v101, s[88:91], s3 offen lds\n"
"s_add_u32 s3, s92, 0x800\n"
"s_mov_b32 m0, s3\n"
"v_xor_b32 v101, 0x0, v100\n"
"buffer_load_dwordx4 v101, s[88:91], s3 offen lds\n"
"s_add_u32 s3, s92, 0xc00\n"
"s_mov_b32 m0, s3\n"
"v_xor_b32 v101, 0x20, v100\n"
"buffer_load_dwordx4 v101, s[88:91], s3 offen lds\n"
"s_xor_b32 s3, s94, 32\n"
"v_xor_b32 v98, s94, v97\n"
"v_xor_b32 v99, s3, v97\n"
"ds_read_b64_tr_b16 v[136:137], v98 offset:16384\n"
"ds_read_b64_tr_b16 v[138:139], v98 offset:16640\n"
"ds_read_b64_tr_b16 v[140:141], v98 offset:18432\n"
"ds_read_b64_tr_b16 v[142:143], v98 offset:18688\n"
"ds_read_b64_tr_b16 v[200:201], v99 offset:16384\n"
"ds_read_b64_tr_b16 v[202:203], v99 offset:16640\n"
"ds_read_b64_tr_b16 v[204:205], v99 offset:18432\n"
"ds_read_b64_tr_b16 v[206:207], v99 offset:18688\n"
"s_waitcnt vmcnt(24) lgkmcnt(0)\n"
"s_barrier\n"
"s_add_u32 s3, s92, 0x4000\n"
"s_mov_b32 m0, s3\n"
"v_xor_b32 v101, 0x80, v100\n"
"buffer_load_dwordx4 v101, s[88:91], s3 offen lds\n"
"s_add_u32 s3, s92, 0x4400\n"
"s_mov_b32 m0, s3\n"
"v_xor_b32 v101, 0xa0, v100\n"
"buffer_load_dwordx4 v101, s[88:91], s3 offen lds\n"
"s_add_u32 s3, s92, 0x4800\n"
"s_mov_b32 m0, s3\n"
"v_xor_b32 v101, 0x80, v100\n"
"buffer_load_dwordx4 v101, s[88:91], s3 offen lds\n"
"s_add_u32 s3, s92, 0x4c00\n"
"s_mov_b32 m0, s3\n"
"v_xor_b32 v101, 0xa0, v100\n"
"buffer_load_dwordx4 v101, s[88:91], s3 offen lds\n"
"s_xor_b32 s3, s94, 32\n"
"v_xor_b32 v98, s94, v97\n"
"v_xor_b32 v99, s3, v97\n"
"ds_read_b64_tr_b16 v[144:145], v98 offset:32768\n"
"ds_read_b64_tr_b16 v[146:147], v98 offset:33024\n"
"ds_read_b64_tr_b16 v[148:149], v98 offset:34816\n"
"ds_read_b64_tr_b16 v[150:151], v98 offset:35072\n"
"ds_read_b64_tr_b16 v[208:209], v99 offset:32768\n"
"ds_read_b64_tr_b16 v[210:211], v99 offset:33024\n"
"ds_read_b64_tr_b16 v[212:213], v99 offset:34816\n"
"ds_read_b64_tr_b16 v[214:215], v99 offset:35072\n"
"s_waitcnt vmcnt(24) lgkmcnt(0)\n"
"s_barrier\n"
"s_add_u32 s3, s92, 0x8000\n"
"s_mov_b32 m0, s3\n"
"v_xor_b32 v101, 0x0, v100\n"
"buffer_load_dwordx4 v101, s[88:91], s3 offen lds\n"
"s_add_u32 s3, s92, 0x8400\n"
"s_mov_b32 m0, s3\n"
"v_xor_b32 v101, 0x20, v100\n"
"buffer_load_dwordx4 v101, s[88:91], s3 offen lds\n"
"s_add_u32 s3, s92, 0x8800\n"
"s_mov_b32 m0, s3\n"
"v_xor_b32 v101, 0x0, v100\n"
"buffer_load_dwordx4 v101, s[88:91], s3 offen lds\n"
"s_add_u32 s3, s92, 0x8c00\n"
"s_mov_b32 m0, s3\n"
"v_xor_b32 v101, 0x20, v100\n"
"buffer_load_dwordx4 v101, s[88:91], s3 offen lds\n"
"s_xor_b32 s3, s94, 32\n"
"v_xor_b32 v98, s94, v97\n"
"v_xor_b32 v99, s3, v97\n"
"ds_read_b64_tr_b16 v[152:153], v98 offset:49152\n"
"ds_read_b64_tr_b16 v[154:155], v98 offset:49408\n"
"ds_read_b64_tr_b16 v[156:157], v98 offset:51200\n"
"ds_read_b64_tr_b16 v[158:159], v98 offset:51456\n"
"ds_read_b64_tr_b16 v[216:217], v99 offset:49152\n"
"ds_read_b64_tr_b16 v[218:219], v99 offset:49408\n"
"ds_read_b64_tr_b16 v[220:221], v99 offset:51200\n"
"ds_read_b64_tr_b16 v[222:223], v99 offset:51456\n"
"s_waitcnt vmcnt(24) lgkmcnt(0)\n"
"s_barrier\n"
"s_add_u32 s3, s92, 0xc000\n"
"s_mov_b32 m0, s3\n"
"v_xor_b32 v101, 0x80, v100\n"
"buffer_load_dwordx4 v101, s[88:91], s3 offen lds\n"
"s_add_u32 s3, s92, 0xc400\n"
"s_mov_b32 m0, s3\n"
"v_xor_b32 v101, 0xa0, v100\n"
"buffer_load_dwordx4 v101, s[88:91], s3 offen lds\n"
"s_add_u32 s3, s92, 0xc800\n"
"s_mov_b32 m0, s3\n"
"v_xor_b32 v101, 0x80, v100\n"
"buffer_load_dwordx4 v101, s[88:91], s3 offen lds\n"
"s_add_u32 s3, s92, 0xcc00\n"
"s_mov_b32 m0, s3\n"
"v_xor_b32 v101, 0xa0, v100\n"
"buffer_load_dwordx4 v101, s[88:91], s3 offen lds\n"
"s_xor_b32 s3, s94, 32\n"
"v_xor_b32 v98, s94, v97\n"
"v_xor_b32 v99, s3, v97\n"
"v_add_u32 v98, 0x10000, v98\n"
"v_add_u32 v99, 0x10000, v99\n"
"ds_read_b64_tr_b16 v[160:161], v98 offset:0\n"
"ds_read_b64_tr_b16 v[162:163], v98 offset:256\n"
"ds_read_b64_tr_b16 v[164:165], v98 offset:2048\n"
"ds_read_b64_tr_b16 v[166:167], v98 offset:2304\n"
"ds_read_b64_tr_b16 v[224:225], v99 offset:0\n"
"ds_read_b64_tr_b16 v[226:227], v99 offset:256\n"
"ds_read_b64_tr_b16 v[228:229], v99 offset:2048\n"
"ds_read_b64_tr_b16 v[230:231], v99 offset:2304\n"
"s_waitcnt vmcnt(24) lgkmcnt(0)\n"
"s_barrier\n"
"s_add_u32 s3, s92, 0x10000\n"
"s_mov_b32 m0, s3\n"
"v_xor_b32 v101, 0x0, v100\n"
"buffer_load_dwordx4 v101, s[88:91], s3 offen lds\n"
"s_add_u32 s3, s92, 0x10400\n"
"s_mov_b32 m0, s3\n"
"v_xor_b32 v101, 0x20, v100\n"
"buffer_load_dwordx4 v101, s[88:91], s3 offen lds\n"
"s_add_u32 s3, s92, 0x10800\n"
"s_mov_b32 m0, s3\n"
"v_xor_b32 v101, 0x0, v100\n"
"buffer_load_dwordx4 v101, s[88:91], s3 offen lds\n"
"s_add_u32 s3, s92, 0x10c00\n"
"s_mov_b32 m0, s3\n"
"v_xor_b32 v101, 0x20, v100\n"
"buffer_load_dwordx4 v101, s[88:91], s3 offen lds\n"
"s_xor_b32 s3, s94, 32\n"
"v_xor_b32 v98, s94, v97\n"
"v_xor_b32 v99, s3, v97\n"
"v_add_u32 v98, 0x10000, v98\n"
"v_add_u32 v99, 0x10000, v99\n"
"ds_read_b64_tr_b16 v[168:169], v98 offset:16384\n"
"ds_read_b64_tr_b16 v[170:171], v98 offset:16640\n"
"ds_read_b64_tr_b16 v[172:173], v98 offset:18432\n"
"ds_read_b64_tr_b16 v[174:175], v98 offset:18688\n"
"ds_read_b64_tr_b16 v[232:233], v99 offset:16384\n"
"ds_read_b64_tr_b16 v[234:235], v99 offset:16640\n"
"ds_read_b64_tr_b16 v[236:237], v99 offset:18432\n"
"ds_read_b64_tr_b16 v[238:239], v99 offset:18688\n"
"s_waitcnt vmcnt(24) lgkmcnt(0)\n"
"s_barrier\n"
"s_add_u32 s3, s92, 0x14000\n"
"s_mov_b32 m0, s3\n"
"v_xor_b32 v101, 0x80, v100\n"
"buffer_load_dwordx4 v101, s[88:91], s3 offen lds\n"
"s_add_u32 s3, s92, 0x14400\n"
"s_mov_b32 m0, s3\n"
"v_xor_b32 v101, 0xa0, v100\n"
"buffer_load_dwordx4 v101, s[88:91], s3 offen lds\n"
"s_add_u32 s3, s92, 0x14800\n"
"s_mov_b32 m0, s3\n"
"v_xor_b32 v101, 0x80, v100\n"
"buffer_load_dwordx4 v101, s[88:91], s3 offen lds\n"
"s_add_u32 s3, s92, 0x14c00\n"
"s_mov_b32 m0, s3\n"
"v_xor_b32 v101, 0xa0, v100\n"
"buffer_load_dwordx4 v101, s[88:91], s3 offen lds\n"
"s_xor_b32 s3, s94, 32\n"
"v_xor_b32 v98, s94, v97\n"
"v_xor_b32 v99, s3, v97\n"
"v_add_u32 v98, 0x10000, v98\n"
"v_add_u32 v99, 0x10000, v99\n"
"ds_read_b64_tr_b16 v[176:177], v98 offset:32768\n"
"ds_read_b64_tr_b16 v[178:179], v98 offset:33024\n"
"ds_read_b64_tr_b16 v[180:181], v98 offset:34816\n"
"ds_read_b64_tr_b16 v[182:183], v98 offset:35072\n"
"ds_read_b64_tr_b16 v[240:241], v99 offset:32768\n"
"ds_read_b64_tr_b16 v[242:243], v99 offset:33024\n"
"ds_read_b64_tr_b16 v[244:245], v99 offset:34816\n"
"ds_read_b64_tr_b16 v[246:247], v99 offset:35072\n"
"s_waitcnt vmcnt(24) lgkmcnt(0)\n"
"s_barrier\n"
"s_add_u32 s3, s92, 0x18000\n"
"s_mov_b32 m0, s3\n"
"v_xor_b32 v101, 0x0, v100\n"
"buffer_load_dwordx4 v101, s[88:91], s3 offen lds\n"
"s_add_u32 s3, s92, 0x18400\n"
"s_mov_b32 m0, s3\n"
"v_xor_b32 v101, 0x20, v100\n"
"buffer_load_dwordx4 v101, s[88:91], s3 offen lds\n"
"s_add_u32 s3, s92, 0x18800\n"
"s_mov_b32 m0, s3\n"
"v_xor_b32 v101, 0x0, v100\n"
"buffer_load_dwordx4 v101, s[88:91], s3 offen lds\n"
"s_add_u32 s3, s92, 0x18c00\n"
"s_mov_b32 m0, s3\n"
"v_xor_b32 v101, 0x20, v100\n"
"buffer_load_dwordx4 v101, s[88:91], s3 offen lds\n"
"s_xor_b32 s3, s94, 32\n"
"v_xor_b32 v98, s94, v97\n"
"v_xor_b32 v99, s3, v97\n"
"v_add_u32 v98, 0x10000, v98\n"
"v_add_u32 v99, 0x10000, v99\n"
"ds_read_b64_tr_b16 v[184:185], v98 offset:49152\n"
"ds_read_b64_tr_b16 v[186:187], v98 offset:49408\n"
"ds_read_b64_tr_b16 v[188:189], v98 offset:51200\n"
"ds_read_b64_tr_b16 v[190:191], v98 offset:51456\n"
"ds_read_b64_tr_b16 v[248:249], v99 offset:49152\n"
"ds_read_b64_tr_b16 v[250:251], v99 offset:49408\n"
"ds_read_b64_tr_b16 v[252:253], v99 offset:51200\n"
"ds_read_b64_tr_b16 v[254:255], v99 offset:51456\n"
"s_waitcnt lgkmcnt(0)\n"
"s_barrier\n"
"s_add_u32 s3, s92, 0x1c000\n"
"s_mov_b32 m0, s3\n"
"v_xor_b32 v101, 0x80, v100\n"
"buffer_load_dwordx4 v101, s[88:91], s3 offen lds\n"
"s_add_u32 s3, s92, 0x1c400\n"
"s_mov_b32 m0, s3\n"
"v_xor_b32 v101, 0xa0, v100\n"
"buffer_load_dwordx4 v101, s[88:91], s3 offen lds\n"
"s_add_u32 s3, s92, 0x1c800\n"
"s_mov_b32 m0, s3\n"
"v_xor_b32 v101, 0x80, v100\n"
"buffer_load_dwordx4 v101, s[88:91], s3 offen lds\n"
"s_add_u32 s3, s92, 0x1cc00\n"
"s_mov_b32 m0, s3\n"
"v_xor_b32 v101, 0xa0, v100\n"
"buffer_load_dwordx4 v101, s[88:91], s3 offen lds\n"
"s_mov_b32 s3, 0x2000\n"
"L_chain_sel:\n"
"buffer_load_dword v102, off, s[24:27], s3 sc1\n"
"s_waitcnt vmcnt(0)\n"
"v_readfirstlane_b32 s49, v102\n"
"s_sub_u32 s47, s47, 1\n"
"s_cbranch_scc1 L_chain_fail\n"
"s_cmp_lt_i32 s49, 0\n"
"s_cbranch_scc1 L_chain_sel\n"
"s_lshl_b32 s49, s49, 13\n"
"s_add_u32 s8, s74, 0x0\n"
"s_addc_u32 s9, s75, 0\n"
"s_add_u32 s8, s8, s49\n"
"s_addc_u32 s9, s9, 0\n"
"s_and_b32 s9, s9, 0xffff\n"
"s_mov_b32 s10, 0x17e000\n"
"s_mov_b32 s11, 0x20000\n"
"s_add_u32 s28, s62, 0x0\n"
"s_addc_u32 s29, s63, 0\n"
"s_and_b32 s29, s29, 0xffff\n"
"s_mov_b32 s30, 0x2000\n"
"s_mov_b32 s31, 0x20000\n"
"v_mov_b32 v102, 0x0\n"
"v_bfe_i32 v126, v107, 0, 1\n"
"v_and_b32 v126, 0x2, v126\n"
"v_add_u32 v102, v102, v126\n"
"v_bfe_i32 v126, v107, 1, 1\n"
"v_and_b32 v126, 0x4, v126\n"
"v_add_u32 v102, v102, v126\n"
"v_bfe_i32 v126, v107, 4, 1\n"
"v_and_b32 v126, 0x40, v126\n"
"v_add_u32 v102, v102, v126\n"
"v_bfe_i32 v126, v107, 5, 1\n"
"v_and_b32 v126, 0x400, v126\n"
"v_add_u32 v102, v102, v126\n"
"s_mul_i32 s3, s5, 0x800\n"
"v_add_u32 v102, s3, v102\n"
"s_lshr_b32 s3, s4, 2\n"
"s_lshl_b32 s3, s3, 7\n"
"s_and_b32 s49, s4, 3\n"
"s_lshl_b32 s49, s49, 3\n"
"s_add_u32 s3, s3, s49\n"
"buffer_load_ushort v103, v102, s[28:31], s3 offen\n"
"buffer_load_ushort v104, v102, s[28:31], s3 offen offset:32\n"
"v_mov_b32 v97, 0x0\n"
"v_bfe_i32 v126, v107, 0, 1\n"
"v_and_b32 v126, 0x8, v126\n"
"v_add_u32 v97, v97, v126\n"
"v_bfe_i32 v126, v107, 1, 1\n"
"v_and_b32 v126, 0x10, v126\n"
"v_add_u32 v97, v97, v126\n"
"v_bfe_i32 v126, v107, 2, 1\n"
"v_and_b32 v126, 0x420, v126\n"
"v_add_u32 v97, v97, v126\n"
"v_bfe_i32 v126, v107, 3, 1\n"
"v_and_b32 v126, 0x1040, v126\n"
"v_add_u32 v97, v97, v126\n"
"v_bfe_i32 v126, v107, 4, 1\n"
"v_and_b32 v126, 0x4080, v126\n"
"v_add_u32 v97, v97, v126\n"
"v_bfe_i32 v126, v107, 5, 1\n"
"v_and_b32 v126, 0x2000, v126\n"
"v_add_u32 v97, v97, v126\n"
"s_mul_i32 s3, s5, 0x100\n"
"v_add_u32 v97, s3, v97\n"
"s_waitcnt vmcnt(18)\n"
"s_barrier\n"
"v_xor_b32 v98, 0, v97\n"
"ds_read_b64_tr_b16 v[0:1], v98 offset:0\n"
"ds_read_b64_tr_b16 v[2:3], v98 offset:2048\n"
"ds_read_b64_tr_b16 v[4:5], v98 offset:32768\n"
"ds_read_b64_tr_b16 v[6:7], v98 offset:34816\n"
"v_xor_b32 v98, 32, v97\n"
"ds_read_b64_tr_b16 v[8:9], v98 offset:0\n"
"ds_read_b64_tr_b16 v[10:11], v98 offset:2048\n"
"ds_read_b64_tr_b16 v[12:13], v98 offset:32768\n"
"ds_read_b64_tr_b16 v[14:15], v98 offset:34816\n"
"v_xor_b32 v98, 64, v97\n"
"ds_read_b64_tr_b16 v[16:17], v98 offset:0\n"
"ds_read_b64_tr_b16 v[18:19], v98 offset:2048\n"
"ds_read_b64_tr_b16 v[20:21], v98 offset:32768\n"
"ds_read_b64_tr_b16 v[22:23], v98 offset:34816\n"
"v_xor_b32 v98, 96, v97\n"
"ds_read_b64_tr_b16 v[24:25], v98 offset:0\n"
"ds_read_b64_tr_b16 v[26:27], v98 offset:2048\n"
"ds_read_b64_tr_b16 v[28:29], v98 offset:32768\n"
"ds_read_b64_tr_b16 v[30:31], v98 offset:34816\n"
"v_xor_b32 v98, 128, v97\n"
"ds_read_b64_tr_b16 v[32:33], v98 offset:0\n"
"ds_read_b64_tr_b16 v[34:35], v98 offset:2048\n"
"ds_read_b64_tr_b16 v[36:37], v98 offset:32768\n"
"ds_read_b64_tr_b16 v[38:39], v98 offset:34816\n"
"v_xor_b32 v98, 160, v97\n"
"ds_read_b64_tr_b16 v[40:41], v98 offset:0\n"
"ds_read_b64_tr_b16 v[42:43], v98 offset:2048\n"
"ds_read_b64_tr_b16 v[44:45], v98 offset:32768\n"
"ds_read_b64_tr_b16 v[46:47], v98 offset:34816\n"
"v_xor_b32 v98, 192, v97\n"
"ds_read_b64_tr_b16 v[48:49], v98 offset:0\n"
"ds_read_b64_tr_b16 v[50:51], v98 offset:2048\n"
"ds_read_b64_tr_b16 v[52:53], v98 offset:32768\n"
"ds_read_b64_tr_b16 v[54:55], v98 offset:34816\n"
"v_xor_b32 v98, 224, v97\n"
"ds_read_b64_tr_b16 v[56:57], v98 offset:0\n"
"ds_read_b64_tr_b16 v[58:59], v98 offset:2048\n"
"ds_read_b64_tr_b16 v[60:61], v98 offset:32768\n"
"ds_read_b64_tr_b16 v[62:63], v98 offset:34816\n"
"s_waitcnt lgkmcnt(0)\n"
"s_barrier\n"
"ds_write_b128 v112, v[0:3] offset:0\n"
"ds_write_b128 v112, v[4:7] offset:1024\n"
"ds_write_b128 v112, v[8:11] offset:2048\n"
"ds_write_b128 v112, v[12:15] offset:3072\n"
"ds_write_b128 v112, v[16:19] offset:4096\n"
"ds_write_b128 v112, v[20:23] offset:5120\n"
"ds_write_b128 v112, v[24:27] offset:6144\n"
"ds_write_b128 v112, v[28:31] offset:7168\n"
"ds_write_b128 v112, v[32:35] offset:8192\n"
"ds_write_b128 v112, v[36:39] offset:9216\n"
"ds_write_b128 v112, v[40:43] offset:10240\n"
"ds_write_b128 v112, v[44:47] offset:11264\n"
"ds_write_b128 v112, v[48:51] offset:12288\n"
"ds_write_b128 v112, v[52:55] offset:13312\n"
"ds_write_b128 v112, v[56:59] offset:14336\n"
"ds_write_b128 v112, v[60:63] offset:15360\n"
"s_waitcnt lgkmcnt(0)\n"
"s_waitcnt vmcnt(0)\n"
"s_barrier\n"
"v_add_u32 v97, 0x10000, v97\n"
"v_lshl_or_b32 v103, v104, 16, v103\n"
"v_min_u32 v103, -2, v103\n"
"s_mov_b64 exec, s[54:55]\n"
"buffer_store_dword v103, v116, s[8:11], s38 offen sc1\n"
"s_mov_b64 exec, -1\n"
"v_xor_b32 v98, 0, v97\n"
"ds_read_b64_tr_b16 v[0:1], v98 offset:0\n"
"ds_read_b64_tr_b16 v[2:3], v98 offset:2048\n"
"ds_read_b64_tr_b16 v[4:5], v98 offset:32768\n"
"ds_read_b64_tr_b16 v[6:7], v98 offset:34816\n"
"v_xor_b32 v98, 32, v97\n"
"ds_read_b64_tr_b16 v[8:9], v98 offset:0\n"
"ds_read_b64_tr_b16 v[10:11], v98 offset:2048\n"
"ds_read_b64_tr_b16 v[12:13], v98 offset:32768\n"
"ds_read_b64_tr_b16 v[14:15], v98 offset:34816\n"
"v_xor_b32 v98, 64, v97\n"
"ds_read_b64_tr_b16 v[16:17], v98 offset:0\n"
"ds_read_b64_tr_b16 v[18:19], v98 offset:2048\n"
"ds_read_b64_tr_b16 v[20:21], v98 offset:32768\n"
"ds_read_b64_tr_b16 v[22:23], v98 offset:34816\n"
"v_xor_b32 v98, 96, v97\n"
"ds_read_b64_tr_b16 v[24:25], v98 offset:0\n"
"ds_read_b64_tr_b16 v[26:27], v98 offset:2048\n"
"ds_read_b64_tr_b16 v[28:29], v98 offset:32768\n"
"ds_read_b64_tr_b16 v[30:31], v98 offset:34816\n"
"v_xor_b32 v98, 128, v97\n"
"ds_read_b64_tr_b16 v[32:33], v98 offset:0\n"
"ds_read_b64_tr_b16 v[34:35], v98 offset:2048\n"
"ds_read_b64_tr_b16 v[36:37], v98 offset:32768\n"
"ds_read_b64_tr_b16 v[38:39], v98 offset:34816\n"
"v_xor_b32 v98, 160, v97\n"
"ds_read_b64_tr_b16 v[40:41], v98 offset:0\n"
"ds_read_b64_tr_b16 v[42:43], v98 offset:2048\n"
"ds_read_b64_tr_b16 v[44:45], v98 offset:32768\n"
"ds_read_b64_tr_b16 v[46:47], v98 offset:34816\n"
"v_xor_b32 v98, 192, v97\n"
"ds_read_b64_tr_b16 v[48:49], v98 offset:0\n"
"ds_read_b64_tr_b16 v[50:51], v98 offset:2048\n"
"ds_read_b64_tr_b16 v[52:53], v98 offset:32768\n"
"ds_read_b64_tr_b16 v[54:55], v98 offset:34816\n"
"v_xor_b32 v98, 224, v97\n"
"ds_read_b64_tr_b16 v[56:57], v98 offset:0\n"
"ds_read_b64_tr_b16 v[58:59], v98 offset:2048\n"
"ds_read_b64_tr_b16 v[60:61], v98 offset:32768\n"
"ds_read_b64_tr_b16 v[62:63], v98 offset:34816\n"
"s_waitcnt lgkmcnt(0)\n"
"s_barrier\n"
"ds_write_b128 v113, v[0:3] offset:0\n"
"ds_write_b128 v113, v[4:7] offset:1024\n"
"ds_write_b128 v113, v[8:11] offset:2048\n"
"ds_write_b128 v113, v[12:15] offset:3072\n"
"ds_write_b128 v113, v[16:19] offset:4096\n"
"ds_write_b128 v113, v[20:23] offset:5120\n"
"ds_write_b128 v113, v[24:27] offset:6144\n"
"ds_write_b128 v113, v[28:31] offset:7168\n"
"ds_write_b128 v113, v[32:35] offset:8192\n"
"ds_write_b128 v113, v[36:39] offset:9216\n"
"ds_write_b128 v113, v[40:43] offset:10240\n"
"ds_write_b128 v113, v[44:47] offset:11264\n"
"ds_write_b128 v113, v[48:51] offset:12288\n"
"ds_write_b128 v113, v[52:55] offset:13312\n"
"ds_write_b128 v113, v[56:59] offset:14336\n"
"ds_write_b128 v113, v[60:63] offset:15360\n"
"s_waitcnt lgkmcnt(0)\n"
"s_add_u32 s38, s38, 0x4000\n"
"s_cmp_gt_i32 s48, 1\n"
"s_cbranch_scc1 Lkvok_1\n"
"Lkvlp_2:\n"
"buffer_load_dwordx2 v[126:127], off, s[24:27], s51 sc1\n"
"s_waitcnt vmcnt(0)\n"
"v_min_i32 v126, v126, v127\n"
"s_nop 0\n"
"v_readfirstlane_b32 s48, v126\n"
"s_sub_u32 s47, s47, 1\n"
"s_cbranch_scc1 L_chain_fail\n"
"s_cmp_gt_i32 s48, 1\n"
"s_cbranch_scc0 Lkvlp_2\n"
"Lkvok_1:\n"
"s_mov_b32 m0, s42\n"
"s_nop 0\n"
"buffer_load_dwordx4 v112, s[12:15], s41 offen sc1 lds\n"
"buffer_load_dwordx4 v112, s[12:15], s41 offen offset:1024 sc1 lds\n"
"s_mov_b32 m0, s95\n"
"s_nop 0\n"
"buffer_load_dwordx4 v112, s[16:19], s41 offen sc1 lds\n"
"buffer_load_dwordx4 v112, s[16:19], s41 offen offset:1024 sc1 lds\n"
"s_add_u32 s41, s41, 0x2000\n"
"s_xor_b32 s95, s95, 0x6000\n"
"v_and_b32 v126, 0x3ff, v112\n"
"v_add_u32 v126, 0x27c00, v126\n"
"ds_write_b128 v126, v[108:111]\n"
"s_mov_b32 s44, 0x7fff0000\n"
"s_mov_b32 s43, -1\n"
"v_mov_b32 v64, 0\n"
"v_mov_b32 v65, 0\n"
"v_mov_b32 v66, 0\n"
"v_mov_b32 v67, 0\n"
"v_mov_b32 v68, 0\n"
"v_mov_b32 v69, 0\n"
"v_mov_b32 v70, 0\n"
"v_mov_b32 v71, 0\n"
"v_mov_b32 v72, 0\n"
"v_mov_b32 v73, 0\n"
"v_mov_b32 v74, 0\n"
"v_mov_b32 v75, 0\n"
"v_mov_b32 v76, 0\n"
"v_mov_b32 v77, 0\n"
"v_mov_b32 v78, 0\n"
"v_mov_b32 v79, 0\n"
"v_mov_b32 v80, 0\n"
"v_mov_b32 v81, 0\n"
"v_mov_b32 v82, 0\n"
"v_mov_b32 v83, 0\n"
"v_mov_b32 v84, 0\n"
"v_mov_b32 v85, 0\n"
"v_mov_b32 v86, 0\n"
"v_mov_b32 v87, 0\n"
"v_mov_b32 v101, 0\n"
"v_mov_b32 v103, 0\n"
"v_mov_b32 v105, 0\n"
"s_waitcnt vmcnt(0) lgkmcnt(0)\n"
"s_barrier\n"
"ds_read_b128 v[32:35], v121 offset:0\n"
"ds_read_b128 v[36:39], v121 offset:2048\n"
"ds_read_b128 v[40:43], v121 offset:4096\n"
"ds_read_b128 v[44:47], v121 offset:6144\n"
"ds_read_b128 v[48:51], v121 offset:128\n"
"ds_read_b128 v[52:55], v121 offset:2176\n"
"ds_read_b128 v[56:59], v121 offset:4224\n"
"ds_read_b128 v[60:63], v121 offset:6272\n"
"s_waitcnt lgkmcnt(0)\n"
"s_barrier\n"
"L_chain_top:\n"
"L_chain_regather:\n"
"buffer_load_dwordx4 v[0:3], v112, s[8:11], s34 offen offset:0 sc1\n"
"s_nop 12\n"
"buffer_load_dwordx4 v[4:7], v112, s[8:11], s34 offen offset:1024 sc1\n"
"s_nop 12\n"
"buffer_load_dwordx4 v[8:11], v112, s[8:11], s34 offen offset:2048 sc1\n"
"s_nop 12\n"
"buffer_load_dwordx4 v[12:15], v112, s[8:11], s34 offen offset:3072 sc1\n"
"s_nop 12\n"
"buffer_load_dwordx4 v[16:19], v112, s[8:11], s35 offen offset:0 sc1\n"
"s_nop 12\n"
"buffer_load_dwordx4 v[20:23], v112, s[8:11], s35 offen offset:1024 sc1\n"
"s_nop 12\n"
"buffer_load_dwordx4 v[24:27], v112, s[8:11], s35 offen offset:2048 sc1\n"
"s_nop 12\n"
"buffer_load_dwordx4 v[28:31], v112, s[8:11], s35 offen offset:3072 sc1\n"
"s_mov_b32 m0, s42\n"
"v_mov_b32 v127, s43\n"
"s_waitcnt vmcnt(7)\n"
"v_smfmac_f32_16x16x64_bf16 v[64:67], v[0:3], a[0:7], v125\n"
"v_smfmac_f32_16x16x64_bf16 v[68:71], v[0:3], a[64:71], v125\n"
"v_smfmac_f32_16x16x64_bf16 v[72:75], v[0:3], a[128:135], v125\n"
"v_smfmac_f32_16x16x64_bf16 v[76:79], v[0:3], a[192:199], v125\n"
"s_waitcnt vmcnt(6)\n"
"v_smfmac_f32_16x16x64_bf16 v[64:67], v[4:7], a[8:15], v125\n"
"buffer_load_dwordx4 v112, s[12:15], s41 offen sc1 lds\n"
"v_smfmac_f32_16x16x64_bf16 v[68:71], v[4:7], a[72:79], v125\n"
"v_smfmac_f32_16x16x64_bf16 v[72:75], v[4:7], a[136:143], v125\n"
"v_smfmac_f32_16x16x64_bf16 v[76:79], v[4:7], a[200:207], v125\n"
"s_waitcnt vmcnt(6)\n"
"v_smfmac_f32_16x16x64_bf16 v[64:67], v[8:11], a[16:23], v125\n"
"buffer_load_dwordx4 v112, s[12:15], s41 offen offset:1024 sc1 lds\n"
"s_mov_b32 m0, s95\n"
"v_smfmac_f32_16x16x64_bf16 v[68:71], v[8:11], a[80:87], v125\n"
"v_smfmac_f32_16x16x64_bf16 v[72:75], v[8:11], a[144:151], v125\n"
"v_smfmac_f32_16x16x64_bf16 v[76:79], v[8:11], a[208:215], v125\n"
"s_waitcnt vmcnt(6)\n"
"v_smfmac_f32_16x16x64_bf16 v[64:67], v[12:15], a[24:31], v125\n"
"buffer_load_dwordx4 v112, s[16:19], s41 offen sc1 lds\n"
"v_smfmac_f32_16x16x64_bf16 v[68:71], v[12:15], a[88:95], v125\n"
"v_smfmac_f32_16x16x64_bf16 v[72:75], v[12:15], a[152:159], v125\n"
"v_smfmac_f32_16x16x64_bf16 v[76:79], v[12:15], a[216:223], v125\n"
"s_waitcnt vmcnt(6)\n"
"v_smfmac_f32_16x16x64_bf16 v[64:67], v[16:19], a[32:39], v125\n"
"buffer_load_dwordx4 v112, s[16:19], s41 offen offset:1024 sc1 lds\n"
"v_smfmac_f32_16x16x64_bf16 v[68:71], v[16:19], a[96:103], v125\n"
"v_smfmac_f32_16x16x64_bf16 v[72:75], v[16:19], a[160:167], v125\n"
"v_smfmac_f32_16x16x64_bf16 v[76:79], v[16:19], a[224:231], v125\n"
"s_waitcnt vmcnt(6)\n"
"v_smfmac_f32_16x16x64_bf16 v[64:67], v[20:23], a[40:47], v125\n"
"buffer_store_dwordx2 v[106:107], v123, s[20:23], s44 offen sc1\n"
"v_smfmac_f32_16x16x64_bf16 v[68:71], v[20:23], a[104:111], v125\n"
"v_smfmac_f32_16x16x64_bf16 v[72:75], v[20:23], a[168:175], v125\n"
"v_smfmac_f32_16x16x64_bf16 v[76:79], v[20:23], a[232:239], v125\n"
"s_waitcnt vmcnt(6)\n"
"v_smfmac_f32_16x16x64_bf16 v[64:67], v[24:27], a[48:55], v125\n"
"buffer_store_dwordx2 v[100:101], v124, s[20:23], s44 offen sc1\n"
"v_smfmac_f32_16x16x64_bf16 v[68:71], v[24:27], a[112:119], v125\n"
"v_smfmac_f32_16x16x64_bf16 v[72:75], v[24:27], a[176:183], v125\n"
"v_smfmac_f32_16x16x64_bf16 v[76:79], v[24:27], a[240:247], v125\n"
"s_waitcnt vmcnt(6)\n"
"v_smfmac_f32_16x16x64_bf16 v[64:67], v[28:31], a[56:63], v125\n"
"s_mov_b64 exec, 1\n"
"buffer_store_dword v127, off, s[24:27], s57 sc1\n"
"s_mov_b64 exec, -1\n"
"v_smfmac_f32_16x16x64_bf16 v[68:71], v[28:31], a[120:127], v125\n"
"v_smfmac_f32_16x16x64_bf16 v[72:75], v[28:31], a[184:191], v125\n"
"v_smfmac_f32_16x16x64_bf16 v[76:79], v[28:31], a[248:255], v125\n"
"v_add_f32 v64, v64, v66\n"
"v_add_f32 v65, v65, v67\n"
"v_add_f32 v68, v68, v70\n"
"v_add_f32 v69, v69, v71\n"
"v_cvt_pk_bf16_f32 v96, v64, v68\n"
"v_cvt_pk_bf16_f32 v97, v65, v69\n"
"v_add_f32 v72, v72, v74\n"
"ds_write2_b32 v117, v96, v97 offset1:144\n"
"v_add_f32 v73, v73, v75\n"
"v_add_f32 v76, v76, v78\n"
"v_add_f32 v77, v77, v79\n"
"v_cvt_pk_bf16_f32 v106, v72, v76\n"
"v_cvt_pk_bf16_f32 v107, v73, v77\n"
"ds_write2_b32 v117, v106, v107 offset0:72 offset1:216\n"
"v_cmp_u_f32 vcc, v64, v65\n"
"v_smfmac_f32_16x16x64_bf16 v[80:83], v[0:3], v[128:135], v125\n"
"v_smfmac_f32_16x16x64_bf16 v[84:87], v[0:3], v[192:199], v125\n"
"v_smfmac_f32_16x16x64_bf16 v[80:83], v[4:7], v[136:143], v125\n"
"v_smfmac_f32_16x16x64_bf16 v[84:87], v[4:7], v[200:207], v125\n"
"s_cbranch_vccnz L_chain_retry2\n"
"L_chain_cont2:\n"
"s_waitcnt lgkmcnt(0)\n"
"s_barrier\n"
"ds_read_b128 v[64:67], v118 offset:0\n"
"ds_read_b128 v[68:71], v118 offset:64\n"
"ds_read_b128 v[72:75], v118 offset:128\n"
"ds_read_b128 v[76:79], v118 offset:192\n"
"v_smfmac_f32_16x16x64_bf16 v[80:83], v[8:11], v[144:151], v125\n"
"v_smfmac_f32_16x16x64_bf16 v[84:87], v[8:11], v[208:215], v125\n"
"v_smfmac_f32_16x16x64_bf16 v[80:83], v[12:15], v[152:159], v125\n"
"v_smfmac_f32_16x16x64_bf16 v[84:87], v[12:15], v[216:223], v125\n"
"s_waitcnt lgkmcnt(3)\n"
"v_mfma_f32_16x16x32_bf16 v[88:91], v[32:35], v[64:67], 0\n"
"ds_read_b128 v[0:3], v118 offset:288\n"
"v_mfma_f32_16x16x32_bf16 v[92:95], v[48:51], v[64:67], 0\n"
"ds_read_b128 v[4:7], v118 offset:352\n"
"s_waitcnt lgkmcnt(4)\n"
"v_mfma_f32_16x16x32_bf16 v[88:91], v[36:39], v[68:71], v[88:91]\n"
"ds_read_b128 v[8:11], v118 offset:416\n"
"v_mfma_f32_16x16x32_bf16 v[92:95], v[52:55], v[68:71], v[92:95]\n"
"ds_read_b128 v[12:15], v118 offset:480\n"
"s_waitcnt lgkmcnt(5)\n"
"v_mfma_f32_16x16x32_bf16 v[88:91], v[40:43], v[72:75], v[88:91]\n"
"v_mfma_f32_16x16x32_bf16 v[92:95], v[56:59], v[72:75], v[92:95]\n"
"s_waitcnt lgkmcnt(4)\n"
"v_mfma_f32_16x16x32_bf16 v[88:91], v[44:47], v[76:79], v[88:91]\n"
"v_mfma_f32_16x16x32_bf16 v[92:95], v[60:63], v[76:79], v[92:95]\n"
"s_waitcnt lgkmcnt(3)\n"
"v_mfma_f32_16x16x32_bf16 v[96:99], v[0:3], v[64:67], 0\n"
"ds_read_b128 v[32:35], v112 offset:0\n"
"ds_read_b128 v[36:39], v112 offset:1024\n"
"s_waitcnt lgkmcnt(4)\n"
"v_mfma_f32_16x16x32_bf16 v[96:99], v[4:7], v[68:71], v[96:99]\n"
"ds_read_b128 v[40:43], v113 offset:0\n"
"v_max3_f32 v126, v88, v89, v90\n"
"s_waitcnt lgkmcnt(4)\n"
"v_mfma_f32_16x16x32_bf16 v[96:99], v[8:11], v[72:75], v[96:99]\n"
"ds_read_b128 v[44:47], v113 offset:1024\n"
"v_max3_f32 v127, v91, v92, v93\n"
"s_waitcnt lgkmcnt(4)\n"
"v_mfma_f32_16x16x32_bf16 v[96:99], v[12:15], v[76:79], v[96:99]\n"
"ds_read_b128 v[48:51], v112 offset:2048\n"
"v_max3_f32 v126, v126, v94, v95\n"
"v_smfmac_f32_16x16x64_bf16 v[80:83], v[16:19], v[160:167], v125\n"
"v_max_f32 v126, v126, v127\n"
"v_smfmac_f32_16x16x64_bf16 v[84:87], v[16:19], v[224:231], v125\n"
"ds_read_b128 v[52:55], v112 offset:3072\n"
"s_cmp_eq_u32 s32, 63\n"
"s_cbranch_scc1 L_chain_last\n"
"v_max3_f32 v127, v126, v96, v97\n"
"v_max3_f32 v126, v126, v96, v97\n"
"v_smfmac_f32_16x16x64_bf16 v[80:83], v[20:23], v[168:175], v125\n"
"ds_read_b128 v[56:59], v113 offset:2048\n"
"v_permlane32_swap_b32 v126, v127\n"
"v_max_f32 v98, v126, v127\n"
"v_max_f32 v126, v126, v127\n"
"v_smfmac_f32_16x16x64_bf16 v[84:87], v[20:23], v[232:239], v125\n"
"ds_read_b128 v[60:63], v113 offset:3072\n"
"v_permlane16_swap_b32 v126, v98\n"
"v_max_f32 v126, v126, v98\n"
"v_mul_f32 v126, s56, v126\n"
"v_fma_f32 v88, v88, s56, -v126\n"
"v_smfmac_f32_16x16x64_bf16 v[80:83], v[24:27], v[176:183], v125\n"
"ds_read_b128 v[64:67], v112 offset:4096\n"
"v_fma_f32 v89, v89, s56, -v126\n"
"v_exp_f32 v88, v88\n"
"ds_read_b128 v[16:19], v122 offset:0\n"
"v_exp_f32 v89, v89\n"
"v_smfmac_f32_16x16x64_bf16 v[84:87], v[24:27], v[240:247], v125\n"
"ds_read_b128 v[20:23], v122 offset:256\n"
"v_fma_f32 v90, v90, s56, -v126\n"
"v_fma_f32 v91, v91, s56, -v126\n"
"v_exp_f32 v90, v90\n"
"v_smfmac_f32_16x16x64_bf16 v[80:83], v[28:31], v[184:191], v125\n"
"ds_read_b128 v[68:71], v112 offset:5120\n"
"v_cvt_pk_bf16_f32 v88, v88, v89\n"
"v_exp_f32 v91, v91\n"
"v_fma_f32 v92, v92, s56, -v126\n"
"v_smfmac_f32_16x16x64_bf16 v[84:87], v[28:31], v[248:255], v125\n"
"ds_read_b128 v[72:75], v113 offset:4096\n"
"v_fma_f32 v93, v93, s56, -v126\n"
"v_exp_f32 v92, v92\n"
"v_cvt_pk_bf16_f32 v89, v90, v91\n"
"v_exp_f32 v93, v93\n"
"v_fma_f32 v94, v94, s56, -v126\n"
"v_fma_f32 v95, v95, s56, -v126\n"
"v_exp_f32 v94, v94\n"
"v_mov_b64 v[28:29], s[6:7]\n"
"v_exp_f32 v95, v95\n"
"v_cvt_pk_bf16_f32 v90, v92, v93\n"
"v_fma_f32 v96, v96, s56, -v126\n"
"v_fma_f32 v97, v97, s56, -v126\n"
"v_mov_b64 v[30:31], s[6:7]\n"
"v_cvt_pk_bf16_f32 v91, v94, v95\n"
"v_exp_f32 v96, v96\n"
"v_exp_f32 v97, v97\n"
"v_mfma_f32_16x16x32_bf16 v[24:27], v[28:31], v[88:91], 0\n"
"v_cvt_pk_bf16_f32 v104, v96, v97\n"
"v_add_f32 v80, v80, v82\n"
"s_waitcnt lgkmcnt(2)\n"
"v_mfma_f32_16x16x32_bf16 v[92:95], v[16:19], v[88:91], 0\n"
"v_add_f32 v81, v81, v83\n"
"v_add_f32 v84, v84, v86\n"
"v_mfma_f32_16x16x16_bf16 v[24:27], v[28:29], v[104:105], v[24:27]\n"
"v_add_f32 v85, v85, v87\n"
"v_cvt_pk_bf16_f32 v100, v80, v81\n"
"v_mfma_f32_16x16x32_bf16 v[96:99], v[20:23], v[88:91], 0\n"
"v_cvt_pk_bf16_f32 v101, v84, v85\n"
"v_cvt_pk_bf16_f32 v102, v84, v85\n"
"ds_read_b128 v[76:79], v113 offset:5120\n"
"v_mfma_f32_16x16x16_bf16 v[92:95], v[100:101], v[104:105], v[92:95]\n"
"ds_read_b128 v[0:3], v112 offset:6144\n"
"ds_read_b128 v[4:7], v112 offset:7168\n"
"ds_read_b128 v[8:11], v113 offset:6144\n"
"v_mfma_f32_16x16x16_bf16 v[96:99], v[102:103], v[104:105], v[96:99]\n"
"v_rcp_f32 v126, v24\n"
"ds_read_b128 v[12:15], v113 offset:7168\n"
"v_mov_b64 v[80:81], 0\n"
"v_mov_b64 v[82:83], 0\n"
"v_mul_f32 v92, v92, v126\n"
"v_mul_f32 v93, v93, v126\n"
"v_mul_f32 v94, v94, v126\n"
"v_mul_f32 v95, v95, v126\n"
"v_cvt_pk_bf16_f32 v88, v92, v93\n"
"v_cvt_pk_bf16_f32 v89, v94, v95\n"
"v_pk_mul_f32 v[96:97], v[96:97], v[126:127] op_sel_hi:[1,0]\n"
"v_pk_mul_f32 v[98:99], v[98:99], v[126:127] op_sel_hi:[1,0]\n"
"s_mov_b64 exec, s[52:53]\n"
"v_cvt_pk_bf16_f32 v90, v96, v97\n"
"v_cvt_pk_bf16_f32 v91, v98, v99\n"
"ds_write_b128 v119, v[88:91]\n"
"s_mov_b64 exec, -1\n"
"v_mov_b64 v[84:85], 0\n"
"v_mov_b64 v[86:87], 0\n"
"v_mov_b64 v[16:17], 0\n"
"v_mov_b64 v[18:19], 0\n"
"v_mov_b64 v[20:21], 0\n"
"v_mov_b64 v[22:23], 0\n"
"v_mov_b64 v[24:25], 0\n"
"v_mov_b64 v[26:27], 0\n"
"v_mov_b64 v[28:29], 0\n"
"v_mov_b64 v[30:31], 0\n"
"v_mov_b64 v[92:93], 0\n"
"v_mov_b64 v[94:95], 0\n"
"v_mov_b64 v[96:97], 0\n"
"v_mov_b64 v[98:99], 0\n"
"s_waitcnt vmcnt(5) lgkmcnt(0)\n"
"s_barrier\n"
"ds_read_b128 v[88:91], v120\n"
"ds_read_b128 v[108:111], v120 offset:128\n"
"s_nop 2\n"
"v_mov_b32 v127, 0\n"
"s_nop 2\n"
"v_mov_b32 v127, 0\n"
"s_nop 2\n"
"v_mov_b32 v127, 0\n"
"s_waitcnt lgkmcnt(1)\n"
"v_smfmac_f32_16x16x64_bf16 v[16:19], v[88:91], v[32:39], v125\n"
"ds_read_b128 v[32:35], v112 offset:8192\n"
"ds_read_b128 v[36:39], v112 offset:9216\n"
"s_waitcnt lgkmcnt(2)\n"
"v_smfmac_f32_16x16x64_bf16 v[16:19], v[108:111], v[40:47], v125\n"
"ds_read_b128 v[40:43], v113 offset:8192\n"
"ds_read_b128 v[44:47], v113 offset:9216\n"
"v_smfmac_f32_16x16x64_bf16 v[20:23], v[88:91], v[48:55], v125\n"
"ds_read_b128 v[48:51], v112 offset:10240\n"
"ds_read_b128 v[52:55], v112 offset:11264\n"
"v_smfmac_f32_16x16x64_bf16 v[20:23], v[108:111], v[56:63], v125\n"
"ds_read_b128 v[56:59], v113 offset:10240\n"
"v_add_f32 v126, v16, v18\n"
"ds_read_b128 v[60:63], v113 offset:11264\n"
"v_add_f32 v127, v17, v19\n"
"v_smfmac_f32_16x16x64_bf16 v[24:27], v[88:91], v[64:71], v125\n"
"ds_read_b128 v[64:67], v112 offset:12288\n"
"v_cvt_pk_bf16_f32 v126, v126, v127\n"
"ds_read_b128 v[68:71], v112 offset:13312\n"
"v_and_or_b32 v16, v126, -2, s33\n"
"v_smfmac_f32_16x16x64_bf16 v[24:27], v[108:111], v[72:79], v125\n"
"ds_read_b128 v[72:75], v113 offset:12288\n"
"v_add_f32 v126, v20, v22\n"
"ds_read_b128 v[76:79], v113 offset:13312\n"
"v_add_f32 v127, v21, v23\n"
"v_smfmac_f32_16x16x64_bf16 v[28:31], v[88:91], v[0:7], v125\n"
"ds_read_b128 v[0:3], v112 offset:14336\n"
"v_cvt_pk_bf16_f32 v126, v126, v127\n"
"ds_read_b128 v[4:7], v112 offset:15360\n"
"v_and_or_b32 v17, v126, -2, s33\n"
"v_smfmac_f32_16x16x64_bf16 v[28:31], v[108:111], v[8:15], v125\n"
"ds_read_b128 v[8:11], v113 offset:14336\n"
"v_add_f32 v126, v24, v26\n"
"ds_read_b128 v[12:15], v113 offset:15360\n"
"v_add_f32 v127, v25, v27\n"
"s_waitcnt lgkmcnt(12)\n"
"v_smfmac_f32_16x16x64_bf16 v[80:83], v[88:91], v[32:39], v125\n"
"v_cvt_pk_bf16_f32 v126, v126, v127\n"
"v_and_or_b32 v18, v126, -2, s33\n"
"v_smfmac_f32_16x16x64_bf16 v[80:83], v[108:111], v[40:47], v125\n"
"v_add_f32 v126, v28, v30\n"
"v_add_f32 v127, v29, v31\n"
"v_cvt_pk_bf16_f32 v126, v126, v127\n"
"v_and_or_b32 v19, v126, -2, s33\n"
"s_waitcnt lgkmcnt(8)\n"
"v_smfmac_f32_16x16x64_bf16 v[84:87], v[88:91], v[48:55], v125\n"
"buffer_store_dwordx4 v[16:19], v114, s[8:11], s39 offen sc1\n"
"v_smfmac_f32_16x16x64_bf16 v[84:87], v[108:111], v[56:63], v125\n"
"s_waitcnt lgkmcnt(4)\n"
"v_smfmac_f32_16x16x64_bf16 v[92:95], v[88:91], v[64:71], v125\n"
"v_add_f32 v126, v80, v82\n"
"v_add_f32 v127, v81, v83\n"
"v_cvt_pk_bf16_f32 v126, v126, v127\n"
"v_and_or_b32 v80, v126, -2, s33\n"
"v_smfmac_f32_16x16x64_bf16 v[92:95], v[108:111], v[72:79], v125\n"
"s_waitcnt lgkmcnt(0)\n"
"v_smfmac_f32_16x16x64_bf16 v[96:99], v[88:91], v[0:7], v125\n"
"v_add_f32 v126, v84, v86\n"
"v_add_f32 v127, v85, v87\n"
"v_cvt_pk_bf16_f32 v126, v126, v127\n"
"v_and_or_b32 v81, v126, -2, s33\n"
"v_smfmac_f32_16x16x64_bf16 v[96:99], v[108:111], v[8:15], v125\n"
"s_nop 0\n"
"v_add_f32 v126, v92, v94\n"
"v_add_f32 v127, v93, v95\n"
"v_cvt_pk_bf16_f32 v126, v126, v127\n"
"v_and_or_b32 v82, v126, -2, s33\n"
"s_nop 2\n"
"v_add_f32 v126, v96, v98\n"
"v_add_f32 v127, v97, v99\n"
"v_cvt_pk_bf16_f32 v126, v126, v127\n"
"v_and_or_b32 v83, v126, -2, s33\n"
"buffer_store_dwordx4 v[80:83], v114, s[8:11], s40 offen sc1\n"
"v_and_b32 v126, 0x3ff, v112\n"
"v_add_u32 v126, 0x27c00, v126\n"
"ds_read_b128 v[108:111], v126\n"
"ds_read_b128 v[32:35], v121 offset:0\n"
"ds_read_b128 v[36:39], v121 offset:2048\n"
"ds_read_b128 v[40:43], v121 offset:4096\n"
"ds_read_b128 v[44:47], v121 offset:6144\n"
"ds_read_b128 v[48:51], v121 offset:128\n"
"ds_read_b128 v[52:55], v121 offset:2176\n"
"ds_read_b128 v[56:59], v121 offset:4224\n"
"ds_read_b128 v[60:63], v121 offset:6272\n"
"v_mov_b64 v[64:65], 0\n"
"v_mov_b64 v[66:67], 0\n"
"v_mov_b64 v[68:69], 0\n"
"v_mov_b64 v[70:71], 0\n"
"v_mov_b64 v[72:73], 0\n"
"v_mov_b64 v[74:75], 0\n"
"v_mov_b64 v[76:77], 0\n"
"v_mov_b64 v[78:79], 0\n"
"s_add_u32 s34, s34, 0x4000\n"
"s_add_u32 s35, s35, 0x4000\n"
"s_lshl_b32 s44, s32, 17\n"
"s_add_u32 s41, s41, 0x2000\n"
"v_mov_b64 v[80:81], 0\n"
"v_mov_b64 v[82:83], 0\n"
"v_mov_b64 v[84:85], 0\n"
"v_mov_b64 v[86:87], 0\n"
"s_xor_b32 s95, s95, 0x6000\n"
"v_xor_b32 v122, 0x6000, v122\n"
"s_mov_b32 s43, s32\n"
"s_waitcnt lgkmcnt(0)\n"
"s_mov_b32 s49, s45\n"
"s_cmp_eq_u32 s49, 0\n"
"s_cbranch_scc1 Ldd_4\n"
".p2align 4\n"
"Ldl_3:\n"
"s_sub_u32 s49, s49, 1\n"
"s_cmp_lg_u32 s49, 0\n"
"s_cbranch_scc1 Ldl_3\n"
"Ldd_4:\n"
"buffer_load_dwordx4 v[0:3], v115, s[8:11], s36 offen sc1\n"
"buffer_load_dwordx4 v[4:7], v115, s[8:11], s37 offen sc1\n"
"s_nop 2\n"
"v_mov_b32 v127, 0\n"
"s_nop 2\n"
"v_mov_b32 v127, 0\n"
"s_nop 2\n"
"v_mov_b32 v127, 0\n"
"s_nop 2\n"
"v_mov_b32 v127, 0\n"
"s_nop 2\n"
"v_mov_b32 v127, 0\n"
"s_nop 2\n"
"v_mov_b32 v127, 0\n"
"s_nop 2\n"
"v_mov_b32 v127, 0\n"
"buffer_load_dwordx4 v[8:11], v115, s[8:11], s36 offen sc1\n"
"buffer_load_dwordx4 v[12:15], v115, s[8:11], s37 offen sc1\n"
"s_nop 2\n"
"v_mov_b32 v127, 0\n"
"s_nop 2\n"
"v_mov_b32 v127, 0\n"
"s_nop 2\n"
"v_mov_b32 v127, 0\n"
"s_nop 2\n"
"v_mov_b32 v127, 0\n"
"L_chain_pollA:\n"
"s_waitcnt vmcnt(3)\n"
"v_mfma_f32_16x16x32_bf16 v[16:19], v[108:111], v[0:3], 0\n"
"v_bitop3_b32 v126, v0, v1, s33 bitop3:0x7e\n"
"v_bitop3_b32 v127, v2, v3, s33 bitop3:0x7e\n"
"s_waitcnt vmcnt(2)\n"
"v_mfma_f32_16x16x32_bf16 v[16:19], v[108:111], v[4:7], v[16:19]\n"
"v_bitop3_b32 v20, v4, v5, s33 bitop3:0x7e\n"
"v_bitop3_b32 v21, v6, v7, s33 bitop3:0x7e\n"
"v_or3_b32 v126, v126, v127, v20\n"
"v_bitop3_b32 v126, v126, v21, 1 bitop3:0xa8\n"
"v_cmp_ne_u32 vcc, 0, v126\n"
"s_nop 2\n"
"v_add_f32_dpp v16, v16, v16 row_ror:4 row_mask:0xf bank_mask:0xf\n"
"v_add_f32_dpp v17, v17, v17 row_ror:4 row_mask:0xf bank_mask:0xf\n"
"s_cbranch_vccnz L_chain_failA\n"
"L_chain_reduced:\n"
"v_add_f32_dpp v16, v16, v16 row_ror:8 row_mask:0xf bank_mask:0xf\n"
"v_add_f32_dpp v17, v17, v17 row_ror:8 row_mask:0xf bank_mask:0xf\n"
"s_mov_b64 exec, s[54:55]\n"
"v_cvt_pk_bf16_f32 v126, v16, v17\n"
"v_min_u32 v126, -2, v126\n"
"buffer_store_dword v126, v116, s[8:11], s38 offen sc1\n"
"s_mov_b64 exec, -1\n"
"s_add_u32 s38, s38, 0x4000\n"
"s_add_u32 s32, s32, 1\n"
"s_xor_b32 s33, s33, 1\n"
"s_add_u32 s50, s32, 1\n"
"s_min_u32 s50, s50, 63\n"
"s_cmp_gt_i32 s48, s50\n"
"s_cbranch_scc1 Lkvok_5\n"
"Lkvlp_6:\n"
"buffer_load_dwordx2 v[126:127], off, s[24:27], s51 sc1\n"
"s_waitcnt vmcnt(0)\n"
"v_min_i32 v126, v126, v127\n"
"s_nop 0\n"
"v_readfirstlane_b32 s48, v126\n"
"s_sub_u32 s47, s47, 1\n"
"s_cbranch_scc1 L_chain_fail\n"
"s_cmp_gt_i32 s48, s50\n"
"s_cbranch_scc0 Lkvlp_6\n"
"Lkvok_5:\n"
"s_barrier\n"
"s_mov_b32 s49, s46\n"
"s_cmp_eq_u32 s49, 0\n"
"s_cbranch_scc1 Ldd_8\n"
".p2align 4\n"
"Ldl_7:\n"
"s_sub_u32 s49, s49, 1\n"
"s_cmp_lg_u32 s49, 0\n"
"s_cbranch_scc1 Ldl_7\n"
"Ldd_8:\n"
"s_branch L_chain_top\n"
"L_chain_failA:\n"
"s_add_u32 s58, s58, 1\n"
"s_sub_u32 s47, s47, 1\n"
"s_cbranch_scc1 L_chain_fail\n"
"buffer_load_dwordx4 v[0:3], v115, s[8:11], s36 offen sc1\n"
"buffer_load_dwordx4 v[4:7], v115, s[8:11], s37 offen sc1\n"
"s_waitcnt vmcnt(3)\n"
"v_mfma_f32_16x16x32_bf16 v[16:19], v[108:111], v[8:11], 0\n"
"v_bitop3_b32 v126, v8, v9, s33 bitop3:0x7e\n"
"v_bitop3_b32 v127, v10, v11, s33 bitop3:0x7e\n"
"s_waitcnt vmcnt(2)\n"
"v_mfma_f32_16x16x32_bf16 v[16:19], v[108:111], v[12:15], v[16:19]\n"
"v_bitop3_b32 v20, v12, v13, s33 bitop3:0x7e\n"
"v_bitop3_b32 v21, v14, v15, s33 bitop3:0x7e\n"
"v_or3_b32 v126, v126, v127, v20\n"
"v_bitop3_b32 v126, v126, v21, 1 bitop3:0xa8\n"
"v_cmp_ne_u32 vcc, 0, v126\n"
"s_nop 2\n"
"v_add_f32_dpp v16, v16, v16 row_ror:4 row_mask:0xf bank_mask:0xf\n"
"v_add_f32_dpp v17, v17, v17 row_ror:4 row_mask:0xf bank_mask:0xf\n"
"s_cbranch_vccz L_chain_reduced\n"
"s_add_u32 s58, s58, 1\n"
"buffer_load_dwordx4 v[8:11], v115, s[8:11], s36 offen sc1\n"
"buffer_load_dwordx4 v[12:15], v115, s[8:11], s37 offen sc1\n"
"s_branch L_chain_pollA\n"
"L_chain_last:\n"
"s_lshl_b32 s44, s32, 17\n"
"v_smfmac_f32_16x16x64_bf16 v[80:83], v[20:23], v[168:175], v125\n"
"v_smfmac_f32_16x16x64_bf16 v[84:87], v[20:23], v[232:239], v125\n"
"v_smfmac_f32_16x16x64_bf16 v[80:83], v[24:27], v[176:183], v125\n"
"v_smfmac_f32_16x16x64_bf16 v[84:87], v[24:27], v[240:247], v125\n"
"v_smfmac_f32_16x16x64_bf16 v[80:83], v[28:31], v[184:191], v125\n"
"v_smfmac_f32_16x16x64_bf16 v[84:87], v[28:31], v[248:255], v125\n"
"buffer_store_dwordx2 v[106:107], v123, s[20:23], s44 offen sc1\n"
"s_nop 7\n"
"v_add_f32 v80, v80, v82\n"
"v_add_f32 v81, v81, v83\n"
"v_add_f32 v84, v84, v86\n"
"v_add_f32 v85, v85, v87\n"
"v_cvt_pk_bf16_f32 v100, v80, v81\n"
"v_cvt_pk_bf16_f32 v101, v84, v85\n"
"buffer_store_dwordx2 v[100:101], v124, s[20:23], s44 offen sc1\n"
"s_branch L_chain_done\n"
"L_chain_retry2:\n"
"v_max_u32 v126, v0, v1\n"
"v_max3_u32 v126, v2, v3, v126\n"
"v_max3_u32 v126, v4, v5, v126\n"
"v_max3_u32 v126, v6, v7, v126\n"
"v_max3_u32 v126, v8, v9, v126\n"
"v_max3_u32 v126, v10, v11, v126\n"
"v_max3_u32 v126, v12, v13, v126\n"
"v_max3_u32 v126, v14, v15, v126\n"
"v_max3_u32 v126, v16, v17, v126\n"
"v_max3_u32 v126, v18, v19, v126\n"
"v_max3_u32 v126, v20, v21, v126\n"
"v_max3_u32 v126, v22, v23, v126\n"
"v_max3_u32 v126, v24, v25, v126\n"
"v_max3_u32 v126, v26, v27, v126\n"
"v_max3_u32 v126, v28, v29, v126\n"
"v_max3_u32 v126, v30, v31, v126\n"
"v_cmp_eq_u32 vcc, -1, v126\n"
"s_cbranch_vccz L_chain_cont2\n"
"s_add_u32 s59, s59, 1\n"
"s_sub_u32 s47, s47, 1\n"
"s_cbranch_scc1 L_chain_fail\n"
"v_mov_b64 v[64:65], 0\n"
"v_mov_b64 v[66:67], 0\n"
"v_mov_b64 v[68:69], 0\n"
"v_mov_b64 v[70:71], 0\n"
"v_mov_b64 v[72:73], 0\n"
"v_mov_b64 v[74:75], 0\n"
"v_mov_b64 v[76:77], 0\n"
"v_mov_b64 v[78:79], 0\n"
"v_mov_b64 v[80:81], 0\n"
"v_mov_b64 v[82:83], 0\n"
"v_mov_b64 v[84:85], 0\n"
"v_mov_b64 v[86:87], 0\n"
"s_mov_b32 s44, 0x7fff0000\n"
"s_branch L_chain_regather\n"
"L_chain_fail:\n"
"s_mov_b64 exec, -1\n"
"v_mov_b32 v126, s32\n"
"s_mov_b32 s3, 0x2080\n"
"buffer_store_dword v126, off, s[24:27], s3 sc1\n"
"L_chain_done:\n"
"s_waitcnt vmcnt(0)\n"
"v_mov_b32 v127, 64\n"
"s_mov_b64 exec, 1\n"
"buffer_store_dword v127, off, s[24:27], s57 sc1\n"
"s_endpgm\n"
"L_bulk:\n"
"s_mul_i32 s3, s2, 256\n"
"s_add_u32 s6, s6, s3\n"
"s_addc_u32 s7, s7, 0\n"
"s_load_dwordx4 s[72:75], s[0:1], 104\n"
"v_lshrrev_b32 v1, 6, v0\n"
"v_and_b32 v255, 63, v0\n"
"s_nop 0\n"
"v_readfirstlane_b32 s5, v1\n"
"s_mov_b32 s8, 0x80000\n"
"s_waitcnt lgkmcnt(0)\n"
"s_mov_b32 s10, s72\n"
"s_mov_b32 s11, s73\n"
"s_add_u32 s12, s74, 0x180000\n"
"s_addc_u32 s13, s75, 0\n"
"s_and_b32 s13, s13, 0xffff\n"
"s_mov_b32 s14, 0x1c000\n"
"s_mov_b32 s15, 0x20000\n"
"L_next:\n"
"s_load_dwordx8 s[64:71], s[6:7], 0\n"
"s_add_u32 s6, s6, 32\n"
"s_addc_u32 s7, s7, 0\n"
"s_waitcnt lgkmcnt(0)\n"
"s_cmp_eq_u32 s64, 1\n"
"s_cbranch_scc1 L_pqk\n"
"s_cmp_eq_u32 s64, 2\n"
"s_cbranch_scc1 L_pv\n"
"s_cmp_eq_u32 s64, 3\n"
"s_cbranch_scc1 L_att\n"
"s_cmp_eq_u32 s64, 4\n"
"s_cbranch_scc1 L_oe\n"
"s_cmp_eq_u32 s64, 5\n"
"s_cbranch_scc1 L_ax\n"
"s_cmp_eq_u32 s64, 6\n"
"s_cbranch_scc1 L_cal\n"
"s_endpgm\n"
"L_bulk_fail:\n"
"s_mov_b64 exec, -1\n"
"v_mov_b32 v0, s64\n"
"v_add_u32 v0, 0x100, v0\n"
"s_mov_b32 s3, 0x2080\n"
"buffer_store_dword v0, off, s[12:15], s3 sc1\n"
"s_endpgm\n"
"L_pqk:\n"
"s_load_dwordx2 s[40:41], s[0:1], s65\n"
"s_load_dwordx2 s[42:43], s[0:1], s66\n"
"s_and_b32 s3, s5, 1\n"
"s_lshl_b32 s3, s3, 7\n"
"v_lshlrev_b32 v136, 4, v255\n"
"v_xor_b32 v128, s3, v136\n"
"v_xor_b32 v129, 16, v128\n"
"v_xor_b32 v130, 32, v128\n"
"v_xor_b32 v131, 48, v128\n"
"v_xor_b32 v132, 64, v128\n"
"v_xor_b32 v133, 80, v128\n"
"v_xor_b32 v134, 96, v128\n"
"v_xor_b32 v135, 112, v128\n"
"s_lshl_b32 s35, s5, 13\n"
"s_add_u32 s29, s69, s35\n"
"s_waitcnt lgkmcnt(0)\n"
"s_add_u32 s20, s42, 0x0\n"
"s_addc_u32 s21, s43, 0\n"
"s_and_b32 s21, s21, 0xffff\n"
"s_mov_b32 s22, 0x200000\n"
"s_mov_b32 s23, 0x20000\n"
"s_add_u32 s16, s40, 0x0\n"
"s_addc_u32 s17, s41, 0\n"
"s_add_u32 s16, s16, s68\n"
"s_addc_u32 s17, s17, 0\n"
"s_and_b32 s17, s17, 0xffff\n"
"s_mov_b32 s18, 0x400000\n"
"s_mov_b32 s19, 0x20000\n"
"s_sub_u32 s18, s18, s68\n"
"s_barrier\n"
"s_add_u32 m0, s35, 0x20000\n"
"s_add_u32 s36, s29, 0x0\n"
"buffer_load_dwordx4 v128, s[20:23], s36 offen offset:0 lds\n"
"buffer_load_dwordx4 v129, s[20:23], s36 offen offset:1024 lds\n"
"buffer_load_dwordx4 v130, s[20:23], s36 offen offset:2048 lds\n"
"buffer_load_dwordx4 v131, s[20:23], s36 offen offset:3072 lds\n"
"s_add_u32 m0, s35, 0x21000\n"
"s_add_u32 s36, s29, 0x1000\n"
"buffer_load_dwordx4 v132, s[20:23], s36 offen offset:0 lds\n"
"buffer_load_dwordx4 v133, s[20:23], s36 offen offset:1024 lds\n"
"buffer_load_dwordx4 v134, s[20:23], s36 offen offset:2048 lds\n"
"buffer_load_dwordx4 v135, s[20:23], s36 offen offset:3072 lds\n"
"s_add_u32 s29, s29, 0x8000\n"
"s_add_u32 m0, s35, 0x18000\n"
"s_add_u32 s36, s29, 0x0\n"
"buffer_load_dwordx4 v128, s[20:23], s36 offen offset:0 lds\n"
"buffer_load_dwordx4 v129, s[20:23], s36 offen offset:1024 lds\n"
"buffer_load_dwordx4 v130, s[20:23], s36 offen offset:2048 lds\n"
"buffer_load_dwordx4 v131, s[20:23], s36 offen offset:3072 lds\n"
"s_add_u32 m0, s35, 0x19000\n"
"s_add_u32 s36, s29, 0x1000\n"
"buffer_load_dwordx4 v132, s[20:23], s36 offen offset:0 lds\n"
"buffer_load_dwordx4 v133, s[20:23], s36 offen offset:1024 lds\n"
"buffer_load_dwordx4 v134, s[20:23], s36 offen offset:2048 lds\n"
"buffer_load_dwordx4 v135, s[20:23], s36 offen offset:3072 lds\n"
"s_add_u32 s29, s29, 0x8000\n"
"v_mov_b32 v0, 0x0\n"
"v_bfe_i32 v4, v255, 0, 1\n"
"v_and_b32 v4, 0x10, v4\n"
"v_xor_b32 v0, v0, v4\n"
"v_bfe_i32 v4, v255, 1, 1\n"
"v_and_b32 v4, 0x20, v4\n"
"v_xor_b32 v0, v0, v4\n"
"v_bfe_i32 v4, v255, 2, 1\n"
"v_and_b32 v4, 0x40, v4\n"
"v_xor_b32 v0, v0, v4\n"
"v_bfe_i32 v4, v255, 3, 1\n"
"v_and_b32 v4, 0x80, v4\n"
"v_xor_b32 v0, v0, v4\n"
"v_bfe_i32 v4, v255, 4, 1\n"
"v_and_b32 v4, 0x100, v4\n"
"v_xor_b32 v0, v0, v4\n"
"v_bfe_i32 v4, v255, 5, 1\n"
"v_and_b32 v4, 0x2040, v4\n"
"v_xor_b32 v0, v0, v4\n"
"s_lshl_b32 s3, s5, 16\n"
"v_add_u32 v0, s3, v0\n"
"v_xor_b32 v1, 0x80, v0\n"
"s_lshl_b32 s37, s5, 12\n"
"s_add_u32 m0, s37, 0x0\n"
"s_mov_b32 s3, 0x0\n"
"buffer_load_dwordx4 v0, s[16:19], s3 offen lds\n"
"s_add_u32 m0, s37, 0x400\n"
"s_mov_b32 s3, 0x4000\n"
"buffer_load_dwordx4 v1, s[16:19], s3 offen lds\n"
"s_add_u32 m0, s37, 0x800\n"
"s_mov_b32 s3, 0x8000\n"
"buffer_load_dwordx4 v0, s[16:19], s3 offen lds\n"
"s_add_u32 m0, s37, 0xc00\n"
"s_mov_b32 s3, 0xc000\n"
"buffer_load_dwordx4 v1, s[16:19], s3 offen lds\n"
"s_add_u32 m0, s37, 0x4000\n"
"s_mov_b32 s3, 0x40000\n"
"buffer_load_dwordx4 v0, s[16:19], s3 offen lds\n"
"s_add_u32 m0, s37, 0x4400\n"
"s_mov_b32 s3, 0x44000\n"
"buffer_load_dwordx4 v1, s[16:19], s3 offen lds\n"
"s_add_u32 m0, s37, 0x4800\n"
"s_mov_b32 s3, 0x48000\n"
"buffer_load_dwordx4 v0, s[16:19], s3 offen lds\n"
"s_add_u32 m0, s37, 0x4c00\n"
"s_mov_b32 s3, 0x4c000\n"
"buffer_load_dwordx4 v1, s[16:19], s3 offen lds\n"
"s_add_u32 m0, s37, 0x8000\n"
"s_mov_b32 s3, 0x80000\n"
"buffer_load_dwordx4 v0, s[16:19], s3 offen lds\n"
"s_add_u32 m0, s37, 0x8400\n"
"s_mov_b32 s3, 0x84000\n"
"buffer_load_dwordx4 v1, s[16:19], s3 offen lds\n"
"s_add_u32 m0, s37, 0x8800\n"
"s_mov_b32 s3, 0x88000\n"
"buffer_load_dwordx4 v0, s[16:19], s3 offen lds\n"
"s_add_u32 m0, s37, 0x8c00\n"
"s_mov_b32 s3, 0x8c000\n"
"buffer_load_dwordx4 v1, s[16:19], s3 offen lds\n"
"s_add_u32 m0, s37, 0xc000\n"
"s_mov_b32 s3, 0xc0000\n"
"buffer_load_dwordx4 v0, s[16:19], s3 offen lds\n"
"s_add_u32 m0, s37, 0xc400\n"
"s_mov_b32 s3, 0xc4000\n"
"buffer_load_dwordx4 v1, s[16:19], s3 offen lds\n"
"s_add_u32 m0, s37, 0xc800\n"
"s_mov_b32 s3, 0xc8000\n"
"buffer_load_dwordx4 v0, s[16:19], s3 offen lds\n"
"s_add_u32 m0, s37, 0xcc00\n"
"s_mov_b32 s3, 0xcc000\n"
"buffer_load_dwordx4 v1, s[16:19], s3 offen lds\n"
"s_add_u32 m0, s37, 0x10000\n"
"s_mov_b32 s3, 0x100000\n"
"buffer_load_dwordx4 v0, s[16:19], s3 offen lds\n"
"s_add_u32 m0, s37, 0x10400\n"
"s_mov_b32 s3, 0x104000\n"
"buffer_load_dwordx4 v1, s[16:19], s3 offen lds\n"
"s_add_u32 m0, s37, 0x10800\n"
"s_mov_b32 s3, 0x108000\n"
"buffer_load_dwordx4 v0, s[16:19], s3 offen lds\n"
"s_add_u32 m0, s37, 0x10c00\n"
"s_mov_b32 s3, 0x10c000\n"
"buffer_load_dwordx4 v1, s[16:19], s3 offen lds\n"
"v_mov_b32 v2, 0x0\n"
"v_bfe_i32 v4, v255, 0, 1\n"
"v_and_b32 v4, 0x8, v4\n"
"v_add_u32 v2, v2, v4\n"
"v_bfe_i32 v4, v255, 1, 1\n"
"v_and_b32 v4, 0x10, v4\n"
"v_add_u32 v2, v2, v4\n"
"v_bfe_i32 v4, v255, 2, 1\n"
"v_and_b32 v4, 0x240, v4\n"
"v_add_u32 v2, v2, v4\n"
"v_bfe_i32 v4, v255, 3, 1\n"
"v_and_b32 v4, 0x480, v4\n"
"v_add_u32 v2, v2, v4\n"
"v_bfe_i32 v4, v255, 4, 1\n"
"v_and_b32 v4, 0x20, v4\n"
"v_add_u32 v2, v2, v4\n"
"v_bfe_i32 v4, v255, 5, 1\n"
"v_and_b32 v4, 0x1000, v4\n"
"v_add_u32 v2, v2, v4\n"
"s_lshl_b32 s3, s5, 7\n"
"v_xor_b32 v2, s3, v2\n"
"v_xor_b32 v3, 64, v2\n"
"v_add_u32 v5, 0x10000, v2\n"
"v_add_u32 v6, 0x10000, v3\n"
"v_mov_b32 v112, 0x8000\n"
"v_bfe_i32 v4, v255, 0, 1\n"
"v_and_b32 v4, 0x410, v4\n"
"v_xor_b32 v112, v112, v4\n"
"v_bfe_i32 v4, v255, 1, 1\n"
"v_and_b32 v4, 0x820, v4\n"
"v_xor_b32 v112, v112, v4\n"
"v_bfe_i32 v4, v255, 2, 1\n"
"v_and_b32 v4, 0x1040, v4\n"
"v_xor_b32 v112, v112, v4\n"
"v_bfe_i32 v4, v255, 3, 1\n"
"v_and_b32 v4, 0x2080, v4\n"
"v_xor_b32 v112, v112, v4\n"
"v_bfe_i32 v4, v255, 4, 1\n"
"v_and_b32 v4, 0x4000, v4\n"
"v_xor_b32 v112, v112, v4\n"
"v_bfe_i32 v4, v255, 5, 1\n"
"v_and_b32 v4, 0x10, v4\n"
"v_xor_b32 v112, v112, v4\n"
"v_xor_b32 v113, 32, v112\n"
"v_xor_b32 v114, 64, v112\n"
"v_xor_b32 v115, 96, v112\n"
"v_xor_b32 v116, 128, v112\n"
"v_xor_b32 v117, 160, v112\n"
"v_xor_b32 v118, 192, v112\n"
"v_xor_b32 v119, 224, v112\n"
"v_add_u32 v120, 0x10000, v112\n"
"v_add_u32 v121, 0x10000, v113\n"
"v_add_u32 v122, 0x10000, v114\n"
"v_add_u32 v123, 0x10000, v115\n"
"v_add_u32 v124, 0x10000, v116\n"
"v_add_u32 v125, 0x10000, v117\n"
"v_add_u32 v126, 0x10000, v118\n"
"v_add_u32 v127, 0x10000, v119\n"
"s_lshr_b32 s3, s5, 1\n"
"s_lshl_b32 s3, s3, 19\n"
"s_and_b32 s36, s5, 1\n"
"s_lshl_b32 s36, s36, 12\n"
"s_add_u32 s3, s3, s36\n"
"s_add_u32 s38, s3, s67\n"
"s_add_u32 s24, s10, 0x0\n"
"s_addc_u32 s25, s11, 0\n"
"s_add_u32 s24, s24, s38\n"
"s_addc_u32 s25, s25, 0\n"
"s_and_b32 s25, s25, 0xffff\n"
"s_mov_b32 s26, 0x7ff00000\n"
"s_mov_b32 s27, 0x20000\n"
"s_lshl_b32 s3, s70, 13\n"
"s_sub_u32 s26, s3, s36\n"
"s_mov_b32 s28, 0xffffe000\n"
"s_lshr_b32 s30, s70, 2\n"
"s_and_b32 s31, s71, 0xffff\n"
"s_mov_b32 s34, 0\n"
"s_cmp_eq_u32 s5, 0\n"
"s_cselect_b32 s31, s31, 0x7fff0000\n"
"s_mov_b32 s33, 1.0\n"
"s_bitcmp1_b32 s71, 31\n"
"s_cselect_b32 s33, 0x3e0293ee, s33\n"
"s_waitcnt vmcnt(16) lgkmcnt(0)\n"
"s_barrier\n"
"s_add_u32 m0, s37, 0x14000\n"
"s_mov_b32 s3, 0x140000\n"
"buffer_load_dwordx4 v0, s[16:19], s3 offen lds\n"
"s_add_u32 m0, s37, 0x14400\n"
"s_mov_b32 s3, 0x144000\n"
"buffer_load_dwordx4 v1, s[16:19], s3 offen lds\n"
"s_add_u32 m0, s37, 0x14800\n"
"s_mov_b32 s3, 0x148000\n"
"buffer_load_dwordx4 v0, s[16:19], s3 offen lds\n"
"s_add_u32 m0, s37, 0x14c00\n"
"s_mov_b32 s3, 0x14c000\n"
"buffer_load_dwordx4 v1, s[16:19], s3 offen lds\n"
"ds_read_b64_tr_b16 a[0:1], v2 offset:0\n"
"ds_read_b64_tr_b16 a[2:3], v2 offset:2048\n"
"ds_read_b64_tr_b16 a[128:129], v3 offset:0\n"
"ds_read_b64_tr_b16 a[130:131], v3 offset:2048\n"
"ds_read_b64_tr_b16 a[4:5], v2 offset:8192\n"
"ds_read_b64_tr_b16 a[6:7], v2 offset:10240\n"
"ds_read_b64_tr_b16 a[132:133], v3 offset:8192\n"
"ds_read_b64_tr_b16 a[134:135], v3 offset:10240\n"
"s_waitcnt vmcnt(16) lgkmcnt(0)\n"
"s_barrier\n"
"s_add_u32 m0, s37, 0x0\n"
"s_mov_b32 s3, 0x180000\n"
"buffer_load_dwordx4 v0, s[16:19], s3 offen lds\n"
"s_add_u32 m0, s37, 0x400\n"
"s_mov_b32 s3, 0x184000\n"
"buffer_load_dwordx4 v1, s[16:19], s3 offen lds\n"
"s_add_u32 m0, s37, 0x800\n"
"s_mov_b32 s3, 0x188000\n"
"buffer_load_dwordx4 v0, s[16:19], s3 offen lds\n"
"s_add_u32 m0, s37, 0xc00\n"
"s_mov_b32 s3, 0x18c000\n"
"buffer_load_dwordx4 v1, s[16:19], s3 offen lds\n"
"ds_read_b64_tr_b16 a[8:9], v2 offset:16384\n"
"ds_read_b64_tr_b16 a[10:11], v2 offset:18432\n"
"ds_read_b64_tr_b16 a[136:137], v3 offset:16384\n"
"ds_read_b64_tr_b16 a[138:139], v3 offset:18432\n"
"ds_read_b64_tr_b16 a[12:13], v2 offset:24576\n"
"ds_read_b64_tr_b16 a[14:15], v2 offset:26624\n"
"ds_read_b64_tr_b16 a[140:141], v3 offset:24576\n"
"ds_read_b64_tr_b16 a[142:143], v3 offset:26624\n"
"s_waitcnt vmcnt(16) lgkmcnt(0)\n"
"s_barrier\n"
"s_add_u32 m0, s37, 0x4000\n"
"s_mov_b32 s3, 0x1c0000\n"
"buffer_load_dwordx4 v0, s[16:19], s3 offen lds\n"
"s_add_u32 m0, s37, 0x4400\n"
"s_mov_b32 s3, 0x1c4000\n"
"buffer_load_dwordx4 v1, s[16:19], s3 offen lds\n"
"s_add_u32 m0, s37, 0x4800\n"
"s_mov_b32 s3, 0x1c8000\n"
"buffer_load_dwordx4 v0, s[16:19], s3 offen lds\n"
"s_add_u32 m0, s37, 0x4c00\n"
"s_mov_b32 s3, 0x1cc000\n"
"buffer_load_dwordx4 v1, s[16:19], s3 offen lds\n"
"ds_read_b64_tr_b16 a[16:17], v2 offset:32768\n"
"ds_read_b64_tr_b16 a[18:19], v2 offset:34816\n"
"ds_read_b64_tr_b16 a[144:145], v3 offset:32768\n"
"ds_read_b64_tr_b16 a[146:147], v3 offset:34816\n"
"ds_read_b64_tr_b16 a[20:21], v2 offset:40960\n"
"ds_read_b64_tr_b16 a[22:23], v2 offset:43008\n"
"ds_read_b64_tr_b16 a[148:149], v3 offset:40960\n"
"ds_read_b64_tr_b16 a[150:151], v3 offset:43008\n"
"s_waitcnt vmcnt(16) lgkmcnt(0)\n"
"s_barrier\n"
"s_add_u32 m0, s37, 0x8000\n"
"s_mov_b32 s3, 0x200000\n"
"buffer_load_dwordx4 v0, s[16:19], s3 offen lds\n"
"s_add_u32 m0, s37, 0x8400\n"
"s_mov_b32 s3, 0x204000\n"
"buffer_load_dwordx4 v1, s[16:19], s3 offen lds\n"
"s_add_u32 m0, s37, 0x8800\n"
"s_mov_b32 s3, 0x208000\n"
"buffer_load_dwordx4 v0, s[16:19], s3 offen lds\n"
"s_add_u32 m0, s37, 0x8c00\n"
"s_mov_b32 s3, 0x20c000\n"
"buffer_load_dwordx4 v1, s[16:19], s3 offen lds\n"
"ds_read_b64_tr_b16 a[24:25], v2 offset:49152\n"
"ds_read_b64_tr_b16 a[26:27], v2 offset:51200\n"
"ds_read_b64_tr_b16 a[152:153], v3 offset:49152\n"
"ds_read_b64_tr_b16 a[154:155], v3 offset:51200\n"
"ds_read_b64_tr_b16 a[28:29], v2 offset:57344\n"
"ds_read_b64_tr_b16 a[30:31], v2 offset:59392\n"
"ds_read_b64_tr_b16 a[156:157], v3 offset:57344\n"
"ds_read_b64_tr_b16 a[158:159], v3 offset:59392\n"
"s_waitcnt vmcnt(16) lgkmcnt(0)\n"
"s_barrier\n"
"s_add_u32 m0, s37, 0xc000\n"
"s_mov_b32 s3, 0x240000\n"
"buffer_load_dwordx4 v0, s[16:19], s3 offen lds\n"
"s_add_u32 m0, s37, 0xc400\n"
"s_mov_b32 s3, 0x244000\n"
"buffer_load_dwordx4 v1, s[16:19], s3 offen lds\n"
"s_add_u32 m0, s37, 0xc800\n"
"s_mov_b32 s3, 0x248000\n"
"buffer_load_dwordx4 v0, s[16:19], s3 offen lds\n"
"s_add_u32 m0, s37, 0xcc00\n"
"s_mov_b32 s3, 0x24c000\n"
"buffer_load_dwordx4 v1, s[16:19], s3 offen lds\n"
"ds_read_b64_tr_b16 a[32:33], v5 offset:0\n"
"ds_read_b64_tr_b16 a[34:35], v5 offset:2048\n"
"ds_read_b64_tr_b16 a[160:161], v6 offset:0\n"
"ds_read_b64_tr_b16 a[162:163], v6 offset:2048\n"
"ds_read_b64_tr_b16 a[36:37], v5 offset:8192\n"
"ds_read_b64_tr_b16 a[38:39], v5 offset:10240\n"
"ds_read_b64_tr_b16 a[164:165], v6 offset:8192\n"
"ds_read_b64_tr_b16 a[166:167], v6 offset:10240\n"
"s_waitcnt vmcnt(16) lgkmcnt(0)\n"
"s_barrier\n"
"s_add_u32 m0, s37, 0x10000\n"
"s_mov_b32 s3, 0x280000\n"
"buffer_load_dwordx4 v0, s[16:19], s3 offen lds\n"
"s_add_u32 m0, s37, 0x10400\n"
"s_mov_b32 s3, 0x284000\n"
"buffer_load_dwordx4 v1, s[16:19], s3 offen lds\n"
"s_add_u32 m0, s37, 0x10800\n"
"s_mov_b32 s3, 0x288000\n"
"buffer_load_dwordx4 v0, s[16:19], s3 offen lds\n"
"s_add_u32 m0, s37, 0x10c00\n"
"s_mov_b32 s3, 0x28c000\n"
"buffer_load_dwordx4 v1, s[16:19], s3 offen lds\n"
"ds_read_b64_tr_b16 a[40:41], v5 offset:16384\n"
"ds_read_b64_tr_b16 a[42:43], v5 offset:18432\n"
"ds_read_b64_tr_b16 a[168:169], v6 offset:16384\n"
"ds_read_b64_tr_b16 a[170:171], v6 offset:18432\n"
"ds_read_b64_tr_b16 a[44:45], v5 offset:24576\n"
"ds_read_b64_tr_b16 a[46:47], v5 offset:26624\n"
"ds_read_b64_tr_b16 a[172:173], v6 offset:24576\n"
"ds_read_b64_tr_b16 a[174:175], v6 offset:26624\n"
"s_waitcnt vmcnt(16) lgkmcnt(0)\n"
"s_barrier\n"
"s_add_u32 m0, s37, 0x14000\n"
"s_mov_b32 s3, 0x2c0000\n"
"buffer_load_dwordx4 v0, s[16:19], s3 offen lds\n"
"s_add_u32 m0, s37, 0x14400\n"
"s_mov_b32 s3, 0x2c4000\n"
"buffer_load_dwordx4 v1, s[16:19], s3 offen lds\n"
"s_add_u32 m0, s37, 0x14800\n"
"s_mov_b32 s3, 0x2c8000\n"
"buffer_load_dwordx4 v0, s[16:19], s3 offen lds\n"
"s_add_u32 m0, s37, 0x14c00\n"
"s_mov_b32 s3, 0x2cc000\n"
"buffer_load_dwordx4 v1, s[16:19], s3 offen lds\n"
"ds_read_b64_tr_b16 a[48:49], v2 offset:0\n"
"ds_read_b64_tr_b16 a[50:51], v2 offset:2048\n"
"ds_read_b64_tr_b16 a[176:177], v3 offset:0\n"
"ds_read_b64_tr_b16 a[178:179], v3 offset:2048\n"
"ds_read_b64_tr_b16 a[52:53], v2 offset:8192\n"
"ds_read_b64_tr_b16 a[54:55], v2 offset:10240\n"
"ds_read_b64_tr_b16 a[180:181], v3 offset:8192\n"
"ds_read_b64_tr_b16 a[182:183], v3 offset:10240\n"
"s_waitcnt vmcnt(16) lgkmcnt(0)\n"
"s_barrier\n"
"s_add_u32 m0, s37, 0x0\n"
"s_mov_b32 s3, 0x300000\n"
"buffer_load_dwordx4 v0, s[16:19], s3 offen lds\n"
"s_add_u32 m0, s37, 0x400\n"
"s_mov_b32 s3, 0x304000\n"
"buffer_load_dwordx4 v1, s[16:19], s3 offen lds\n"
"s_add_u32 m0, s37, 0x800\n"
"s_mov_b32 s3, 0x308000\n"
"buffer_load_dwordx4 v0, s[16:19], s3 offen lds\n"
"s_add_u32 m0, s37, 0xc00\n"
"s_mov_b32 s3, 0x30c000\n"
"buffer_load_dwordx4 v1, s[16:19], s3 offen lds\n"
"ds_read_b64_tr_b16 a[56:57], v2 offset:16384\n"
"ds_read_b64_tr_b16 a[58:59], v2 offset:18432\n"
"ds_read_b64_tr_b16 a[184:185], v3 offset:16384\n"
"ds_read_b64_tr_b16 a[186:187], v3 offset:18432\n"
"ds_read_b64_tr_b16 a[60:61], v2 offset:24576\n"
"ds_read_b64_tr_b16 a[62:63], v2 offset:26624\n"
"ds_read_b64_tr_b16 a[188:189], v3 offset:24576\n"
"ds_read_b64_tr_b16 a[190:191], v3 offset:26624\n"
"s_waitcnt vmcnt(16) lgkmcnt(0)\n"
"s_barrier\n"
"s_add_u32 m0, s37, 0x4000\n"
"s_mov_b32 s3, 0x340000\n"
"buffer_load_dwordx4 v0, s[16:19], s3 offen lds\n"
"s_add_u32 m0, s37, 0x4400\n"
"s_mov_b32 s3, 0x344000\n"
"buffer_load_dwordx4 v1, s[16:19], s3 offen lds\n"
"s_add_u32 m0, s37, 0x4800\n"
"s_mov_b32 s3, 0x348000\n"
"buffer_load_dwordx4 v0, s[16:19], s3 offen lds\n"
"s_add_u32 m0, s37, 0x4c00\n"
"s_mov_b32 s3, 0x34c000\n"
"buffer_load_dwordx4 v1, s[16:19], s3 offen lds\n"
"ds_read_b64_tr_b16 a[64:65], v2 offset:32768\n"
"ds_read_b64_tr_b16 a[66:67], v2 offset:34816\n"
"ds_read_b64_tr_b16 a[192:193], v3 offset:32768\n"
"ds_read_b64_tr_b16 a[194:195], v3 offset:34816\n"
"ds_read_b64_tr_b16 a[68:69], v2 offset:40960\n"
"ds_read_b64_tr_b16 a[70:71], v2 offset:43008\n"
"ds_read_b64_tr_b16 a[196:197], v3 offset:40960\n"
"ds_read_b64_tr_b16 a[198:199], v3 offset:43008\n"
"s_waitcnt vmcnt(16) lgkmcnt(0)\n"
"s_barrier\n"
"s_add_u32 m0, s37, 0x8000\n"
"s_mov_b32 s3, 0x380000\n"
"buffer_load_dwordx4 v0, s[16:19], s3 offen lds\n"
"s_add_u32 m0, s37, 0x8400\n"
"s_mov_b32 s3, 0x384000\n"
"buffer_load_dwordx4 v1, s[16:19], s3 offen lds\n"
"s_add_u32 m0, s37, 0x8800\n"
"s_mov_b32 s3, 0x388000\n"
"buffer_load_dwordx4 v0, s[16:19], s3 offen lds\n"
"s_add_u32 m0, s37, 0x8c00\n"
"s_mov_b32 s3, 0x38c000\n"
"buffer_load_dwordx4 v1, s[16:19], s3 offen lds\n"
"ds_read_b64_tr_b16 a[72:73], v2 offset:49152\n"
"ds_read_b64_tr_b16 a[74:75], v2 offset:51200\n"
"ds_read_b64_tr_b16 a[200:201], v3 offset:49152\n"
"ds_read_b64_tr_b16 a[202:203], v3 offset:51200\n"
"ds_read_b64_tr_b16 a[76:77], v2 offset:57344\n"
"ds_read_b64_tr_b16 a[78:79], v2 offset:59392\n"
"ds_read_b64_tr_b16 a[204:205], v3 offset:57344\n"
"ds_read_b64_tr_b16 a[206:207], v3 offset:59392\n"
"s_waitcnt vmcnt(16) lgkmcnt(0)\n"
"s_barrier\n"
"s_add_u32 m0, s37, 0xc000\n"
"s_mov_b32 s3, 0x3c0000\n"
"buffer_load_dwordx4 v0, s[16:19], s3 offen lds\n"
"s_add_u32 m0, s37, 0xc400\n"
"s_mov_b32 s3, 0x3c4000\n"
"buffer_load_dwordx4 v1, s[16:19], s3 offen lds\n"
"s_add_u32 m0, s37, 0xc800\n"
"s_mov_b32 s3, 0x3c8000\n"
"buffer_load_dwordx4 v0, s[16:19], s3 offen lds\n"
"s_add_u32 m0, s37, 0xcc00\n"
"s_mov_b32 s3, 0x3cc000\n"
"buffer_load_dwordx4 v1, s[16:19], s3 offen lds\n"
"ds_read_b64_tr_b16 a[80:81], v5 offset:0\n"
"ds_read_b64_tr_b16 a[82:83], v5 offset:2048\n"
"ds_read_b64_tr_b16 a[208:209], v6 offset:0\n"
"ds_read_b64_tr_b16 a[210:211], v6 offset:2048\n"
"ds_read_b64_tr_b16 a[84:85], v5 offset:8192\n"
"ds_read_b64_tr_b16 a[86:87], v5 offset:10240\n"
"ds_read_b64_tr_b16 a[212:213], v6 offset:8192\n"
"ds_read_b64_tr_b16 a[214:215], v6 offset:10240\n"
"s_waitcnt vmcnt(16) lgkmcnt(0)\n"
"s_barrier\n"
"ds_read_b64_tr_b16 a[88:89], v5 offset:16384\n"
"ds_read_b64_tr_b16 a[90:91], v5 offset:18432\n"
"ds_read_b64_tr_b16 a[216:217], v6 offset:16384\n"
"ds_read_b64_tr_b16 a[218:219], v6 offset:18432\n"
"ds_read_b64_tr_b16 a[92:93], v5 offset:24576\n"
"ds_read_b64_tr_b16 a[94:95], v5 offset:26624\n"
"ds_read_b64_tr_b16 a[220:221], v6 offset:24576\n"
"ds_read_b64_tr_b16 a[222:223], v6 offset:26624\n"
"s_waitcnt vmcnt(12) lgkmcnt(0)\n"
"s_barrier\n"
"ds_read_b64_tr_b16 a[96:97], v2 offset:0\n"
"ds_read_b64_tr_b16 a[98:99], v2 offset:2048\n"
"ds_read_b64_tr_b16 a[224:225], v3 offset:0\n"
"ds_read_b64_tr_b16 a[226:227], v3 offset:2048\n"
"ds_read_b64_tr_b16 a[100:101], v2 offset:8192\n"
"ds_read_b64_tr_b16 a[102:103], v2 offset:10240\n"
"ds_read_b64_tr_b16 a[228:229], v3 offset:8192\n"
"ds_read_b64_tr_b16 a[230:231], v3 offset:10240\n"
"s_waitcnt vmcnt(8) lgkmcnt(0)\n"
"s_barrier\n"
"ds_read_b64_tr_b16 a[104:105], v2 offset:16384\n"
"ds_read_b64_tr_b16 a[106:107], v2 offset:18432\n"
"ds_read_b64_tr_b16 a[232:233], v3 offset:16384\n"
"ds_read_b64_tr_b16 a[234:235], v3 offset:18432\n"
"ds_read_b64_tr_b16 a[108:109], v2 offset:24576\n"
"ds_read_b64_tr_b16 a[110:111], v2 offset:26624\n"
"ds_read_b64_tr_b16 a[236:237], v3 offset:24576\n"
"ds_read_b64_tr_b16 a[238:239], v3 offset:26624\n"
"s_waitcnt vmcnt(4) lgkmcnt(0)\n"
"s_barrier\n"
"ds_read_b64_tr_b16 a[112:113], v2 offset:32768\n"
"ds_read_b64_tr_b16 a[114:115], v2 offset:34816\n"
"ds_read_b64_tr_b16 a[240:241], v3 offset:32768\n"
"ds_read_b64_tr_b16 a[242:243], v3 offset:34816\n"
"ds_read_b64_tr_b16 a[116:117], v2 offset:40960\n"
"ds_read_b64_tr_b16 a[118:119], v2 offset:43008\n"
"ds_read_b64_tr_b16 a[244:245], v3 offset:40960\n"
"ds_read_b64_tr_b16 a[246:247], v3 offset:43008\n"
"s_waitcnt vmcnt(0) lgkmcnt(0)\n"
"s_barrier\n"
"ds_read_b64_tr_b16 a[120:121], v2 offset:49152\n"
"ds_read_b64_tr_b16 a[122:123], v2 offset:51200\n"
"ds_read_b64_tr_b16 a[248:249], v3 offset:49152\n"
"ds_read_b64_tr_b16 a[250:251], v3 offset:51200\n"
"ds_read_b64_tr_b16 a[124:125], v2 offset:57344\n"
"ds_read_b64_tr_b16 a[126:127], v2 offset:59392\n"
"ds_read_b64_tr_b16 a[252:253], v3 offset:57344\n"
"ds_read_b64_tr_b16 a[254:255], v3 offset:59392\n"
"s_waitcnt lgkmcnt(0)\n"
"s_barrier\n"
"ds_read_b128 v[64:67], v120 offset:32768\n"
"ds_read_b128 v[68:71], v121 offset:32768\n"
"ds_read_b128 v[72:75], v122 offset:32768\n"
"ds_read_b128 v[76:79], v123 offset:32768\n"
"ds_read_b128 v[80:83], v124 offset:32768\n"
"ds_read_b128 v[84:87], v125 offset:32768\n"
".p2align 6\n"
"L_pqk_loop:\n"
"s_waitcnt lgkmcnt(5)\n"
"v_mfma_f32_32x32x16_bf16 v[0:15], a[0:3], v[64:67], 0\n"
"ds_read_b128 v[88:91], v126 offset:32768\n"
"v_mfma_f32_32x32x16_bf16 v[16:31], a[128:131], v[64:67], 0\n"
"s_waitcnt lgkmcnt(5)\n"
"v_mfma_f32_32x32x16_bf16 v[0:15], a[4:7], v[68:71], v[0:15]\n"
"ds_read_b128 v[92:95], v127 offset:32768\n"
"v_mfma_f32_32x32x16_bf16 v[16:31], a[132:135], v[68:71], v[16:31]\n"
"s_waitcnt lgkmcnt(5)\n"
"v_mfma_f32_32x32x16_bf16 v[0:15], a[8:11], v[72:75], v[0:15]\n"
"ds_read_b128 v[64:67], v120 offset:33024\n"
"v_mfma_f32_32x32x16_bf16 v[16:31], a[136:139], v[72:75], v[16:31]\n"
"v_mul_f32 v32, s33, v32\n"
"v_mul_f32 v48, s33, v48\n"
"v_cvt_pk_bf16_f32 v96, v32, v48\n"
"s_waitcnt lgkmcnt(5)\n"
"v_mfma_f32_32x32x16_bf16 v[0:15], a[12:15], v[76:79], v[0:15]\n"
"ds_read_b128 v[68:71], v121 offset:33024\n"
"v_mul_f32 v33, s33, v33\n"
"v_mul_f32 v49, s33, v49\n"
"v_cvt_pk_bf16_f32 v97, v33, v49\n"
"v_mfma_f32_32x32x16_bf16 v[16:31], a[140:143], v[76:79], v[16:31]\n"
"v_mul_f32 v34, s33, v34\n"
"v_mul_f32 v50, s33, v50\n"
"v_cvt_pk_bf16_f32 v98, v34, v50\n"
"s_waitcnt lgkmcnt(5)\n"
"v_mfma_f32_32x32x16_bf16 v[0:15], a[16:19], v[80:83], v[0:15]\n"
"ds_read_b128 v[72:75], v122 offset:33024\n"
"v_mul_f32 v35, s33, v35\n"
"v_mul_f32 v51, s33, v51\n"
"v_cvt_pk_bf16_f32 v99, v35, v51\n"
"v_mfma_f32_32x32x16_bf16 v[16:31], a[144:147], v[80:83], v[16:31]\n"
"buffer_store_dwordx4 v[96:99], v136, s[24:27], s28 offen offset:0 sc1\n"
"v_mul_f32 v36, s33, v36\n"
"v_mul_f32 v52, s33, v52\n"
"s_waitcnt lgkmcnt(5)\n"
"v_mfma_f32_32x32x16_bf16 v[0:15], a[20:23], v[84:87], v[0:15]\n"
"ds_read_b128 v[76:79], v123 offset:33024\n"
"v_cvt_pk_bf16_f32 v100, v36, v52\n"
"v_mul_f32 v37, s33, v37\n"
"v_mul_f32 v53, s33, v53\n"
"v_mfma_f32_32x32x16_bf16 v[16:31], a[148:151], v[84:87], v[16:31]\n"
"v_cvt_pk_bf16_f32 v101, v37, v53\n"
"v_mul_f32 v38, s33, v38\n"
"v_mul_f32 v54, s33, v54\n"
"s_waitcnt lgkmcnt(5)\n"
"v_mfma_f32_32x32x16_bf16 v[0:15], a[24:27], v[88:91], v[0:15]\n"
"ds_read_b128 v[80:83], v124 offset:33024\n"
"v_cvt_pk_bf16_f32 v102, v38, v54\n"
"v_mul_f32 v39, s33, v39\n"
"v_mul_f32 v55, s33, v55\n"
"v_mfma_f32_32x32x16_bf16 v[16:31], a[152:155], v[88:91], v[16:31]\n"
"v_cvt_pk_bf16_f32 v103, v39, v55\n"
"buffer_store_dwordx4 v[100:103], v136, s[24:27], s28 offen offset:1024 sc1\n"
"v_mul_f32 v40, s33, v40\n"
"s_waitcnt lgkmcnt(5)\n"
"v_mfma_f32_32x32x16_bf16 v[0:15], a[28:31], v[92:95], v[0:15]\n"
"ds_read_b128 v[84:87], v125 offset:33024\n"
"v_mul_f32 v56, s33, v56\n"
"v_cvt_pk_bf16_f32 v104, v40, v56\n"
"v_mul_f32 v41, s33, v41\n"
"v_mfma_f32_32x32x16_bf16 v[16:31], a[156:159], v[92:95], v[16:31]\n"
"v_mul_f32 v57, s33, v57\n"
"v_cvt_pk_bf16_f32 v105, v41, v57\n"
"v_mul_f32 v42, s33, v42\n"
"s_waitcnt lgkmcnt(5)\n"
"v_mfma_f32_32x32x16_bf16 v[0:15], a[32:35], v[64:67], v[0:15]\n"
"ds_read_b128 v[88:91], v126 offset:33024\n"
"v_mul_f32 v58, s33, v58\n"
"v_cvt_pk_bf16_f32 v106, v42, v58\n"
"v_mul_f32 v43, s33, v43\n"
"v_mfma_f32_32x32x16_bf16 v[16:31], a[160:163], v[64:67], v[16:31]\n"
"v_mul_f32 v59, s33, v59\n"
"v_cvt_pk_bf16_f32 v107, v43, v59\n"
"buffer_store_dwordx4 v[104:107], v136, s[24:27], s28 offen offset:2048 sc1\n"
"s_waitcnt lgkmcnt(5)\n"
"v_mfma_f32_32x32x16_bf16 v[0:15], a[36:39], v[68:71], v[0:15]\n"
"ds_read_b128 v[92:95], v127 offset:33024\n"
"v_mul_f32 v44, s33, v44\n"
"v_mul_f32 v60, s33, v60\n"
"v_cvt_pk_bf16_f32 v108, v44, v60\n"
"v_mfma_f32_32x32x16_bf16 v[16:31], a[164:167], v[68:71], v[16:31]\n"
"v_mul_f32 v45, s33, v45\n"
"v_mul_f32 v61, s33, v61\n"
"v_cvt_pk_bf16_f32 v109, v45, v61\n"
"s_waitcnt lgkmcnt(5)\n"
"v_mfma_f32_32x32x16_bf16 v[0:15], a[40:43], v[72:75], v[0:15]\n"
"ds_read_b128 v[64:67], v120 offset:33280\n"
"v_mul_f32 v46, s33, v46\n"
"v_mul_f32 v62, s33, v62\n"
"v_cvt_pk_bf16_f32 v110, v46, v62\n"
"v_mfma_f32_32x32x16_bf16 v[16:31], a[168:171], v[72:75], v[16:31]\n"
"v_mul_f32 v47, s33, v47\n"
"v_mul_f32 v63, s33, v63\n"
"v_cvt_pk_bf16_f32 v111, v47, v63\n"
"s_waitcnt lgkmcnt(5)\n"
"v_mfma_f32_32x32x16_bf16 v[0:15], a[44:47], v[76:79], v[0:15]\n"
"ds_read_b128 v[68:71], v121 offset:33280\n"
"buffer_store_dwordx4 v[108:111], v136, s[24:27], s28 offen offset:3072 sc1\n"
"v_mfma_f32_32x32x16_bf16 v[16:31], a[172:175], v[76:79], v[16:31]\n"
"s_waitcnt lgkmcnt(5)\n"
"v_mfma_f32_32x32x16_bf16 v[0:15], a[48:51], v[80:83], v[0:15]\n"
"ds_read_b128 v[72:75], v122 offset:33280\n"
"v_mfma_f32_32x32x16_bf16 v[16:31], a[176:179], v[80:83], v[16:31]\n"
"s_waitcnt lgkmcnt(5)\n"
"v_mfma_f32_32x32x16_bf16 v[0:15], a[52:55], v[84:87], v[0:15]\n"
"ds_read_b128 v[76:79], v123 offset:33280\n"
"s_add_u32 m0, s35, 0x10000\n"
"s_add_u32 s36, s29, 0x0\n"
"buffer_load_dwordx4 v128, s[20:23], s36 offen offset:0 lds\n"
"v_mfma_f32_32x32x16_bf16 v[16:31], a[180:183], v[84:87], v[16:31]\n"
"s_waitcnt lgkmcnt(5)\n"
"v_mfma_f32_32x32x16_bf16 v[0:15], a[56:59], v[88:91], v[0:15]\n"
"ds_read_b128 v[80:83], v124 offset:33280\n"
"buffer_load_dwordx4 v129, s[20:23], s36 offen offset:1024 lds\n"
"v_mfma_f32_32x32x16_bf16 v[16:31], a[184:187], v[88:91], v[16:31]\n"
"s_waitcnt lgkmcnt(5)\n"
"v_mfma_f32_32x32x16_bf16 v[0:15], a[60:63], v[92:95], v[0:15]\n"
"ds_read_b128 v[84:87], v125 offset:33280\n"
"buffer_load_dwordx4 v130, s[20:23], s36 offen offset:2048 lds\n"
"v_mfma_f32_32x32x16_bf16 v[16:31], a[188:191], v[92:95], v[16:31]\n"
"s_waitcnt lgkmcnt(5)\n"
"v_mfma_f32_32x32x16_bf16 v[0:15], a[64:67], v[64:67], v[0:15]\n"
"ds_read_b128 v[88:91], v126 offset:33280\n"
"buffer_load_dwordx4 v131, s[20:23], s36 offen offset:3072 lds\n"
"v_mfma_f32_32x32x16_bf16 v[16:31], a[192:195], v[64:67], v[16:31]\n"
"s_waitcnt lgkmcnt(5)\n"
"v_mfma_f32_32x32x16_bf16 v[0:15], a[68:71], v[68:71], v[0:15]\n"
"ds_read_b128 v[92:95], v127 offset:33280\n"
"s_add_u32 m0, s35, 0x11000\n"
"s_add_u32 s36, s29, 0x1000\n"
"buffer_load_dwordx4 v132, s[20:23], s36 offen offset:0 lds\n"
"v_mfma_f32_32x32x16_bf16 v[16:31], a[196:199], v[68:71], v[16:31]\n"
"s_waitcnt lgkmcnt(5)\n"
"v_mfma_f32_32x32x16_bf16 v[0:15], a[72:75], v[72:75], v[0:15]\n"
"ds_read_b128 v[64:67], v120 offset:33536\n"
"buffer_load_dwordx4 v133, s[20:23], s36 offen offset:1024 lds\n"
"v_mfma_f32_32x32x16_bf16 v[16:31], a[200:203], v[72:75], v[16:31]\n"
"s_waitcnt lgkmcnt(5)\n"
"v_mfma_f32_32x32x16_bf16 v[0:15], a[76:79], v[76:79], v[0:15]\n"
"ds_read_b128 v[68:71], v121 offset:33536\n"
"buffer_load_dwordx4 v134, s[20:23], s36 offen offset:2048 lds\n"
"v_mfma_f32_32x32x16_bf16 v[16:31], a[204:207], v[76:79], v[16:31]\n"
"s_waitcnt lgkmcnt(5)\n"
"v_mfma_f32_32x32x16_bf16 v[0:15], a[80:83], v[80:83], v[0:15]\n"
"ds_read_b128 v[72:75], v122 offset:33536\n"
"buffer_load_dwordx4 v135, s[20:23], s36 offen offset:3072 lds\n"
"v_mfma_f32_32x32x16_bf16 v[16:31], a[208:211], v[80:83], v[16:31]\n"
"s_waitcnt lgkmcnt(5)\n"
"v_mfma_f32_32x32x16_bf16 v[0:15], a[84:87], v[84:87], v[0:15]\n"
"ds_read_b128 v[76:79], v123 offset:33536\n"
"v_mfma_f32_32x32x16_bf16 v[16:31], a[212:215], v[84:87], v[16:31]\n"
"s_add_u32 s29, s29, 0x8000\n"
"s_add_u32 s28, s28, 0x2000\n"
"s_waitcnt lgkmcnt(5)\n"
"v_mfma_f32_32x32x16_bf16 v[0:15], a[88:91], v[88:91], v[0:15]\n"
"ds_read_b128 v[80:83], v124 offset:33536\n"
"v_mov_b32 v96, s34\n"
"v_mfma_f32_32x32x16_bf16 v[16:31], a[216:219], v[88:91], v[16:31]\n"
"s_waitcnt lgkmcnt(5)\n"
"v_mfma_f32_32x32x16_bf16 v[0:15], a[92:95], v[92:95], v[0:15]\n"
"ds_read_b128 v[84:87], v125 offset:33536\n"
"s_add_u32 s34, s34, 1\n"
"v_mfma_f32_32x32x16_bf16 v[16:31], a[220:223], v[92:95], v[16:31]\n"
"s_waitcnt lgkmcnt(5)\n"
"v_mfma_f32_32x32x16_bf16 v[0:15], a[96:99], v[64:67], v[0:15]\n"
"ds_read_b128 v[88:91], v126 offset:33536\n"
"v_mfma_f32_32x32x16_bf16 v[16:31], a[224:227], v[64:67], v[16:31]\n"
"s_waitcnt lgkmcnt(5)\n"
"v_mfma_f32_32x32x16_bf16 v[0:15], a[100:103], v[68:71], v[0:15]\n"
"ds_read_b128 v[92:95], v127 offset:33536\n"
"v_mfma_f32_32x32x16_bf16 v[16:31], a[228:231], v[68:71], v[16:31]\n"
"s_waitcnt vmcnt(8)\n"
"s_barrier\n"
"s_waitcnt lgkmcnt(5)\n"
"v_mfma_f32_32x32x16_bf16 v[0:15], a[104:107], v[72:75], v[0:15]\n"
"ds_read_b128 v[64:67], v120 offset:0\n"
"v_mfma_f32_32x32x16_bf16 v[16:31], a[232:235], v[72:75], v[16:31]\n"
"s_mov_b64 exec, 1\n"
"buffer_store_dword v96, off, s[12:15], s31 sc1\n"
"s_mov_b64 exec, -1\n"
"s_waitcnt lgkmcnt(5)\n"
"v_mfma_f32_32x32x16_bf16 v[0:15], a[108:111], v[76:79], v[0:15]\n"
"ds_read_b128 v[68:71], v121 offset:0\n"
"v_mfma_f32_32x32x16_bf16 v[16:31], a[236:239], v[76:79], v[16:31]\n"
"s_waitcnt lgkmcnt(5)\n"
"v_mfma_f32_32x32x16_bf16 v[0:15], a[112:115], v[80:83], v[0:15]\n"
"ds_read_b128 v[72:75], v122 offset:0\n"
"v_mfma_f32_32x32x16_bf16 v[16:31], a[240:243], v[80:83], v[16:31]\n"
"s_waitcnt lgkmcnt(5)\n"
"v_mfma_f32_32x32x16_bf16 v[0:15], a[116:119], v[84:87], v[0:15]\n"
"ds_read_b128 v[76:79], v123 offset:0\n"
"v_mfma_f32_32x32x16_bf16 v[16:31], a[244:247], v[84:87], v[16:31]\n"
"s_waitcnt lgkmcnt(5)\n"
"v_mfma_f32_32x32x16_bf16 v[0:15], a[120:123], v[88:91], v[0:15]\n"
"ds_read_b128 v[80:83], v124 offset:0\n"
"v_mfma_f32_32x32x16_bf16 v[16:31], a[248:251], v[88:91], v[16:31]\n"
"s_waitcnt lgkmcnt(5)\n"
"v_mfma_f32_32x32x16_bf16 v[0:15], a[124:127], v[92:95], v[0:15]\n"
"ds_read_b128 v[84:87], v125 offset:0\n"
"v_mfma_f32_32x32x16_bf16 v[16:31], a[252:255], v[92:95], v[16:31]\n"
"s_waitcnt lgkmcnt(5)\n"
"v_mfma_f32_32x32x16_bf16 v[32:47], a[0:3], v[64:67], 0\n"
"ds_read_b128 v[88:91], v126 offset:0\n"
"v_mfma_f32_32x32x16_bf16 v[48:63], a[128:131], v[64:67], 0\n"
"s_waitcnt lgkmcnt(5)\n"
"v_mfma_f32_32x32x16_bf16 v[32:47], a[4:7], v[68:71], v[32:47]\n"
"ds_read_b128 v[92:95], v127 offset:0\n"
"v_mfma_f32_32x32x16_bf16 v[48:63], a[132:135], v[68:71], v[48:63]\n"
"s_waitcnt lgkmcnt(5)\n"
"v_mfma_f32_32x32x16_bf16 v[32:47], a[8:11], v[72:75], v[32:47]\n"
"ds_read_b128 v[64:67], v120 offset:256\n"
"v_mfma_f32_32x32x16_bf16 v[48:63], a[136:139], v[72:75], v[48:63]\n"
"v_mul_f32 v0, s33, v0\n"
"v_mul_f32 v16, s33, v16\n"
"v_cvt_pk_bf16_f32 v96, v0, v16\n"
"s_waitcnt lgkmcnt(5)\n"
"v_mfma_f32_32x32x16_bf16 v[32:47], a[12:15], v[76:79], v[32:47]\n"
"ds_read_b128 v[68:71], v121 offset:256\n"
"v_mul_f32 v1, s33, v1\n"
"v_mul_f32 v17, s33, v17\n"
"v_cvt_pk_bf16_f32 v97, v1, v17\n"
"v_mfma_f32_32x32x16_bf16 v[48:63], a[140:143], v[76:79], v[48:63]\n"
"v_mul_f32 v2, s33, v2\n"
"v_mul_f32 v18, s33, v18\n"
"v_cvt_pk_bf16_f32 v98, v2, v18\n"
"s_waitcnt lgkmcnt(5)\n"
"v_mfma_f32_32x32x16_bf16 v[32:47], a[16:19], v[80:83], v[32:47]\n"
"ds_read_b128 v[72:75], v122 offset:256\n"
"v_mul_f32 v3, s33, v3\n"
"v_mul_f32 v19, s33, v19\n"
"v_cvt_pk_bf16_f32 v99, v3, v19\n"
"v_mfma_f32_32x32x16_bf16 v[48:63], a[144:147], v[80:83], v[48:63]\n"
"buffer_store_dwordx4 v[96:99], v136, s[24:27], s28 offen offset:0 sc1\n"
"v_mul_f32 v4, s33, v4\n"
"v_mul_f32 v20, s33, v20\n"
"s_waitcnt lgkmcnt(5)\n"
"v_mfma_f32_32x32x16_bf16 v[32:47], a[20:23], v[84:87], v[32:47]\n"
"ds_read_b128 v[76:79], v123 offset:256\n"
"v_cvt_pk_bf16_f32 v100, v4, v20\n"
"v_mul_f32 v5, s33, v5\n"
"v_mul_f32 v21, s33, v21\n"
"v_mfma_f32_32x32x16_bf16 v[48:63], a[148:151], v[84:87], v[48:63]\n"
"v_cvt_pk_bf16_f32 v101, v5, v21\n"
"v_mul_f32 v6, s33, v6\n"
"v_mul_f32 v22, s33, v22\n"
"s_waitcnt lgkmcnt(5)\n"
"v_mfma_f32_32x32x16_bf16 v[32:47], a[24:27], v[88:91], v[32:47]\n"
"ds_read_b128 v[80:83], v124 offset:256\n"
"v_cvt_pk_bf16_f32 v102, v6, v22\n"
"v_mul_f32 v7, s33, v7\n"
"v_mul_f32 v23, s33, v23\n"
"v_mfma_f32_32x32x16_bf16 v[48:63], a[152:155], v[88:91], v[48:63]\n"
"v_cvt_pk_bf16_f32 v103, v7, v23\n"
"buffer_store_dwordx4 v[100:103], v136, s[24:27], s28 offen offset:1024 sc1\n"
"v_mul_f32 v8, s33, v8\n"
"s_waitcnt lgkmcnt(5)\n"
"v_mfma_f32_32x32x16_bf16 v[32:47], a[28:31], v[92:95], v[32:47]\n"
"ds_read_b128 v[84:87], v125 offset:256\n"
"v_mul_f32 v24, s33, v24\n"
"v_cvt_pk_bf16_f32 v104, v8, v24\n"
"v_mul_f32 v9, s33, v9\n"
"v_mfma_f32_32x32x16_bf16 v[48:63], a[156:159], v[92:95], v[48:63]\n"
"v_mul_f32 v25, s33, v25\n"
"v_cvt_pk_bf16_f32 v105, v9, v25\n"
"v_mul_f32 v10, s33, v10\n"
"s_waitcnt lgkmcnt(5)\n"
"v_mfma_f32_32x32x16_bf16 v[32:47], a[32:35], v[64:67], v[32:47]\n"
"ds_read_b128 v[88:91], v126 offset:256\n"
"v_mul_f32 v26, s33, v26\n"
"v_cvt_pk_bf16_f32 v106, v10, v26\n"
"v_mul_f32 v11, s33, v11\n"
"v_mfma_f32_32x32x16_bf16 v[48:63], a[160:163], v[64:67], v[48:63]\n"
"v_mul_f32 v27, s33, v27\n"
"v_cvt_pk_bf16_f32 v107, v11, v27\n"
"buffer_store_dwordx4 v[104:107], v136, s[24:27], s28 offen offset:2048 sc1\n"
"s_waitcnt lgkmcnt(5)\n"
"v_mfma_f32_32x32x16_bf16 v[32:47], a[36:39], v[68:71], v[32:47]\n"
"ds_read_b128 v[92:95], v127 offset:256\n"
"v_mul_f32 v12, s33, v12\n"
"v_mul_f32 v28, s33, v28\n"
"v_cvt_pk_bf16_f32 v108, v12, v28\n"
"v_mfma_f32_32x32x16_bf16 v[48:63], a[164:167], v[68:71], v[48:63]\n"
"v_mul_f32 v13, s33, v13\n"
"v_mul_f32 v29, s33, v29\n"
"v_cvt_pk_bf16_f32 v109, v13, v29\n"
"s_waitcnt lgkmcnt(5)\n"
"v_mfma_f32_32x32x16_bf16 v[32:47], a[40:43], v[72:75], v[32:47]\n"
"ds_read_b128 v[64:67], v120 offset:512\n"
"v_mul_f32 v14, s33, v14\n"
"v_mul_f32 v30, s33, v30\n"
"v_cvt_pk_bf16_f32 v110, v14, v30\n"
"v_mfma_f32_32x32x16_bf16 v[48:63], a[168:171], v[72:75], v[48:63]\n"
"v_mul_f32 v15, s33, v15\n"
"v_mul_f32 v31, s33, v31\n"
"v_cvt_pk_bf16_f32 v111, v15, v31\n"
"s_waitcnt lgkmcnt(5)\n"
"v_mfma_f32_32x32x16_bf16 v[32:47], a[44:47], v[76:79], v[32:47]\n"
"ds_read_b128 v[68:71], v121 offset:512\n"
"buffer_store_dwordx4 v[108:111], v136, s[24:27], s28 offen offset:3072 sc1\n"
"v_mfma_f32_32x32x16_bf16 v[48:63], a[172:175], v[76:79], v[48:63]\n"
"s_waitcnt lgkmcnt(5)\n"
"v_mfma_f32_32x32x16_bf16 v[32:47], a[48:51], v[80:83], v[32:47]\n"
"ds_read_b128 v[72:75], v122 offset:512\n"
"v_mfma_f32_32x32x16_bf16 v[48:63], a[176:179], v[80:83], v[48:63]\n"
"s_waitcnt lgkmcnt(5)\n"
"v_mfma_f32_32x32x16_bf16 v[32:47], a[52:55], v[84:87], v[32:47]\n"
"ds_read_b128 v[76:79], v123 offset:512\n"
"s_add_u32 m0, s35, 0x8000\n"
"s_add_u32 s36, s29, 0x0\n"
"buffer_load_dwordx4 v128, s[20:23], s36 offen offset:0 lds\n"
"v_mfma_f32_32x32x16_bf16 v[48:63], a[180:183], v[84:87], v[48:63]\n"
"s_waitcnt lgkmcnt(5)\n"
"v_mfma_f32_32x32x16_bf16 v[32:47], a[56:59], v[88:91], v[32:47]\n"
"ds_read_b128 v[80:83], v124 offset:512\n"
"buffer_load_dwordx4 v129, s[20:23], s36 offen offset:1024 lds\n"
"v_mfma_f32_32x32x16_bf16 v[48:63], a[184:187], v[88:91], v[48:63]\n"
"s_waitcnt lgkmcnt(5)\n"
"v_mfma_f32_32x32x16_bf16 v[32:47], a[60:63], v[92:95], v[32:47]\n"
"ds_read_b128 v[84:87], v125 offset:512\n"
"buffer_load_dwordx4 v130, s[20:23], s36 offen offset:2048 lds\n"
"v_mfma_f32_32x32x16_bf16 v[48:63], a[188:191], v[92:95], v[48:63]\n"
"s_waitcnt lgkmcnt(5)\n"
"v_mfma_f32_32x32x16_bf16 v[32:47], a[64:67], v[64:67], v[32:47]\n"
"ds_read_b128 v[88:91], v126 offset:512\n"
"buffer_load_dwordx4 v131, s[20:23], s36 offen offset:3072 lds\n"
"v_mfma_f32_32x32x16_bf16 v[48:63], a[192:195], v[64:67], v[48:63]\n"
"s_waitcnt lgkmcnt(5)\n"
"v_mfma_f32_32x32x16_bf16 v[32:47], a[68:71], v[68:71], v[32:47]\n"
"ds_read_b128 v[92:95], v127 offset:512\n"
"s_add_u32 m0, s35, 0x9000\n"
"s_add_u32 s36, s29, 0x1000\n"
"buffer_load_dwordx4 v132, s[20:23], s36 offen offset:0 lds\n"
"v_mfma_f32_32x32x16_bf16 v[48:63], a[196:199], v[68:71], v[48:63]\n"
"s_waitcnt lgkmcnt(5)\n"
"v_mfma_f32_32x32x16_bf16 v[32:47], a[72:75], v[72:75], v[32:47]\n"
"ds_read_b128 v[64:67], v120 offset:768\n"
"buffer_load_dwordx4 v133, s[20:23], s36 offen offset:1024 lds\n"
"v_mfma_f32_32x32x16_bf16 v[48:63], a[200:203], v[72:75], v[48:63]\n"
"s_waitcnt lgkmcnt(5)\n"
"v_mfma_f32_32x32x16_bf16 v[32:47], a[76:79], v[76:79], v[32:47]\n"
"ds_read_b128 v[68:71], v121 offset:768\n"
"buffer_load_dwordx4 v134, s[20:23], s36 offen offset:2048 lds\n"
"v_mfma_f32_32x32x16_bf16 v[48:63], a[204:207], v[76:79], v[48:63]\n"
"s_waitcnt lgkmcnt(5)\n"
"v_mfma_f32_32x32x16_bf16 v[32:47], a[80:83], v[80:83], v[32:47]\n"
"ds_read_b128 v[72:75], v122 offset:768\n"
"buffer_load_dwordx4 v135, s[20:23], s36 offen offset:3072 lds\n"
"v_mfma_f32_32x32x16_bf16 v[48:63], a[208:211], v[80:83], v[48:63]\n"
"s_waitcnt lgkmcnt(5)\n"
"v_mfma_f32_32x32x16_bf16 v[32:47], a[84:87], v[84:87], v[32:47]\n"
"ds_read_b128 v[76:79], v123 offset:768\n"
"v_mfma_f32_32x32x16_bf16 v[48:63], a[212:215], v[84:87], v[48:63]\n"
"s_add_u32 s29, s29, 0x8000\n"
"s_add_u32 s28, s28, 0x2000\n"
"s_waitcnt lgkmcnt(5)\n"
"v_mfma_f32_32x32x16_bf16 v[32:47], a[88:91], v[88:91], v[32:47]\n"
"ds_read_b128 v[80:83], v124 offset:768\n"
"v_mov_b32 v96, s34\n"
"v_mfma_f32_32x32x16_bf16 v[48:63], a[216:219], v[88:91], v[48:63]\n"
"s_waitcnt lgkmcnt(5)\n"
"v_mfma_f32_32x32x16_bf16 v[32:47], a[92:95], v[92:95], v[32:47]\n"
"ds_read_b128 v[84:87], v125 offset:768\n"
"s_add_u32 s34, s34, 1\n"
"v_mfma_f32_32x32x16_bf16 v[48:63], a[220:223], v[92:95], v[48:63]\n"
"s_waitcnt lgkmcnt(5)\n"
"v_mfma_f32_32x32x16_bf16 v[32:47], a[96:99], v[64:67], v[32:47]\n"
"ds_read_b128 v[88:91], v126 offset:768\n"
"v_mfma_f32_32x32x16_bf16 v[48:63], a[224:227], v[64:67], v[48:63]\n"
"s_waitcnt lgkmcnt(5)\n"
"v_mfma_f32_32x32x16_bf16 v[32:47], a[100:103], v[68:71], v[32:47]\n"
"ds_read_b128 v[92:95], v127 offset:768\n"
"v_mfma_f32_32x32x16_bf16 v[48:63], a[228:231], v[68:71], v[48:63]\n"
"s_waitcnt vmcnt(8)\n"
"s_barrier\n"
"s_waitcnt lgkmcnt(5)\n"
"v_mfma_f32_32x32x16_bf16 v[32:47], a[104:107], v[72:75], v[32:47]\n"
"ds_read_b128 v[64:67], v112 offset:32768\n"
"v_mfma_f32_32x32x16_bf16 v[48:63], a[232:235], v[72:75], v[48:63]\n"
"s_mov_b64 exec, 1\n"
"buffer_store_dword v96, off, s[12:15], s31 sc1\n"
"s_mov_b64 exec, -1\n"
"s_waitcnt lgkmcnt(5)\n"
"v_mfma_f32_32x32x16_bf16 v[32:47], a[108:111], v[76:79], v[32:47]\n"
"ds_read_b128 v[68:71], v113 offset:32768\n"
"v_mfma_f32_32x32x16_bf16 v[48:63], a[236:239], v[76:79], v[48:63]\n"
"s_waitcnt lgkmcnt(5)\n"
"v_mfma_f32_32x32x16_bf16 v[32:47], a[112:115], v[80:83], v[32:47]\n"
"ds_read_b128 v[72:75], v114 offset:32768\n"
"v_mfma_f32_32x32x16_bf16 v[48:63], a[240:243], v[80:83], v[48:63]\n"
"s_waitcnt lgkmcnt(5)\n"
"v_mfma_f32_32x32x16_bf16 v[32:47], a[116:119], v[84:87], v[32:47]\n"
"ds_read_b128 v[76:79], v115 offset:32768\n"
"v_mfma_f32_32x32x16_bf16 v[48:63], a[244:247], v[84:87], v[48:63]\n"
"s_waitcnt lgkmcnt(5)\n"
"v_mfma_f32_32x32x16_bf16 v[32:47], a[120:123], v[88:91], v[32:47]\n"
"ds_read_b128 v[80:83], v116 offset:32768\n"
"v_mfma_f32_32x32x16_bf16 v[48:63], a[248:251], v[88:91], v[48:63]\n"
"s_waitcnt lgkmcnt(5)\n"
"v_mfma_f32_32x32x16_bf16 v[32:47], a[124:127], v[92:95], v[32:47]\n"
"ds_read_b128 v[84:87], v117 offset:32768\n"
"v_mfma_f32_32x32x16_bf16 v[48:63], a[252:255], v[92:95], v[48:63]\n"
"s_waitcnt lgkmcnt(5)\n"
"v_mfma_f32_32x32x16_bf16 v[0:15], a[0:3], v[64:67], 0\n"
"ds_read_b128 v[88:91], v118 offset:32768\n"
"v_mfma_f32_32x32x16_bf16 v[16:31], a[128:131], v[64:67], 0\n"
"s_waitcnt lgkmcnt(5)\n"
"v_mfma_f32_32x32x16_bf16 v[0:15], a[4:7], v[68:71], v[0:15]\n"
"ds_read_b128 v[92:95], v119 offset:32768\n"
"v_mfma_f32_32x32x16_bf16 v[16:31], a[132:135], v[68:71], v[16:31]\n"
"s_waitcnt lgkmcnt(5)\n"
"v_mfma_f32_32x32x16_bf16 v[0:15], a[8:11], v[72:75], v[0:15]\n"
"ds_read_b128 v[64:67], v112 offset:33024\n"
"v_mfma_f32_32x32x16_bf16 v[16:31], a[136:139], v[72:75], v[16:31]\n"
"v_mul_f32 v32, s33, v32\n"
"v_mul_f32 v48, s33, v48\n"
"v_cvt_pk_bf16_f32 v96, v32, v48\n"
"s_waitcnt lgkmcnt(5)\n"
"v_mfma_f32_32x32x16_bf16 v[0:15], a[12:15], v[76:79], v[0:15]\n"
"ds_read_b128 v[68:71], v113 offset:33024\n"
"v_mul_f32 v33, s33, v33\n"
"v_mul_f32 v49, s33, v49\n"
"v_cvt_pk_bf16_f32 v97, v33, v49\n"
"v_mfma_f32_32x32x16_bf16 v[16:31], a[140:143], v[76:79], v[16:31]\n"
"v_mul_f32 v34, s33, v34\n"
"v_mul_f32 v50, s33, v50\n"
"v_cvt_pk_bf16_f32 v98, v34, v50\n"
"s_waitcnt lgkmcnt(5)\n"
"v_mfma_f32_32x32x16_bf16 v[0:15], a[16:19], v[80:83], v[0:15]\n"
"ds_read_b128 v[72:75], v114 offset:33024\n"
"v_mul_f32 v35, s33, v35\n"
"v_mul_f32 v51, s33, v51\n"
"v_cvt_pk_bf16_f32 v99, v35, v51\n"
"v_mfma_f32_32x32x16_bf16 v[16:31], a[144:147], v[80:83], v[16:31]\n"
"buffer_store_dwordx4 v[96:99], v136, s[24:27], s28 offen offset:0 sc1\n"
"v_mul_f32 v36, s33, v36\n"
"v_mul_f32 v52, s33, v52\n"
"s_waitcnt lgkmcnt(5)\n"
"v_mfma_f32_32x32x16_bf16 v[0:15], a[20:23], v[84:87], v[0:15]\n"
"ds_read_b128 v[76:79], v115 offset:33024\n"
"v_cvt_pk_bf16_f32 v100, v36, v52\n"
"v_mul_f32 v37, s33, v37\n"
"v_mul_f32 v53, s33, v53\n"
"v_mfma_f32_32x32x16_bf16 v[16:31], a[148:151], v[84:87], v[16:31]\n"
"v_cvt_pk_bf16_f32 v101, v37, v53\n"
"v_mul_f32 v38, s33, v38\n"
"v_mul_f32 v54, s33, v54\n"
"s_waitcnt lgkmcnt(5)\n"
"v_mfma_f32_32x32x16_bf16 v[0:15], a[24:27], v[88:91], v[0:15]\n"
"ds_read_b128 v[80:83], v116 offset:33024\n"
"v_cvt_pk_bf16_f32 v102, v38, v54\n"
"v_mul_f32 v39, s33, v39\n"
"v_mul_f32 v55, s33, v55\n"
"v_mfma_f32_32x32x16_bf16 v[16:31], a[152:155], v[88:91], v[16:31]\n"
"v_cvt_pk_bf16_f32 v103, v39, v55\n"
"buffer_store_dwordx4 v[100:103], v136, s[24:27], s28 offen offset:1024 sc1\n"
"v_mul_f32 v40, s33, v40\n"
"s_waitcnt lgkmcnt(5)\n"
"v_mfma_f32_32x32x16_bf16 v[0:15], a[28:31], v[92:95], v[0:15]\n"
"ds_read_b128 v[84:87], v117 offset:33024\n"
"v_mul_f32 v56, s33, v56\n"
"v_cvt_pk_bf16_f32 v104, v40, v56\n"
"v_mul_f32 v41, s33, v41\n"
"v_mfma_f32_32x32x16_bf16 v[16:31], a[156:159], v[92:95], v[16:31]\n"
"v_mul_f32 v57, s33, v57\n"
"v_cvt_pk_bf16_f32 v105, v41, v57\n"
"v_mul_f32 v42, s33, v42\n"
"s_waitcnt lgkmcnt(5)\n"
"v_mfma_f32_32x32x16_bf16 v[0:15], a[32:35], v[64:67], v[0:15]\n"
"ds_read_b128 v[88:91], v118 offset:33024\n"
"v_mul_f32 v58, s33, v58\n"
"v_cvt_pk_bf16_f32 v106, v42, v58\n"
"v_mul_f32 v43, s33, v43\n"
"v_mfma_f32_32x32x16_bf16 v[16:31], a[160:163], v[64:67], v[16:31]\n"
"v_mul_f32 v59, s33, v59\n"
"v_cvt_pk_bf16_f32 v107, v43, v59\n"
"buffer_store_dwordx4 v[104:107], v136, s[24:27], s28 offen offset:2048 sc1\n"
"s_waitcnt lgkmcnt(5)\n"
"v_mfma_f32_32x32x16_bf16 v[0:15], a[36:39], v[68:71], v[0:15]\n"
"ds_read_b128 v[92:95], v119 offset:33024\n"
"v_mul_f32 v44, s33, v44\n"
"v_mul_f32 v60, s33, v60\n"
"v_cvt_pk_bf16_f32 v108, v44, v60\n"
"v_mfma_f32_32x32x16_bf16 v[16:31], a[164:167], v[68:71], v[16:31]\n"
"v_mul_f32 v45, s33, v45\n"
"v_mul_f32 v61, s33, v61\n"
"v_cvt_pk_bf16_f32 v109, v45, v61\n"
"s_waitcnt lgkmcnt(5)\n"
"v_mfma_f32_32x32x16_bf16 v[0:15], a[40:43], v[72:75], v[0:15]\n"
"ds_read_b128 v[64:67], v112 offset:33280\n"
"v_mul_f32 v46, s33, v46\n"
"v_mul_f32 v62, s33, v62\n"
"v_cvt_pk_bf16_f32 v110, v46, v62\n"
"v_mfma_f32_32x32x16_bf16 v[16:31], a[168:171], v[72:75], v[16:31]\n"
"v_mul_f32 v47, s33, v47\n"
"v_mul_f32 v63, s33, v63\n"
"v_cvt_pk_bf16_f32 v111, v47, v63\n"
"s_waitcnt lgkmcnt(5)\n"
"v_mfma_f32_32x32x16_bf16 v[0:15], a[44:47], v[76:79], v[0:15]\n"
"ds_read_b128 v[68:71], v113 offset:33280\n"
"buffer_store_dwordx4 v[108:111], v136, s[24:27], s28 offen offset:3072 sc1\n"
"v_mfma_f32_32x32x16_bf16 v[16:31], a[172:175], v[76:79], v[16:31]\n"
"s_waitcnt lgkmcnt(5)\n"
"v_mfma_f32_32x32x16_bf16 v[0:15], a[48:51], v[80:83], v[0:15]\n"
"ds_read_b128 v[72:75], v114 offset:33280\n"
"v_mfma_f32_32x32x16_bf16 v[16:31], a[176:179], v[80:83], v[16:31]\n"
"s_waitcnt lgkmcnt(5)\n"
"v_mfma_f32_32x32x16_bf16 v[0:15], a[52:55], v[84:87], v[0:15]\n"
"ds_read_b128 v[76:79], v115 offset:33280\n"
"s_add_u32 m0, s35, 0x20000\n"
"s_add_u32 s36, s29, 0x0\n"
"buffer_load_dwordx4 v128, s[20:23], s36 offen offset:0 lds\n"
"v_mfma_f32_32x32x16_bf16 v[16:31], a[180:183], v[84:87], v[16:31]\n"
"s_waitcnt lgkmcnt(5)\n"
"v_mfma_f32_32x32x16_bf16 v[0:15], a[56:59], v[88:91], v[0:15]\n"
"ds_read_b128 v[80:83], v116 offset:33280\n"
"buffer_load_dwordx4 v129, s[20:23], s36 offen offset:1024 lds\n"
"v_mfma_f32_32x32x16_bf16 v[16:31], a[184:187], v[88:91], v[16:31]\n"
"s_waitcnt lgkmcnt(5)\n"
"v_mfma_f32_32x32x16_bf16 v[0:15], a[60:63], v[92:95], v[0:15]\n"
"ds_read_b128 v[84:87], v117 offset:33280\n"
"buffer_load_dwordx4 v130, s[20:23], s36 offen offset:2048 lds\n"
"v_mfma_f32_32x32x16_bf16 v[16:31], a[188:191], v[92:95], v[16:31]\n"
"s_waitcnt lgkmcnt(5)\n"
"v_mfma_f32_32x32x16_bf16 v[0:15], a[64:67], v[64:67], v[0:15]\n"
"ds_read_b128 v[88:91], v118 offset:33280\n"
"buffer_load_dwordx4 v131, s[20:23], s36 offen offset:3072 lds\n"
"v_mfma_f32_32x32x16_bf16 v[16:31], a[192:195], v[64:67], v[16:31]\n"
"s_waitcnt lgkmcnt(5)\n"
"v_mfma_f32_32x32x16_bf16 v[0:15], a[68:71], v[68:71], v[0:15]\n"
"ds_read_b128 v[92:95], v119 offset:33280\n"
"s_add_u32 m0, s35, 0x21000\n"
"s_add_u32 s36, s29, 0x1000\n"
"buffer_load_dwordx4 v132, s[20:23], s36 offen offset:0 lds\n"
"v_mfma_f32_32x32x16_bf16 v[16:31], a[196:199], v[68:71], v[16:31]\n"
"s_waitcnt lgkmcnt(5)\n"
"v_mfma_f32_32x32x16_bf16 v[0:15], a[72:75], v[72:75], v[0:15]\n"
"ds_read_b128 v[64:67], v112 offset:33536\n"
"buffer_load_dwordx4 v133, s[20:23], s36 offen offset:1024 lds\n"
"v_mfma_f32_32x32x16_bf16 v[16:31], a[200:203], v[72:75], v[16:31]\n"
"s_waitcnt lgkmcnt(5)\n"
"v_mfma_f32_32x32x16_bf16 v[0:15], a[76:79], v[76:79], v[0:15]\n"
"ds_read_b128 v[68:71], v113 offset:33536\n"
"buffer_load_dwordx4 v134, s[20:23], s36 offen offset:2048 lds\n"
"v_mfma_f32_32x32x16_bf16 v[16:31], a[204:207], v[76:79], v[16:31]\n"
"s_waitcnt lgkmcnt(5)\n"
"v_mfma_f32_32x32x16_bf16 v[0:15], a[80:83], v[80:83], v[0:15]\n"
"ds_read_b128 v[72:75], v114 offset:33536\n"
"buffer_load_dwordx4 v135, s[20:23], s36 offen offset:3072 lds\n"
"v_mfma_f32_32x32x16_bf16 v[16:31], a[208:211], v[80:83], v[16:31]\n"
"s_waitcnt lgkmcnt(5)\n"
"v_mfma_f32_32x32x16_bf16 v[0:15], a[84:87], v[84:87], v[0:15]\n"
"ds_read_b128 v[76:79], v115 offset:33536\n"
"v_mfma_f32_32x32x16_bf16 v[16:31], a[212:215], v[84:87], v[16:31]\n"
"s_add_u32 s29, s29, 0x8000\n"
"s_add_u32 s28, s28, 0x2000\n"
"s_waitcnt lgkmcnt(5)\n"
"v_mfma_f32_32x32x16_bf16 v[0:15], a[88:91], v[88:91], v[0:15]\n"
"ds_read_b128 v[80:83], v116 offset:33536\n"
"v_mov_b32 v96, s34\n"
"v_mfma_f32_32x32x16_bf16 v[16:31], a[216:219], v[88:91], v[16:31]\n"
"s_waitcnt lgkmcnt(5)\n"
"v_mfma_f32_32x32x16_bf16 v[0:15], a[92:95], v[92:95], v[0:15]\n"
"ds_read_b128 v[84:87], v117 offset:33536\n"
"s_add_u32 s34, s34, 1\n"
"v_mfma_f32_32x32x16_bf16 v[16:31], a[220:223], v[92:95], v[16:31]\n"
"s_waitcnt lgkmcnt(5)\n"
"v_mfma_f32_32x32x16_bf16 v[0:15], a[96:99], v[64:67], v[0:15]\n"
"ds_read_b128 v[88:91], v118 offset:33536\n"
"v_mfma_f32_32x32x16_bf16 v[16:31], a[224:227], v[64:67], v[16:31]\n"
"s_waitcnt lgkmcnt(5)\n"
"v_mfma_f32_32x32x16_bf16 v[0:15], a[100:103], v[68:71], v[0:15]\n"
"ds_read_b128 v[92:95], v119 offset:33536\n"
"v_mfma_f32_32x32x16_bf16 v[16:31], a[228:231], v[68:71], v[16:31]\n"
"s_waitcnt vmcnt(8)\n"
"s_barrier\n"
"s_waitcnt lgkmcnt(5)\n"
"v_mfma_f32_32x32x16_bf16 v[0:15], a[104:107], v[72:75], v[0:15]\n"
"ds_read_b128 v[64:67], v112 offset:0\n"
"v_mfma_f32_32x32x16_bf16 v[16:31], a[232:235], v[72:75], v[16:31]\n"
"s_mov_b64 exec, 1\n"
"buffer_store_dword v96, off, s[12:15], s31 sc1\n"
"s_mov_b64 exec, -1\n"
"s_waitcnt lgkmcnt(5)\n"
"v_mfma_f32_32x32x16_bf16 v[0:15], a[108:111], v[76:79], v[0:15]\n"
"ds_read_b128 v[68:71], v113 offset:0\n"
"v_mfma_f32_32x32x16_bf16 v[16:31], a[236:239], v[76:79], v[16:31]\n"
"s_waitcnt lgkmcnt(5)\n"
"v_mfma_f32_32x32x16_bf16 v[0:15], a[112:115], v[80:83], v[0:15]\n"
"ds_read_b128 v[72:75], v114 offset:0\n"
"v_mfma_f32_32x32x16_bf16 v[16:31], a[240:243], v[80:83], v[16:31]\n"
"s_waitcnt lgkmcnt(5)\n"
"v_mfma_f32_32x32x16_bf16 v[0:15], a[116:119], v[84:87], v[0:15]\n"
"ds_read_b128 v[76:79], v115 offset:0\n"
"v_mfma_f32_32x32x16_bf16 v[16:31], a[244:247], v[84:87], v[16:31]\n"
"s_waitcnt lgkmcnt(5)\n"
"v_mfma_f32_32x32x16_bf16 v[0:15], a[120:123], v[88:91], v[0:15]\n"
"ds_read_b128 v[80:83], v116 offset:0\n"
"v_mfma_f32_32x32x16_bf16 v[16:31], a[248:251], v[88:91], v[16:31]\n"
"s_waitcnt lgkmcnt(5)\n"
"v_mfma_f32_32x32x16_bf16 v[0:15], a[124:127], v[92:95], v[0:15]\n"
"ds_read_b128 v[84:87], v117 offset:0\n"
"v_mfma_f32_32x32x16_bf16 v[16:31], a[252:255], v[92:95], v[16:31]\n"
"s_waitcnt lgkmcnt(5)\n"
"v_mfma_f32_32x32x16_bf16 v[32:47], a[0:3], v[64:67], 0\n"
"ds_read_b128 v[88:91], v118 offset:0\n"
"v_mfma_f32_32x32x16_bf16 v[48:63], a[128:131], v[64:67], 0\n"
"s_waitcnt lgkmcnt(5)\n"
"v_mfma_f32_32x32x16_bf16 v[32:47], a[4:7], v[68:71], v[32:47]\n"
"ds_read_b128 v[92:95], v119 offset:0\n"
"v_mfma_f32_32x32x16_bf16 v[48:63], a[132:135], v[68:71], v[48:63]\n"
"s_waitcnt lgkmcnt(5)\n"
"v_mfma_f32_32x32x16_bf16 v[32:47], a[8:11], v[72:75], v[32:47]\n"
"ds_read_b128 v[64:67], v112 offset:256\n"
"v_mfma_f32_32x32x16_bf16 v[48:63], a[136:139], v[72:75], v[48:63]\n"
"v_mul_f32 v0, s33, v0\n"
"v_mul_f32 v16, s33, v16\n"
"v_cvt_pk_bf16_f32 v96, v0, v16\n"
"s_waitcnt lgkmcnt(5)\n"
"v_mfma_f32_32x32x16_bf16 v[32:47], a[12:15], v[76:79], v[32:47]\n"
"ds_read_b128 v[68:71], v113 offset:256\n"
"v_mul_f32 v1, s33, v1\n"
"v_mul_f32 v17, s33, v17\n"
"v_cvt_pk_bf16_f32 v97, v1, v17\n"
"v_mfma_f32_32x32x16_bf16 v[48:63], a[140:143], v[76:79], v[48:63]\n"
"v_mul_f32 v2, s33, v2\n"
"v_mul_f32 v18, s33, v18\n"
"v_cvt_pk_bf16_f32 v98, v2, v18\n"
"s_waitcnt lgkmcnt(5)\n"
"v_mfma_f32_32x32x16_bf16 v[32:47], a[16:19], v[80:83], v[32:47]\n"
"ds_read_b128 v[72:75], v114 offset:256\n"
"v_mul_f32 v3, s33, v3\n"
"v_mul_f32 v19, s33, v19\n"
"v_cvt_pk_bf16_f32 v99, v3, v19\n"
"v_mfma_f32_32x32x16_bf16 v[48:63], a[144:147], v[80:83], v[48:63]\n"
"buffer_store_dwordx4 v[96:99], v136, s[24:27], s28 offen offset:0 sc1\n"
"v_mul_f32 v4, s33, v4\n"
"v_mul_f32 v20, s33, v20\n"
"s_waitcnt lgkmcnt(5)\n"
"v_mfma_f32_32x32x16_bf16 v[32:47], a[20:23], v[84:87], v[32:47]\n"
"ds_read_b128 v[76:79], v115 offset:256\n"
"v_cvt_pk_bf16_f32 v100, v4, v20\n"
"v_mul_f32 v5, s33, v5\n"
"v_mul_f32 v21, s33, v21\n"
"v_mfma_f32_32x32x16_bf16 v[48:63], a[148:151], v[84:87], v[48:63]\n"
"v_cvt_pk_bf16_f32 v101, v5, v21\n"
"v_mul_f32 v6, s33, v6\n"
"v_mul_f32 v22, s33, v22\n"
"s_waitcnt lgkmcnt(5)\n"
"v_mfma_f32_32x32x16_bf16 v[32:47], a[24:27], v[88:91], v[32:47]\n"
"ds_read_b128 v[80:83], v116 offset:256\n"
"v_cvt_pk_bf16_f32 v102, v6, v22\n"
"v_mul_f32 v7, s33, v7\n"
"v_mul_f32 v23, s33, v23\n"
"v_mfma_f32_32x32x16_bf16 v[48:63], a[152:155], v[88:91], v[48:63]\n"
"v_cvt_pk_bf16_f32 v103, v7, v23\n"
"buffer_store_dwordx4 v[100:103], v136, s[24:27], s28 offen offset:1024 sc1\n"
"v_mul_f32 v8, s33, v8\n"
"s_waitcnt lgkmcnt(5)\n"
"v_mfma_f32_32x32x16_bf16 v[32:47], a[28:31], v[92:95], v[32:47]\n"
"ds_read_b128 v[84:87], v117 offset:256\n"
"v_mul_f32 v24, s33, v24\n"
"v_cvt_pk_bf16_f32 v104, v8, v24\n"
"v_mul_f32 v9, s33, v9\n"
"v_mfma_f32_32x32x16_bf16 v[48:63], a[156:159], v[92:95], v[48:63]\n"
"v_mul_f32 v25, s33, v25\n"
"v_cvt_pk_bf16_f32 v105, v9, v25\n"
"v_mul_f32 v10, s33, v10\n"
"s_waitcnt lgkmcnt(5)\n"
"v_mfma_f32_32x32x16_bf16 v[32:47], a[32:35], v[64:67], v[32:47]\n"
"ds_read_b128 v[88:91], v118 offset:256\n"
"v_mul_f32 v26, s33, v26\n"
"v_cvt_pk_bf16_f32 v106, v10, v26\n"
"v_mul_f32 v11, s33, v11\n"
"v_mfma_f32_32x32x16_bf16 v[48:63], a[160:163], v[64:67], v[48:63]\n"
"v_mul_f32 v27, s33, v27\n"
"v_cvt_pk_bf16_f32 v107, v11, v27\n"
"buffer_store_dwordx4 v[104:107], v136, s[24:27], s28 offen offset:2048 sc1\n"
"s_waitcnt lgkmcnt(5)\n"
"v_mfma_f32_32x32x16_bf16 v[32:47], a[36:39], v[68:71], v[32:47]\n"
"ds_read_b128 v[92:95], v119 offset:256\n"
"v_mul_f32 v12, s33, v12\n"
"v_mul_f32 v28, s33, v28\n"
"v_cvt_pk_bf16_f32 v108, v12, v28\n"
"v_mfma_f32_32x32x16_bf16 v[48:63], a[164:167], v[68:71], v[48:63]\n"
"v_mul_f32 v13, s33, v13\n"
"v_mul_f32 v29, s33, v29\n"
"v_cvt_pk_bf16_f32 v109, v13, v29\n"
"s_waitcnt lgkmcnt(5)\n"
"v_mfma_f32_32x32x16_bf16 v[32:47], a[40:43], v[72:75], v[32:47]\n"
"ds_read_b128 v[64:67], v112 offset:512\n"
"v_mul_f32 v14, s33, v14\n"
"v_mul_f32 v30, s33, v30\n"
"v_cvt_pk_bf16_f32 v110, v14, v30\n"
"v_mfma_f32_32x32x16_bf16 v[48:63], a[168:171], v[72:75], v[48:63]\n"
"v_mul_f32 v15, s33, v15\n"
"v_mul_f32 v31, s33, v31\n"
"v_cvt_pk_bf16_f32 v111, v15, v31\n"
"s_waitcnt lgkmcnt(5)\n"
"v_mfma_f32_32x32x16_bf16 v[32:47], a[44:47], v[76:79], v[32:47]\n"
"ds_read_b128 v[68:71], v113 offset:512\n"
"buffer_store_dwordx4 v[108:111], v136, s[24:27], s28 offen offset:3072 sc1\n"
"v_mfma_f32_32x32x16_bf16 v[48:63], a[172:175], v[76:79], v[48:63]\n"
"s_waitcnt lgkmcnt(5)\n"
"v_mfma_f32_32x32x16_bf16 v[32:47], a[48:51], v[80:83], v[32:47]\n"
"ds_read_b128 v[72:75], v114 offset:512\n"
"v_mfma_f32_32x32x16_bf16 v[48:63], a[176:179], v[80:83], v[48:63]\n"
"s_waitcnt lgkmcnt(5)\n"
"v_mfma_f32_32x32x16_bf16 v[32:47], a[52:55], v[84:87], v[32:47]\n"
"ds_read_b128 v[76:79], v115 offset:512\n"
"s_add_u32 m0, s35, 0x18000\n"
"s_add_u32 s36, s29, 0x0\n"
"buffer_load_dwordx4 v128, s[20:23], s36 offen offset:0 lds\n"
"v_mfma_f32_32x32x16_bf16 v[48:63], a[180:183], v[84:87], v[48:63]\n"
"s_waitcnt lgkmcnt(5)\n"
"v_mfma_f32_32x32x16_bf16 v[32:47], a[56:59], v[88:91], v[32:47]\n"
"ds_read_b128 v[80:83], v116 offset:512\n"
"buffer_load_dwordx4 v129, s[20:23], s36 offen offset:1024 lds\n"
"v_mfma_f32_32x32x16_bf16 v[48:63], a[184:187], v[88:91], v[48:63]\n"
"s_waitcnt lgkmcnt(5)\n"
"v_mfma_f32_32x32x16_bf16 v[32:47], a[60:63], v[92:95], v[32:47]\n"
"ds_read_b128 v[84:87], v117 offset:512\n"
"buffer_load_dwordx4 v130, s[20:23], s36 offen offset:2048 lds\n"
"v_mfma_f32_32x32x16_bf16 v[48:63], a[188:191], v[92:95], v[48:63]\n"
"s_waitcnt lgkmcnt(5)\n"
"v_mfma_f32_32x32x16_bf16 v[32:47], a[64:67], v[64:67], v[32:47]\n"
"ds_read_b128 v[88:91], v118 offset:512\n"
"buffer_load_dwordx4 v131, s[20:23], s36 offen offset:3072 lds\n"
"v_mfma_f32_32x32x16_bf16 v[48:63], a[192:195], v[64:67], v[48:63]\n"
"s_waitcnt lgkmcnt(5)\n"
"v_mfma_f32_32x32x16_bf16 v[32:47], a[68:71], v[68:71], v[32:47]\n"
"ds_read_b128 v[92:95], v119 offset:512\n"
"s_add_u32 m0, s35, 0x19000\n"
"s_add_u32 s36, s29, 0x1000\n"
"buffer_load_dwordx4 v132, s[20:23], s36 offen offset:0 lds\n"
"v_mfma_f32_32x32x16_bf16 v[48:63], a[196:199], v[68:71], v[48:63]\n"
"s_waitcnt lgkmcnt(5)\n"
"v_mfma_f32_32x32x16_bf16 v[32:47], a[72:75], v[72:75], v[32:47]\n"
"ds_read_b128 v[64:67], v112 offset:768\n"
"buffer_load_dwordx4 v133, s[20:23], s36 offen offset:1024 lds\n"
"v_mfma_f32_32x32x16_bf16 v[48:63], a[200:203], v[72:75], v[48:63]\n"
"s_waitcnt lgkmcnt(5)\n"
"v_mfma_f32_32x32x16_bf16 v[32:47], a[76:79], v[76:79], v[32:47]\n"
"ds_read_b128 v[68:71], v113 offset:768\n"
"buffer_load_dwordx4 v134, s[20:23], s36 offen offset:2048 lds\n"
"v_mfma_f32_32x32x16_bf16 v[48:63], a[204:207], v[76:79], v[48:63]\n"
"s_waitcnt lgkmcnt(5)\n"
"v_mfma_f32_32x32x16_bf16 v[32:47], a[80:83], v[80:83], v[32:47]\n"
"ds_read_b128 v[72:75], v114 offset:768\n"
"buffer_load_dwordx4 v135, s[20:23], s36 offen offset:3072 lds\n"
"v_mfma_f32_32x32x16_bf16 v[48:63], a[208:211], v[80:83], v[48:63]\n"
"s_waitcnt lgkmcnt(5)\n"
"v_mfma_f32_32x32x16_bf16 v[32:47], a[84:87], v[84:87], v[32:47]\n"
"ds_read_b128 v[76:79], v115 offset:768\n"
"v_mfma_f32_32x32x16_bf16 v[48:63], a[212:215], v[84:87], v[48:63]\n"
"s_add_u32 s29, s29, 0x8000\n"
"s_add_u32 s28, s28, 0x2000\n"
"s_waitcnt lgkmcnt(5)\n"
"v_mfma_f32_32x32x16_bf16 v[32:47], a[88:91], v[88:91], v[32:47]\n"
"ds_read_b128 v[80:83], v116 offset:768\n"
"v_mov_b32 v96, s34\n"
"v_mfma_f32_32x32x16_bf16 v[48:63], a[216:219], v[88:91], v[48:63]\n"
"s_waitcnt lgkmcnt(5)\n"
"v_mfma_f32_32x32x16_bf16 v[32:47], a[92:95], v[92:95], v[32:47]\n"
"ds_read_b128 v[84:87], v117 offset:768\n"
"s_add_u32 s34, s34, 1\n"
"v_mfma_f32_32x32x16_bf16 v[48:63], a[220:223], v[92:95], v[48:63]\n"
"s_waitcnt lgkmcnt(5)\n"
"v_mfma_f32_32x32x16_bf16 v[32:47], a[96:99], v[64:67], v[32:47]\n"
"ds_read_b128 v[88:91], v118 offset:768\n"
"v_mfma_f32_32x32x16_bf16 v[48:63], a[224:227], v[64:67], v[48:63]\n"
"s_waitcnt lgkmcnt(5)\n"
"v_mfma_f32_32x32x16_bf16 v[32:47], a[100:103], v[68:71], v[32:47]\n"
"ds_read_b128 v[92:95], v119 offset:768\n"
"v_mfma_f32_32x32x16_bf16 v[48:63], a[228:231], v[68:71], v[48:63]\n"
"s_waitcnt vmcnt(8)\n"
"s_barrier\n"
"s_waitcnt lgkmcnt(5)\n"
"v_mfma_f32_32x32x16_bf16 v[32:47], a[104:107], v[72:75], v[32:47]\n"
"ds_read_b128 v[64:67], v120 offset:32768\n"
"v_mfma_f32_32x32x16_bf16 v[48:63], a[232:235], v[72:75], v[48:63]\n"
"s_mov_b64 exec, 1\n"
"buffer_store_dword v96, off, s[12:15], s31 sc1\n"
"s_mov_b64 exec, -1\n"
"s_waitcnt lgkmcnt(5)\n"
"v_mfma_f32_32x32x16_bf16 v[32:47], a[108:111], v[76:79], v[32:47]\n"
"ds_read_b128 v[68:71], v121 offset:32768\n"
"v_mfma_f32_32x32x16_bf16 v[48:63], a[236:239], v[76:79], v[48:63]\n"
"s_waitcnt lgkmcnt(5)\n"
"v_mfma_f32_32x32x16_bf16 v[32:47], a[112:115], v[80:83], v[32:47]\n"
"ds_read_b128 v[72:75], v122 offset:32768\n"
"v_mfma_f32_32x32x16_bf16 v[48:63], a[240:243], v[80:83], v[48:63]\n"
"s_waitcnt lgkmcnt(5)\n"
"v_mfma_f32_32x32x16_bf16 v[32:47], a[116:119], v[84:87], v[32:47]\n"
"ds_read_b128 v[76:79], v123 offset:32768\n"
"v_mfma_f32_32x32x16_bf16 v[48:63], a[244:247], v[84:87], v[48:63]\n"
"s_waitcnt lgkmcnt(5)\n"
"v_mfma_f32_32x32x16_bf16 v[32:47], a[120:123], v[88:91], v[32:47]\n"
"ds_read_b128 v[80:83], v124 offset:32768\n"
"v_mfma_f32_32x32x16_bf16 v[48:63], a[248:251], v[88:91], v[48:63]\n"
"s_waitcnt lgkmcnt(5)\n"
"v_mfma_f32_32x32x16_bf16 v[32:47], a[124:127], v[92:95], v[32:47]\n"
"ds_read_b128 v[84:87], v125 offset:32768\n"
"v_mfma_f32_32x32x16_bf16 v[48:63], a[252:255], v[92:95], v[48:63]\n"
"s_sub_u32 s30, s30, 1\n"
"s_cmp_lg_u32 s30, 0\n"
"s_cbranch_scc1 L_pqk_loop\n"
"s_nop 7\n"
"s_nop 3\n"
"v_mul_f32 v32, s33, v32\n"
"v_mul_f32 v48, s33, v48\n"
"v_cvt_pk_bf16_f32 v96, v32, v48\n"
"v_mul_f32 v33, s33, v33\n"
"v_mul_f32 v49, s33, v49\n"
"v_cvt_pk_bf16_f32 v97, v33, v49\n"
"v_mul_f32 v34, s33, v34\n"
"v_mul_f32 v50, s33, v50\n"
"v_cvt_pk_bf16_f32 v98, v34, v50\n"
"v_mul_f32 v35, s33, v35\n"
"v_mul_f32 v51, s33, v51\n"
"v_cvt_pk_bf16_f32 v99, v35, v51\n"
"buffer_store_dwordx4 v[96:99], v136, s[24:27], s28 offen offset:0 sc1\n"
"v_mul_f32 v36, s33, v36\n"
"v_mul_f32 v52, s33, v52\n"
"v_cvt_pk_bf16_f32 v100, v36, v52\n"
"v_mul_f32 v37, s33, v37\n"
"v_mul_f32 v53, s33, v53\n"
"v_cvt_pk_bf16_f32 v101, v37, v53\n"
"v_mul_f32 v38, s33, v38\n"
"v_mul_f32 v54, s33, v54\n"
"v_cvt_pk_bf16_f32 v102, v38, v54\n"
"v_mul_f32 v39, s33, v39\n"
"v_mul_f32 v55, s33, v55\n"
"v_cvt_pk_bf16_f32 v103, v39, v55\n"
"buffer_store_dwordx4 v[100:103], v136, s[24:27], s28 offen offset:1024 sc1\n"
"v_mul_f32 v40, s33, v40\n"
"v_mul_f32 v56, s33, v56\n"
"v_cvt_pk_bf16_f32 v104, v40, v56\n"
"v_mul_f32 v41, s33, v41\n"
"v_mul_f32 v57, s33, v57\n"
"v_cvt_pk_bf16_f32 v105, v41, v57\n"
"v_mul_f32 v42, s33, v42\n"
"v_mul_f32 v58, s33, v58\n"
"v_cvt_pk_bf16_f32 v106, v42, v58\n"
"v_mul_f32 v43, s33, v43\n"
"v_mul_f32 v59, s33, v59\n"
"v_cvt_pk_bf16_f32 v107, v43, v59\n"
"buffer_store_dwordx4 v[104:107], v136, s[24:27], s28 offen offset:2048 sc1\n"
"v_mul_f32 v44, s33, v44\n"
"v_mul_f32 v60, s33, v60\n"
"v_cvt_pk_bf16_f32 v108, v44, v60\n"
"v_mul_f32 v45, s33, v45\n"
"v_mul_f32 v61, s33, v61\n"
"v_cvt_pk_bf16_f32 v109, v45, v61\n"
"v_mul_f32 v46, s33, v46\n"
"v_mul_f32 v62, s33, v62\n"
"v_cvt_pk_bf16_f32 v110, v46, v62\n"
"v_mul_f32 v47, s33, v47\n"
"v_mul_f32 v63, s33, v63\n"
"v_cvt_pk_bf16_f32 v111, v47, v63\n"
"buffer_store_dwordx4 v[108:111], v136, s[24:27], s28 offen offset:3072 sc1\n"
"v_mov_b32 v0, s34\n"
"s_waitcnt vmcnt(0) lgkmcnt(0)\n"
"s_barrier\n"
"s_mov_b64 exec, 1\n"
"buffer_store_dword v0, off, s[12:15], s31 sc1\n"
"s_mov_b64 exec, -1\n"
"s_branch L_next\n"
"L_pv:\n"
"s_load_dwordx2 s[40:41], s[0:1], s65\n"
"s_load_dwordx2 s[42:43], s[0:1], s66\n"
"s_and_b32 s3, s5, 1\n"
"s_lshl_b32 s3, s3, 7\n"
"v_lshlrev_b32 v136, 4, v255\n"
"v_xor_b32 v128, s3, v136\n"
"v_xor_b32 v129, 16, v128\n"
"v_xor_b32 v130, 32, v128\n"
"v_xor_b32 v131, 48, v128\n"
"v_xor_b32 v132, 64, v128\n"
"v_xor_b32 v133, 80, v128\n"
"v_xor_b32 v134, 96, v128\n"
"v_xor_b32 v135, 112, v128\n"
"s_lshl_b32 s35, s5, 13\n"
"s_add_u32 s29, s69, s35\n"
"s_waitcnt lgkmcnt(0)\n"
"s_add_u32 s20, s42, 0x0\n"
"s_addc_u32 s21, s43, 0\n"
"s_and_b32 s21, s21, 0xffff\n"
"s_mov_b32 s22, 0x200000\n"
"s_mov_b32 s23, 0x20000\n"
"s_add_u32 s16, s40, 0x0\n"
"s_addc_u32 s17, s41, 0\n"
"s_add_u32 s16, s16, s68\n"
"s_addc_u32 s17, s17, 0\n"
"s_and_b32 s17, s17, 0xffff\n"
"s_mov_b32 s18, 0x400000\n"
"s_mov_b32 s19, 0x20000\n"
"s_sub_u32 s18, s18, s68\n"
"s_barrier\n"
"s_add_u32 m0, s35, 0x20000\n"
"s_add_u32 s36, s29, 0x0\n"
"buffer_load_dwordx4 v128, s[20:23], s36 offen offset:0 lds\n"
"buffer_load_dwordx4 v129, s[20:23], s36 offen offset:1024 lds\n"
"buffer_load_dwordx4 v130, s[20:23], s36 offen offset:2048 lds\n"
"buffer_load_dwordx4 v131, s[20:23], s36 offen offset:3072 lds\n"
"s_add_u32 m0, s35, 0x21000\n"
"s_add_u32 s36, s29, 0x1000\n"
"buffer_load_dwordx4 v132, s[20:23], s36 offen offset:0 lds\n"
"buffer_load_dwordx4 v133, s[20:23], s36 offen offset:1024 lds\n"
"buffer_load_dwordx4 v134, s[20:23], s36 offen offset:2048 lds\n"
"buffer_load_dwordx4 v135, s[20:23], s36 offen offset:3072 lds\n"
"s_add_u32 s29, s29, 0x8000\n"
"s_add_u32 m0, s35, 0x18000\n"
"s_add_u32 s36, s29, 0x0\n"
"buffer_load_dwordx4 v128, s[20:23], s36 offen offset:0 lds\n"
"buffer_load_dwordx4 v129, s[20:23], s36 offen offset:1024 lds\n"
"buffer_load_dwordx4 v130, s[20:23], s36 offen offset:2048 lds\n"
"buffer_load_dwordx4 v131, s[20:23], s36 offen offset:3072 lds\n"
"s_add_u32 m0, s35, 0x19000\n"
"s_add_u32 s36, s29, 0x1000\n"
"buffer_load_dwordx4 v132, s[20:23], s36 offen offset:0 lds\n"
"buffer_load_dwordx4 v133, s[20:23], s36 offen offset:1024 lds\n"
"buffer_load_dwordx4 v134, s[20:23], s36 offen offset:2048 lds\n"
"buffer_load_dwordx4 v135, s[20:23], s36 offen offset:3072 lds\n"
"s_add_u32 s29, s29, 0x8000\n"
"v_mov_b32 v0, 0x0\n"
"v_bfe_i32 v4, v255, 0, 1\n"
"v_and_b32 v4, 0x10, v4\n"
"v_xor_b32 v0, v0, v4\n"
"v_bfe_i32 v4, v255, 1, 1\n"
"v_and_b32 v4, 0x20, v4\n"
"v_xor_b32 v0, v0, v4\n"
"v_bfe_i32 v4, v255, 2, 1\n"
"v_and_b32 v4, 0x40, v4\n"
"v_xor_b32 v0, v0, v4\n"
"v_bfe_i32 v4, v255, 3, 1\n"
"v_and_b32 v4, 0x80, v4\n"
"v_xor_b32 v0, v0, v4\n"
"v_bfe_i32 v4, v255, 4, 1\n"
"v_and_b32 v4, 0x100, v4\n"
"v_xor_b32 v0, v0, v4\n"
"v_bfe_i32 v4, v255, 5, 1\n"
"v_and_b32 v4, 0x2040, v4\n"
"v_xor_b32 v0, v0, v4\n"
"s_lshl_b32 s3, s5, 16\n"
"v_add_u32 v0, s3, v0\n"
"v_xor_b32 v1, 0x80, v0\n"
"s_lshl_b32 s37, s5, 12\n"
"s_add_u32 m0, s37, 0x0\n"
"s_mov_b32 s3, 0x0\n"
"buffer_load_dwordx4 v0, s[16:19], s3 offen lds\n"
"s_add_u32 m0, s37, 0x400\n"
"s_mov_b32 s3, 0x4000\n"
"buffer_load_dwordx4 v1, s[16:19], s3 offen lds\n"
"s_add_u32 m0, s37, 0x800\n"
"s_mov_b32 s3, 0x8000\n"
"buffer_load_dwordx4 v0, s[16:19], s3 offen lds\n"
"s_add_u32 m0, s37, 0xc00\n"
"s_mov_b32 s3, 0xc000\n"
"buffer_load_dwordx4 v1, s[16:19], s3 offen lds\n"
"s_add_u32 m0, s37, 0x4000\n"
"s_mov_b32 s3, 0x40000\n"
"buffer_load_dwordx4 v0, s[16:19], s3 offen lds\n"
"s_add_u32 m0, s37, 0x4400\n"
"s_mov_b32 s3, 0x44000\n"
"buffer_load_dwordx4 v1, s[16:19], s3 offen lds\n"
"s_add_u32 m0, s37, 0x4800\n"
"s_mov_b32 s3, 0x48000\n"
"buffer_load_dwordx4 v0, s[16:19], s3 offen lds\n"
"s_add_u32 m0, s37, 0x4c00\n"
"s_mov_b32 s3, 0x4c000\n"
"buffer_load_dwordx4 v1, s[16:19], s3 offen lds\n"
"s_add_u32 m0, s37, 0x8000\n"
"s_mov_b32 s3, 0x80000\n"
"buffer_load_dwordx4 v0, s[16:19], s3 offen lds\n"
"s_add_u32 m0, s37, 0x8400\n"
"s_mov_b32 s3, 0x84000\n"
"buffer_load_dwordx4 v1, s[16:19], s3 offen lds\n"
"s_add_u32 m0, s37, 0x8800\n"
"s_mov_b32 s3, 0x88000\n"
"buffer_load_dwordx4 v0, s[16:19], s3 offen lds\n"
"s_add_u32 m0, s37, 0x8c00\n"
"s_mov_b32 s3, 0x8c000\n"
"buffer_load_dwordx4 v1, s[16:19], s3 offen lds\n"
"s_add_u32 m0, s37, 0xc000\n"
"s_mov_b32 s3, 0xc0000\n"
"buffer_load_dwordx4 v0, s[16:19], s3 offen lds\n"
"s_add_u32 m0, s37, 0xc400\n"
"s_mov_b32 s3, 0xc4000\n"
"buffer_load_dwordx4 v1, s[16:19], s3 offen lds\n"
"s_add_u32 m0, s37, 0xc800\n"
"s_mov_b32 s3, 0xc8000\n"
"buffer_load_dwordx4 v0, s[16:19], s3 offen lds\n"
"s_add_u32 m0, s37, 0xcc00\n"
"s_mov_b32 s3, 0xcc000\n"
"buffer_load_dwordx4 v1, s[16:19], s3 offen lds\n"
"s_add_u32 m0, s37, 0x10000\n"
"s_mov_b32 s3, 0x100000\n"
"buffer_load_dwordx4 v0, s[16:19], s3 offen lds\n"
"s_add_u32 m0, s37, 0x10400\n"
"s_mov_b32 s3, 0x104000\n"
"buffer_load_dwordx4 v1, s[16:19], s3 offen lds\n"
"s_add_u32 m0, s37, 0x10800\n"
"s_mov_b32 s3, 0x108000\n"
"buffer_load_dwordx4 v0, s[16:19], s3 offen lds\n"
"s_add_u32 m0, s37, 0x10c00\n"
"s_mov_b32 s3, 0x10c000\n"
"buffer_load_dwordx4 v1, s[16:19], s3 offen lds\n"
"v_mov_b32 v2, 0x0\n"
"v_bfe_i32 v4, v255, 0, 1\n"
"v_and_b32 v4, 0x8, v4\n"
"v_add_u32 v2, v2, v4\n"
"v_bfe_i32 v4, v255, 1, 1\n"
"v_and_b32 v4, 0x10, v4\n"
"v_add_u32 v2, v2, v4\n"
"v_bfe_i32 v4, v255, 2, 1\n"
"v_and_b32 v4, 0x240, v4\n"
"v_add_u32 v2, v2, v4\n"
"v_bfe_i32 v4, v255, 3, 1\n"
"v_and_b32 v4, 0x480, v4\n"
"v_add_u32 v2, v2, v4\n"
"v_bfe_i32 v4, v255, 4, 1\n"
"v_and_b32 v4, 0x20, v4\n"
"v_add_u32 v2, v2, v4\n"
"v_bfe_i32 v4, v255, 5, 1\n"
"v_and_b32 v4, 0x1000, v4\n"
"v_add_u32 v2, v2, v4\n"
"s_lshl_b32 s3, s5, 7\n"
"v_xor_b32 v2, s3, v2\n"
"v_xor_b32 v3, 64, v2\n"
"v_add_u32 v5, 0x10000, v2\n"
"v_add_u32 v6, 0x10000, v3\n"
"v_mov_b32 v112, 0x8000\n"
"v_bfe_i32 v4, v255, 0, 1\n"
"v_and_b32 v4, 0x410, v4\n"
"v_xor_b32 v112, v112, v4\n"
"v_bfe_i32 v4, v255, 1, 1\n"
"v_and_b32 v4, 0x820, v4\n"
"v_xor_b32 v112, v112, v4\n"
"v_bfe_i32 v4, v255, 2, 1\n"
"v_and_b32 v4, 0x1040, v4\n"
"v_xor_b32 v112, v112, v4\n"
"v_bfe_i32 v4, v255, 3, 1\n"
"v_and_b32 v4, 0x2080, v4\n"
"v_xor_b32 v112, v112, v4\n"
"v_bfe_i32 v4, v255, 4, 1\n"
"v_and_b32 v4, 0x4000, v4\n"
"v_xor_b32 v112, v112, v4\n"
"v_bfe_i32 v4, v255, 5, 1\n"
"v_and_b32 v4, 0x10, v4\n"
"v_xor_b32 v112, v112, v4\n"
"v_xor_b32 v113, 32, v112\n"
"v_xor_b32 v114, 64, v112\n"
"v_xor_b32 v115, 96, v112\n"
"v_xor_b32 v116, 128, v112\n"
"v_xor_b32 v117, 160, v112\n"
"v_xor_b32 v118, 192, v112\n"
"v_xor_b32 v119, 224, v112\n"
"v_add_u32 v120, 0x10000, v112\n"
"v_add_u32 v121, 0x10000, v113\n"
"v_add_u32 v122, 0x10000, v114\n"
"v_add_u32 v123, 0x10000, v115\n"
"v_add_u32 v124, 0x10000, v116\n"
"v_add_u32 v125, 0x10000, v117\n"
"v_add_u32 v126, 0x10000, v118\n"
"v_add_u32 v127, 0x10000, v119\n"
"s_lshr_b32 s3, s5, 1\n"
"s_lshl_b32 s3, s3, 19\n"
"s_and_b32 s36, s5, 1\n"
"s_lshl_b32 s36, s36, 12\n"
"s_add_u32 s3, s3, s36\n"
"s_add_u32 s38, s3, s67\n"
"s_add_u32 s24, s10, 0x0\n"
"s_addc_u32 s25, s11, 0\n"
"s_add_u32 s24, s24, s38\n"
"s_addc_u32 s25, s25, 0\n"
"s_and_b32 s25, s25, 0xffff\n"
"s_mov_b32 s26, 0x7ff00000\n"
"s_mov_b32 s27, 0x20000\n"
"s_lshl_b32 s3, s70, 13\n"
"s_sub_u32 s26, s3, s36\n"
"s_mov_b32 s28, 0xffffe000\n"
"s_lshr_b32 s30, s70, 2\n"
"s_and_b32 s31, s71, 0xffff\n"
"s_mov_b32 s34, 0\n"
"s_cmp_eq_u32 s5, 0\n"
"s_cselect_b32 s31, s31, 0x7fff0000\n"
"s_mov_b32 s33, 1.0\n"
"s_bitcmp1_b32 s71, 31\n"
"s_cselect_b32 s33, 0x3e0293ee, s33\n"
"s_waitcnt vmcnt(16) lgkmcnt(0)\n"
"s_barrier\n"
"s_add_u32 m0, s37, 0x14000\n"
"s_mov_b32 s3, 0x140000\n"
"buffer_load_dwordx4 v0, s[16:19], s3 offen lds\n"
"s_add_u32 m0, s37, 0x14400\n"
"s_mov_b32 s3, 0x144000\n"
"buffer_load_dwordx4 v1, s[16:19], s3 offen lds\n"
"s_add_u32 m0, s37, 0x14800\n"
"s_mov_b32 s3, 0x148000\n"
"buffer_load_dwordx4 v0, s[16:19], s3 offen lds\n"
"s_add_u32 m0, s37, 0x14c00\n"
"s_mov_b32 s3, 0x14c000\n"
"buffer_load_dwordx4 v1, s[16:19], s3 offen lds\n"
"ds_read_b64_tr_b16 a[0:1], v2 offset:0\n"
"ds_read_b64_tr_b16 a[2:3], v2 offset:2048\n"
"ds_read_b64_tr_b16 a[128:129], v3 offset:0\n"
"ds_read_b64_tr_b16 a[130:131], v3 offset:2048\n"
"ds_read_b64_tr_b16 a[4:5], v2 offset:8192\n"
"ds_read_b64_tr_b16 a[6:7], v2 offset:10240\n"
"ds_read_b64_tr_b16 a[132:133], v3 offset:8192\n"
"ds_read_b64_tr_b16 a[134:135], v3 offset:10240\n"
"s_waitcnt vmcnt(16) lgkmcnt(0)\n"
"s_barrier\n"
"s_add_u32 m0, s37, 0x0\n"
"s_mov_b32 s3, 0x180000\n"
"buffer_load_dwordx4 v0, s[16:19], s3 offen lds\n"
"s_add_u32 m0, s37, 0x400\n"
"s_mov_b32 s3, 0x184000\n"
"buffer_load_dwordx4 v1, s[16:19], s3 offen lds\n"
"s_add_u32 m0, s37, 0x800\n"
"s_mov_b32 s3, 0x188000\n"
"buffer_load_dwordx4 v0, s[16:19], s3 offen lds\n"
"s_add_u32 m0, s37, 0xc00\n"
"s_mov_b32 s3, 0x18c000\n"
"buffer_load_dwordx4 v1, s[16:19], s3 offen lds\n"
"ds_read_b64_tr_b16 a[8:9], v2 offset:16384\n"
"ds_read_b64_tr_b16 a[10:11], v2 offset:18432\n"
"ds_read_b64_tr_b16 a[136:137], v3 offset:16384\n"
"ds_read_b64_tr_b16 a[138:139], v3 offset:18432\n"
"ds_read_b64_tr_b16 a[12:13], v2 offset:24576\n"
"ds_read_b64_tr_b16 a[14:15], v2 offset:26624\n"
"ds_read_b64_tr_b16 a[140:141], v3 offset:24576\n"
"ds_read_b64_tr_b16 a[142:143], v3 offset:26624\n"
"s_waitcnt vmcnt(16) lgkmcnt(0)\n"
"s_barrier\n"
"s_add_u32 m0, s37, 0x4000\n"
"s_mov_b32 s3, 0x1c0000\n"
"buffer_load_dwordx4 v0, s[16:19], s3 offen lds\n"
"s_add_u32 m0, s37, 0x4400\n"
"s_mov_b32 s3, 0x1c4000\n"
"buffer_load_dwordx4 v1, s[16:19], s3 offen lds\n"
"s_add_u32 m0, s37, 0x4800\n"
"s_mov_b32 s3, 0x1c8000\n"
"buffer_load_dwordx4 v0, s[16:19], s3 offen lds\n"
"s_add_u32 m0, s37, 0x4c00\n"
"s_mov_b32 s3, 0x1cc000\n"
"buffer_load_dwordx4 v1, s[16:19], s3 offen lds\n"
"ds_read_b64_tr_b16 a[16:17], v2 offset:32768\n"
"ds_read_b64_tr_b16 a[18:19], v2 offset:34816\n"
"ds_read_b64_tr_b16 a[144:145], v3 offset:32768\n"
"ds_read_b64_tr_b16 a[146:147], v3 offset:34816\n"
"ds_read_b64_tr_b16 a[20:21], v2 offset:40960\n"
"ds_read_b64_tr_b16 a[22:23], v2 offset:43008\n"
"ds_read_b64_tr_b16 a[148:149], v3 offset:40960\n"
"ds_read_b64_tr_b16 a[150:151], v3 offset:43008\n"
"s_waitcnt vmcnt(16) lgkmcnt(0)\n"
"s_barrier\n"
"s_add_u32 m0, s37, 0x8000\n"
"s_mov_b32 s3, 0x200000\n"
"buffer_load_dwordx4 v0, s[16:19], s3 offen lds\n"
"s_add_u32 m0, s37, 0x8400\n"
"s_mov_b32 s3, 0x204000\n"
"buffer_load_dwordx4 v1, s[16:19], s3 offen lds\n"
"s_add_u32 m0, s37, 0x8800\n"
"s_mov_b32 s3, 0x208000\n"
"buffer_load_dwordx4 v0, s[16:19], s3 offen lds\n"
"s_add_u32 m0, s37, 0x8c00\n"
"s_mov_b32 s3, 0x20c000\n"
"buffer_load_dwordx4 v1, s[16:19], s3 offen lds\n"
"ds_read_b64_tr_b16 a[24:25], v2 offset:49152\n"
"ds_read_b64_tr_b16 a[26:27], v2 offset:51200\n"
"ds_read_b64_tr_b16 a[152:153], v3 offset:49152\n"
"ds_read_b64_tr_b16 a[154:155], v3 offset:51200\n"
"ds_read_b64_tr_b16 a[28:29], v2 offset:57344\n"
"ds_read_b64_tr_b16 a[30:31], v2 offset:59392\n"
"ds_read_b64_tr_b16 a[156:157], v3 offset:57344\n"
"ds_read_b64_tr_b16 a[158:159], v3 offset:59392\n"
"s_waitcnt vmcnt(16) lgkmcnt(0)\n"
"s_barrier\n"
"s_add_u32 m0, s37, 0xc000\n"
"s_mov_b32 s3, 0x240000\n"
"buffer_load_dwordx4 v0, s[16:19], s3 offen lds\n"
"s_add_u32 m0, s37, 0xc400\n"
"s_mov_b32 s3, 0x244000\n"
"buffer_load_dwordx4 v1, s[16:19], s3 offen lds\n"
"s_add_u32 m0, s37, 0xc800\n"
"s_mov_b32 s3, 0x248000\n"
"buffer_load_dwordx4 v0, s[16:19], s3 offen lds\n"
"s_add_u32 m0, s37, 0xcc00\n"
"s_mov_b32 s3, 0x24c000\n"
"buffer_load_dwordx4 v1, s[16:19], s3 offen lds\n"
"ds_read_b64_tr_b16 a[32:33], v5 offset:0\n"
"ds_read_b64_tr_b16 a[34:35], v5 offset:2048\n"
"ds_read_b64_tr_b16 a[160:161], v6 offset:0\n"
"ds_read_b64_tr_b16 a[162:163], v6 offset:2048\n"
"ds_read_b64_tr_b16 a[36:37], v5 offset:8192\n"
"ds_read_b64_tr_b16 a[38:39], v5 offset:10240\n"
"ds_read_b64_tr_b16 a[164:165], v6 offset:8192\n"
"ds_read_b64_tr_b16 a[166:167], v6 offset:10240\n"
"s_waitcnt vmcnt(16) lgkmcnt(0)\n"
"s_barrier\n"
"s_add_u32 m0, s37, 0x10000\n"
"s_mov_b32 s3, 0x280000\n"
"buffer_load_dwordx4 v0, s[16:19], s3 offen lds\n"
"s_add_u32 m0, s37, 0x10400\n"
"s_mov_b32 s3, 0x284000\n"
"buffer_load_dwordx4 v1, s[16:19], s3 offen lds\n"
"s_add_u32 m0, s37, 0x10800\n"
"s_mov_b32 s3, 0x288000\n"
"buffer_load_dwordx4 v0, s[16:19], s3 offen lds\n"
"s_add_u32 m0, s37, 0x10c00\n"
"s_mov_b32 s3, 0x28c000\n"
"buffer_load_dwordx4 v1, s[16:19], s3 offen lds\n"
"ds_read_b64_tr_b16 a[40:41], v5 offset:16384\n"
"ds_read_b64_tr_b16 a[42:43], v5 offset:18432\n"
"ds_read_b64_tr_b16 a[168:169], v6 offset:16384\n"
"ds_read_b64_tr_b16 a[170:171], v6 offset:18432\n"
"ds_read_b64_tr_b16 a[44:45], v5 offset:24576\n"
"ds_read_b64_tr_b16 a[46:47], v5 offset:26624\n"
"ds_read_b64_tr_b16 a[172:173], v6 offset:24576\n"
"ds_read_b64_tr_b16 a[174:175], v6 offset:26624\n"
"s_waitcnt vmcnt(16) lgkmcnt(0)\n"
"s_barrier\n"
"s_add_u32 m0, s37, 0x14000\n"
"s_mov_b32 s3, 0x2c0000\n"
"buffer_load_dwordx4 v0, s[16:19], s3 offen lds\n"
"s_add_u32 m0, s37, 0x14400\n"
"s_mov_b32 s3, 0x2c4000\n"
"buffer_load_dwordx4 v1, s[16:19], s3 offen lds\n"
"s_add_u32 m0, s37, 0x14800\n"
"s_mov_b32 s3, 0x2c8000\n"
"buffer_load_dwordx4 v0, s[16:19], s3 offen lds\n"
"s_add_u32 m0, s37, 0x14c00\n"
"s_mov_b32 s3, 0x2cc000\n"
"buffer_load_dwordx4 v1, s[16:19], s3 offen lds\n"
"ds_read_b64_tr_b16 a[48:49], v2 offset:0\n"
"ds_read_b64_tr_b16 a[50:51], v2 offset:2048\n"
"ds_read_b64_tr_b16 a[176:177], v3 offset:0\n"
"ds_read_b64_tr_b16 a[178:179], v3 offset:2048\n"
"ds_read_b64_tr_b16 a[52:53], v2 offset:8192\n"
"ds_read_b64_tr_b16 a[54:55], v2 offset:10240\n"
"ds_read_b64_tr_b16 a[180:181], v3 offset:8192\n"
"ds_read_b64_tr_b16 a[182:183], v3 offset:10240\n"
"s_waitcnt vmcnt(16) lgkmcnt(0)\n"
"s_barrier\n"
"s_add_u32 m0, s37, 0x0\n"
"s_mov_b32 s3, 0x300000\n"
"buffer_load_dwordx4 v0, s[16:19], s3 offen lds\n"
"s_add_u32 m0, s37, 0x400\n"
"s_mov_b32 s3, 0x304000\n"
"buffer_load_dwordx4 v1, s[16:19], s3 offen lds\n"
"s_add_u32 m0, s37, 0x800\n"
"s_mov_b32 s3, 0x308000\n"
"buffer_load_dwordx4 v0, s[16:19], s3 offen lds\n"
"s_add_u32 m0, s37, 0xc00\n"
"s_mov_b32 s3, 0x30c000\n"
"buffer_load_dwordx4 v1, s[16:19], s3 offen lds\n"
"ds_read_b64_tr_b16 a[56:57], v2 offset:16384\n"
"ds_read_b64_tr_b16 a[58:59], v2 offset:18432\n"
"ds_read_b64_tr_b16 a[184:185], v3 offset:16384\n"
"ds_read_b64_tr_b16 a[186:187], v3 offset:18432\n"
"ds_read_b64_tr_b16 a[60:61], v2 offset:24576\n"
"ds_read_b64_tr_b16 a[62:63], v2 offset:26624\n"
"ds_read_b64_tr_b16 a[188:189], v3 offset:24576\n"
"ds_read_b64_tr_b16 a[190:191], v3 offset:26624\n"
"s_waitcnt vmcnt(16) lgkmcnt(0)\n"
"s_barrier\n"
"s_add_u32 m0, s37, 0x4000\n"
"s_mov_b32 s3, 0x340000\n"
"buffer_load_dwordx4 v0, s[16:19], s3 offen lds\n"
"s_add_u32 m0, s37, 0x4400\n"
"s_mov_b32 s3, 0x344000\n"
"buffer_load_dwordx4 v1, s[16:19], s3 offen lds\n"
"s_add_u32 m0, s37, 0x4800\n"
"s_mov_b32 s3, 0x348000\n"
"buffer_load_dwordx4 v0, s[16:19], s3 offen lds\n"
"s_add_u32 m0, s37, 0x4c00\n"
"s_mov_b32 s3, 0x34c000\n"
"buffer_load_dwordx4 v1, s[16:19], s3 offen lds\n"
"ds_read_b64_tr_b16 a[64:65], v2 offset:32768\n"
"ds_read_b64_tr_b16 a[66:67], v2 offset:34816\n"
"ds_read_b64_tr_b16 a[192:193], v3 offset:32768\n"
"ds_read_b64_tr_b16 a[194:195], v3 offset:34816\n"
"ds_read_b64_tr_b16 a[68:69], v2 offset:40960\n"
"ds_read_b64_tr_b16 a[70:71], v2 offset:43008\n"
"ds_read_b64_tr_b16 a[196:197], v3 offset:40960\n"
"ds_read_b64_tr_b16 a[198:199], v3 offset:43008\n"
"s_waitcnt vmcnt(16) lgkmcnt(0)\n"
"s_barrier\n"
"s_add_u32 m0, s37, 0x8000\n"
"s_mov_b32 s3, 0x380000\n"
"buffer_load_dwordx4 v0, s[16:19], s3 offen lds\n"
"s_add_u32 m0, s37, 0x8400\n"
"s_mov_b32 s3, 0x384000\n"
"buffer_load_dwordx4 v1, s[16:19], s3 offen lds\n"
"s_add_u32 m0, s37, 0x8800\n"
"s_mov_b32 s3, 0x388000\n"
"buffer_load_dwordx4 v0, s[16:19], s3 offen lds\n"
"s_add_u32 m0, s37, 0x8c00\n"
"s_mov_b32 s3, 0x38c000\n"
"buffer_load_dwordx4 v1, s[16:19], s3 offen lds\n"
"ds_read_b64_tr_b16 a[72:73], v2 offset:49152\n"
"ds_read_b64_tr_b16 a[74:75], v2 offset:51200\n"
"ds_read_b64_tr_b16 a[200:201], v3 offset:49152\n"
"ds_read_b64_tr_b16 a[202:203], v3 offset:51200\n"
"ds_read_b64_tr_b16 a[76:77], v2 offset:57344\n"
"ds_read_b64_tr_b16 a[78:79], v2 offset:59392\n"
"ds_read_b64_tr_b16 a[204:205], v3 offset:57344\n"
"ds_read_b64_tr_b16 a[206:207], v3 offset:59392\n"
"s_waitcnt vmcnt(16) lgkmcnt(0)\n"
"s_barrier\n"
"s_add_u32 m0, s37, 0xc000\n"
"s_mov_b32 s3, 0x3c0000\n"
"buffer_load_dwordx4 v0, s[16:19], s3 offen lds\n"
"s_add_u32 m0, s37, 0xc400\n"
"s_mov_b32 s3, 0x3c4000\n"
"buffer_load_dwordx4 v1, s[16:19], s3 offen lds\n"
"s_add_u32 m0, s37, 0xc800\n"
"s_mov_b32 s3, 0x3c8000\n"
"buffer_load_dwordx4 v0, s[16:19], s3 offen lds\n"
"s_add_u32 m0, s37, 0xcc00\n"
"s_mov_b32 s3, 0x3cc000\n"
"buffer_load_dwordx4 v1, s[16:19], s3 offen lds\n"
"ds_read_b64_tr_b16 a[80:81], v5 offset:0\n"
"ds_read_b64_tr_b16 a[82:83], v5 offset:2048\n"
"ds_read_b64_tr_b16 a[208:209], v6 offset:0\n"
"ds_read_b64_tr_b16 a[210:211], v6 offset:2048\n"
"ds_read_b64_tr_b16 a[84:85], v5 offset:8192\n"
"ds_read_b64_tr_b16 a[86:87], v5 offset:10240\n"
"ds_read_b64_tr_b16 a[212:213], v6 offset:8192\n"
"ds_read_b64_tr_b16 a[214:215], v6 offset:10240\n"
"s_waitcnt vmcnt(16) lgkmcnt(0)\n"
"s_barrier\n"
"ds_read_b64_tr_b16 a[88:89], v5 offset:16384\n"
"ds_read_b64_tr_b16 a[90:91], v5 offset:18432\n"
"ds_read_b64_tr_b16 a[216:217], v6 offset:16384\n"
"ds_read_b64_tr_b16 a[218:219], v6 offset:18432\n"
"ds_read_b64_tr_b16 a[92:93], v5 offset:24576\n"
"ds_read_b64_tr_b16 a[94:95], v5 offset:26624\n"
"ds_read_b64_tr_b16 a[220:221], v6 offset:24576\n"
"ds_read_b64_tr_b16 a[222:223], v6 offset:26624\n"
"s_waitcnt vmcnt(12) lgkmcnt(0)\n"
"s_barrier\n"
"ds_read_b64_tr_b16 a[96:97], v2 offset:0\n"
"ds_read_b64_tr_b16 a[98:99], v2 offset:2048\n"
"ds_read_b64_tr_b16 a[224:225], v3 offset:0\n"
"ds_read_b64_tr_b16 a[226:227], v3 offset:2048\n"
"ds_read_b64_tr_b16 a[100:101], v2 offset:8192\n"
"ds_read_b64_tr_b16 a[102:103], v2 offset:10240\n"
"ds_read_b64_tr_b16 a[228:229], v3 offset:8192\n"
"ds_read_b64_tr_b16 a[230:231], v3 offset:10240\n"
"s_waitcnt vmcnt(8) lgkmcnt(0)\n"
"s_barrier\n"
"ds_read_b64_tr_b16 a[104:105], v2 offset:16384\n"
"ds_read_b64_tr_b16 a[106:107], v2 offset:18432\n"
"ds_read_b64_tr_b16 a[232:233], v3 offset:16384\n"
"ds_read_b64_tr_b16 a[234:235], v3 offset:18432\n"
"ds_read_b64_tr_b16 a[108:109], v2 offset:24576\n"
"ds_read_b64_tr_b16 a[110:111], v2 offset:26624\n"
"ds_read_b64_tr_b16 a[236:237], v3 offset:24576\n"
"ds_read_b64_tr_b16 a[238:239], v3 offset:26624\n"
"s_waitcnt vmcnt(4) lgkmcnt(0)\n"
"s_barrier\n"
"ds_read_b64_tr_b16 a[112:113], v2 offset:32768\n"
"ds_read_b64_tr_b16 a[114:115], v2 offset:34816\n"
"ds_read_b64_tr_b16 a[240:241], v3 offset:32768\n"
"ds_read_b64_tr_b16 a[242:243], v3 offset:34816\n"
"ds_read_b64_tr_b16 a[116:117], v2 offset:40960\n"
"ds_read_b64_tr_b16 a[118:119], v2 offset:43008\n"
"ds_read_b64_tr_b16 a[244:245], v3 offset:40960\n"
"ds_read_b64_tr_b16 a[246:247], v3 offset:43008\n"
"s_waitcnt vmcnt(0) lgkmcnt(0)\n"
"s_barrier\n"
"ds_read_b64_tr_b16 a[120:121], v2 offset:49152\n"
"ds_read_b64_tr_b16 a[122:123], v2 offset:51200\n"
"ds_read_b64_tr_b16 a[248:249], v3 offset:49152\n"
"ds_read_b64_tr_b16 a[250:251], v3 offset:51200\n"
"ds_read_b64_tr_b16 a[124:125], v2 offset:57344\n"
"ds_read_b64_tr_b16 a[126:127], v2 offset:59392\n"
"ds_read_b64_tr_b16 a[252:253], v3 offset:57344\n"
"ds_read_b64_tr_b16 a[254:255], v3 offset:59392\n"
"s_waitcnt lgkmcnt(0)\n"
"s_barrier\n"
"ds_read_b128 v[64:67], v120 offset:32768\n"
"ds_read_b128 v[68:71], v121 offset:32768\n"
"ds_read_b128 v[72:75], v122 offset:32768\n"
"ds_read_b128 v[76:79], v123 offset:32768\n"
"ds_read_b128 v[80:83], v124 offset:32768\n"
"ds_read_b128 v[84:87], v125 offset:32768\n"
".p2align 6\n"
"L_pv_loop:\n"
"s_waitcnt lgkmcnt(5)\n"
"v_mfma_f32_32x32x16_bf16 v[0:15], v[64:67], a[0:3], 0\n"
"ds_read_b128 v[88:91], v126 offset:32768\n"
"v_mfma_f32_32x32x16_bf16 v[16:31], v[64:67], a[128:131], 0\n"
"s_waitcnt lgkmcnt(5)\n"
"v_mfma_f32_32x32x16_bf16 v[0:15], v[68:71], a[4:7], v[0:15]\n"
"ds_read_b128 v[92:95], v127 offset:32768\n"
"v_mfma_f32_32x32x16_bf16 v[16:31], v[68:71], a[132:135], v[16:31]\n"
"s_waitcnt lgkmcnt(5)\n"
"v_mfma_f32_32x32x16_bf16 v[0:15], v[72:75], a[8:11], v[0:15]\n"
"ds_read_b128 v[64:67], v120 offset:33024\n"
"v_mfma_f32_32x32x16_bf16 v[16:31], v[72:75], a[136:139], v[16:31]\n"
"v_cvt_pk_bf16_f32 v96, v32, v33\n"
"v_cvt_pk_bf16_f32 v97, v34, v35\n"
"s_waitcnt lgkmcnt(5)\n"
"v_mfma_f32_32x32x16_bf16 v[0:15], v[76:79], a[12:15], v[0:15]\n"
"ds_read_b128 v[68:71], v121 offset:33024\n"
"v_cvt_pk_bf16_f32 v98, v36, v37\n"
"v_cvt_pk_bf16_f32 v99, v38, v39\n"
"v_mfma_f32_32x32x16_bf16 v[16:31], v[76:79], a[140:143], v[16:31]\n"
"buffer_store_dwordx4 v[96:99], v136, s[24:27], s28 offen offset:0 sc1\n"
"v_cvt_pk_bf16_f32 v100, v40, v41\n"
"s_waitcnt lgkmcnt(5)\n"
"v_mfma_f32_32x32x16_bf16 v[0:15], v[80:83], a[16:19], v[0:15]\n"
"ds_read_b128 v[72:75], v122 offset:33024\n"
"v_cvt_pk_bf16_f32 v101, v42, v43\n"
"v_cvt_pk_bf16_f32 v102, v44, v45\n"
"v_mfma_f32_32x32x16_bf16 v[16:31], v[80:83], a[144:147], v[16:31]\n"
"v_cvt_pk_bf16_f32 v103, v46, v47\n"
"buffer_store_dwordx4 v[100:103], v136, s[24:27], s28 offen offset:1024 sc1\n"
"s_waitcnt lgkmcnt(5)\n"
"v_mfma_f32_32x32x16_bf16 v[0:15], v[84:87], a[20:23], v[0:15]\n"
"ds_read_b128 v[76:79], v123 offset:33024\n"
"v_cvt_pk_bf16_f32 v104, v48, v49\n"
"v_cvt_pk_bf16_f32 v105, v50, v51\n"
"v_mfma_f32_32x32x16_bf16 v[16:31], v[84:87], a[148:151], v[16:31]\n"
"v_cvt_pk_bf16_f32 v106, v52, v53\n"
"v_cvt_pk_bf16_f32 v107, v54, v55\n"
"s_waitcnt lgkmcnt(5)\n"
"v_mfma_f32_32x32x16_bf16 v[0:15], v[88:91], a[24:27], v[0:15]\n"
"ds_read_b128 v[80:83], v124 offset:33024\n"
"buffer_store_dwordx4 v[104:107], v136, s[24:27], s28 offen offset:2048 sc1\n"
"v_cvt_pk_bf16_f32 v108, v56, v57\n"
"v_mfma_f32_32x32x16_bf16 v[16:31], v[88:91], a[152:155], v[16:31]\n"
"v_cvt_pk_bf16_f32 v109, v58, v59\n"
"v_cvt_pk_bf16_f32 v110, v60, v61\n"
"s_waitcnt lgkmcnt(5)\n"
"v_mfma_f32_32x32x16_bf16 v[0:15], v[92:95], a[28:31], v[0:15]\n"
"ds_read_b128 v[84:87], v125 offset:33024\n"
"v_cvt_pk_bf16_f32 v111, v62, v63\n"
"buffer_store_dwordx4 v[108:111], v136, s[24:27], s28 offen offset:3072 sc1\n"
"v_mfma_f32_32x32x16_bf16 v[16:31], v[92:95], a[156:159], v[16:31]\n"
"s_waitcnt lgkmcnt(5)\n"
"v_mfma_f32_32x32x16_bf16 v[0:15], v[64:67], a[32:35], v[0:15]\n"
"ds_read_b128 v[88:91], v126 offset:33024\n"
"v_mfma_f32_32x32x16_bf16 v[16:31], v[64:67], a[160:163], v[16:31]\n"
"s_waitcnt lgkmcnt(5)\n"
"v_mfma_f32_32x32x16_bf16 v[0:15], v[68:71], a[36:39], v[0:15]\n"
"ds_read_b128 v[92:95], v127 offset:33024\n"
"v_mfma_f32_32x32x16_bf16 v[16:31], v[68:71], a[164:167], v[16:31]\n"
"s_waitcnt lgkmcnt(5)\n"
"v_mfma_f32_32x32x16_bf16 v[0:15], v[72:75], a[40:43], v[0:15]\n"
"ds_read_b128 v[64:67], v120 offset:33280\n"
"v_mfma_f32_32x32x16_bf16 v[16:31], v[72:75], a[168:171], v[16:31]\n"
"s_waitcnt lgkmcnt(5)\n"
"v_mfma_f32_32x32x16_bf16 v[0:15], v[76:79], a[44:47], v[0:15]\n"
"ds_read_b128 v[68:71], v121 offset:33280\n"
"v_mfma_f32_32x32x16_bf16 v[16:31], v[76:79], a[172:175], v[16:31]\n"
"s_waitcnt lgkmcnt(5)\n"
"v_mfma_f32_32x32x16_bf16 v[0:15], v[80:83], a[48:51], v[0:15]\n"
"ds_read_b128 v[72:75], v122 offset:33280\n"
"v_mfma_f32_32x32x16_bf16 v[16:31], v[80:83], a[176:179], v[16:31]\n"
"s_waitcnt lgkmcnt(5)\n"
"v_mfma_f32_32x32x16_bf16 v[0:15], v[84:87], a[52:55], v[0:15]\n"
"ds_read_b128 v[76:79], v123 offset:33280\n"
"s_add_u32 m0, s35, 0x10000\n"
"s_add_u32 s36, s29, 0x0\n"
"buffer_load_dwordx4 v128, s[20:23], s36 offen offset:0 lds\n"
"v_mfma_f32_32x32x16_bf16 v[16:31], v[84:87], a[180:183], v[16:31]\n"
"s_waitcnt lgkmcnt(5)\n"
"v_mfma_f32_32x32x16_bf16 v[0:15], v[88:91], a[56:59], v[0:15]\n"
"ds_read_b128 v[80:83], v124 offset:33280\n"
"buffer_load_dwordx4 v129, s[20:23], s36 offen offset:1024 lds\n"
"v_mfma_f32_32x32x16_bf16 v[16:31], v[88:91], a[184:187], v[16:31]\n"
"s_waitcnt lgkmcnt(5)\n"
"v_mfma_f32_32x32x16_bf16 v[0:15], v[92:95], a[60:63], v[0:15]\n"
"ds_read_b128 v[84:87], v125 offset:33280\n"
"buffer_load_dwordx4 v130, s[20:23], s36 offen offset:2048 lds\n"
"v_mfma_f32_32x32x16_bf16 v[16:31], v[92:95], a[188:191], v[16:31]\n"
"s_waitcnt lgkmcnt(5)\n"
"v_mfma_f32_32x32x16_bf16 v[0:15], v[64:67], a[64:67], v[0:15]\n"
"ds_read_b128 v[88:91], v126 offset:33280\n"
"buffer_load_dwordx4 v131, s[20:23], s36 offen offset:3072 lds\n"
"v_mfma_f32_32x32x16_bf16 v[16:31], v[64:67], a[192:195], v[16:31]\n"
"s_waitcnt lgkmcnt(5)\n"
"v_mfma_f32_32x32x16_bf16 v[0:15], v[68:71], a[68:71], v[0:15]\n"
"ds_read_b128 v[92:95], v127 offset:33280\n"
"s_add_u32 m0, s35, 0x11000\n"
"s_add_u32 s36, s29, 0x1000\n"
"buffer_load_dwordx4 v132, s[20:23], s36 offen offset:0 lds\n"
"v_mfma_f32_32x32x16_bf16 v[16:31], v[68:71], a[196:199], v[16:31]\n"
"s_waitcnt lgkmcnt(5)\n"
"v_mfma_f32_32x32x16_bf16 v[0:15], v[72:75], a[72:75], v[0:15]\n"
"ds_read_b128 v[64:67], v120 offset:33536\n"
"buffer_load_dwordx4 v133, s[20:23], s36 offen offset:1024 lds\n"
"v_mfma_f32_32x32x16_bf16 v[16:31], v[72:75], a[200:203], v[16:31]\n"
"s_waitcnt lgkmcnt(5)\n"
"v_mfma_f32_32x32x16_bf16 v[0:15], v[76:79], a[76:79], v[0:15]\n"
"ds_read_b128 v[68:71], v121 offset:33536\n"
"buffer_load_dwordx4 v134, s[20:23], s36 offen offset:2048 lds\n"
"v_mfma_f32_32x32x16_bf16 v[16:31], v[76:79], a[204:207], v[16:31]\n"
"s_waitcnt lgkmcnt(5)\n"
"v_mfma_f32_32x32x16_bf16 v[0:15], v[80:83], a[80:83], v[0:15]\n"
"ds_read_b128 v[72:75], v122 offset:33536\n"
"buffer_load_dwordx4 v135, s[20:23], s36 offen offset:3072 lds\n"
"v_mfma_f32_32x32x16_bf16 v[16:31], v[80:83], a[208:211], v[16:31]\n"
"s_waitcnt lgkmcnt(5)\n"
"v_mfma_f32_32x32x16_bf16 v[0:15], v[84:87], a[84:87], v[0:15]\n"
"ds_read_b128 v[76:79], v123 offset:33536\n"
"v_mfma_f32_32x32x16_bf16 v[16:31], v[84:87], a[212:215], v[16:31]\n"
"s_add_u32 s29, s29, 0x8000\n"
"s_add_u32 s28, s28, 0x2000\n"
"s_waitcnt lgkmcnt(5)\n"
"v_mfma_f32_32x32x16_bf16 v[0:15], v[88:91], a[88:91], v[0:15]\n"
"ds_read_b128 v[80:83], v124 offset:33536\n"
"v_mov_b32 v96, s34\n"
"v_mfma_f32_32x32x16_bf16 v[16:31], v[88:91], a[216:219], v[16:31]\n"
"s_waitcnt lgkmcnt(5)\n"
"v_mfma_f32_32x32x16_bf16 v[0:15], v[92:95], a[92:95], v[0:15]\n"
"ds_read_b128 v[84:87], v125 offset:33536\n"
"s_add_u32 s34, s34, 1\n"
"v_mfma_f32_32x32x16_bf16 v[16:31], v[92:95], a[220:223], v[16:31]\n"
"s_waitcnt lgkmcnt(5)\n"
"v_mfma_f32_32x32x16_bf16 v[0:15], v[64:67], a[96:99], v[0:15]\n"
"ds_read_b128 v[88:91], v126 offset:33536\n"
"v_mfma_f32_32x32x16_bf16 v[16:31], v[64:67], a[224:227], v[16:31]\n"
"s_waitcnt lgkmcnt(5)\n"
"v_mfma_f32_32x32x16_bf16 v[0:15], v[68:71], a[100:103], v[0:15]\n"
"ds_read_b128 v[92:95], v127 offset:33536\n"
"v_mfma_f32_32x32x16_bf16 v[16:31], v[68:71], a[228:231], v[16:31]\n"
"s_waitcnt vmcnt(8)\n"
"s_barrier\n"
"s_waitcnt lgkmcnt(5)\n"
"v_mfma_f32_32x32x16_bf16 v[0:15], v[72:75], a[104:107], v[0:15]\n"
"ds_read_b128 v[64:67], v120 offset:0\n"
"v_mfma_f32_32x32x16_bf16 v[16:31], v[72:75], a[232:235], v[16:31]\n"
"s_mov_b64 exec, 1\n"
"buffer_store_dword v96, off, s[12:15], s31 sc1\n"
"s_mov_b64 exec, -1\n"
"s_waitcnt lgkmcnt(5)\n"
"v_mfma_f32_32x32x16_bf16 v[0:15], v[76:79], a[108:111], v[0:15]\n"
"ds_read_b128 v[68:71], v121 offset:0\n"
"v_mfma_f32_32x32x16_bf16 v[16:31], v[76:79], a[236:239], v[16:31]\n"
"s_waitcnt lgkmcnt(5)\n"
"v_mfma_f32_32x32x16_bf16 v[0:15], v[80:83], a[112:115], v[0:15]\n"
"ds_read_b128 v[72:75], v122 offset:0\n"
"v_mfma_f32_32x32x16_bf16 v[16:31], v[80:83], a[240:243], v[16:31]\n"
"s_waitcnt lgkmcnt(5)\n"
"v_mfma_f32_32x32x16_bf16 v[0:15], v[84:87], a[116:119], v[0:15]\n"
"ds_read_b128 v[76:79], v123 offset:0\n"
"v_mfma_f32_32x32x16_bf16 v[16:31], v[84:87], a[244:247], v[16:31]\n"
"s_waitcnt lgkmcnt(5)\n"
"v_mfma_f32_32x32x16_bf16 v[0:15], v[88:91], a[120:123], v[0:15]\n"
"ds_read_b128 v[80:83], v124 offset:0\n"
"v_mfma_f32_32x32x16_bf16 v[16:31], v[88:91], a[248:251], v[16:31]\n"
"s_waitcnt lgkmcnt(5)\n"
"v_mfma_f32_32x32x16_bf16 v[0:15], v[92:95], a[124:127], v[0:15]\n"
"ds_read_b128 v[84:87], v125 offset:0\n"
"v_mfma_f32_32x32x16_bf16 v[16:31], v[92:95], a[252:255], v[16:31]\n"
"s_waitcnt lgkmcnt(5)\n"
"v_mfma_f32_32x32x16_bf16 v[32:47], v[64:67], a[0:3], 0\n"
"ds_read_b128 v[88:91], v126 offset:0\n"
"v_mfma_f32_32x32x16_bf16 v[48:63], v[64:67], a[128:131], 0\n"
"s_waitcnt lgkmcnt(5)\n"
"v_mfma_f32_32x32x16_bf16 v[32:47], v[68:71], a[4:7], v[32:47]\n"
"ds_read_b128 v[92:95], v127 offset:0\n"
"v_mfma_f32_32x32x16_bf16 v[48:63], v[68:71], a[132:135], v[48:63]\n"
"s_waitcnt lgkmcnt(5)\n"
"v_mfma_f32_32x32x16_bf16 v[32:47], v[72:75], a[8:11], v[32:47]\n"
"ds_read_b128 v[64:67], v120 offset:256\n"
"v_mfma_f32_32x32x16_bf16 v[48:63], v[72:75], a[136:139], v[48:63]\n"
"v_cvt_pk_bf16_f32 v96, v0, v1\n"
"v_cvt_pk_bf16_f32 v97, v2, v3\n"
"s_waitcnt lgkmcnt(5)\n"
"v_mfma_f32_32x32x16_bf16 v[32:47], v[76:79], a[12:15], v[32:47]\n"
"ds_read_b128 v[68:71], v121 offset:256\n"
"v_cvt_pk_bf16_f32 v98, v4, v5\n"
"v_cvt_pk_bf16_f32 v99, v6, v7\n"
"v_mfma_f32_32x32x16_bf16 v[48:63], v[76:79], a[140:143], v[48:63]\n"
"buffer_store_dwordx4 v[96:99], v136, s[24:27], s28 offen offset:0 sc1\n"
"v_cvt_pk_bf16_f32 v100, v8, v9\n"
"s_waitcnt lgkmcnt(5)\n"
"v_mfma_f32_32x32x16_bf16 v[32:47], v[80:83], a[16:19], v[32:47]\n"
"ds_read_b128 v[72:75], v122 offset:256\n"
"v_cvt_pk_bf16_f32 v101, v10, v11\n"
"v_cvt_pk_bf16_f32 v102, v12, v13\n"
"v_mfma_f32_32x32x16_bf16 v[48:63], v[80:83], a[144:147], v[48:63]\n"
"v_cvt_pk_bf16_f32 v103, v14, v15\n"
"buffer_store_dwordx4 v[100:103], v136, s[24:27], s28 offen offset:1024 sc1\n"
"s_waitcnt lgkmcnt(5)\n"
"v_mfma_f32_32x32x16_bf16 v[32:47], v[84:87], a[20:23], v[32:47]\n"
"ds_read_b128 v[76:79], v123 offset:256\n"
"v_cvt_pk_bf16_f32 v104, v16, v17\n"
"v_cvt_pk_bf16_f32 v105, v18, v19\n"
"v_mfma_f32_32x32x16_bf16 v[48:63], v[84:87], a[148:151], v[48:63]\n"
"v_cvt_pk_bf16_f32 v106, v20, v21\n"
"v_cvt_pk_bf16_f32 v107, v22, v23\n"
"s_waitcnt lgkmcnt(5)\n"
"v_mfma_f32_32x32x16_bf16 v[32:47], v[88:91], a[24:27], v[32:47]\n"
"ds_read_b128 v[80:83], v124 offset:256\n"
"buffer_store_dwordx4 v[104:107], v136, s[24:27], s28 offen offset:2048 sc1\n"
"v_cvt_pk_bf16_f32 v108, v24, v25\n"
"v_mfma_f32_32x32x16_bf16 v[48:63], v[88:91], a[152:155], v[48:63]\n"
"v_cvt_pk_bf16_f32 v109, v26, v27\n"
"v_cvt_pk_bf16_f32 v110, v28, v29\n"
"s_waitcnt lgkmcnt(5)\n"
"v_mfma_f32_32x32x16_bf16 v[32:47], v[92:95], a[28:31], v[32:47]\n"
"ds_read_b128 v[84:87], v125 offset:256\n"
"v_cvt_pk_bf16_f32 v111, v30, v31\n"
"buffer_store_dwordx4 v[108:111], v136, s[24:27], s28 offen offset:3072 sc1\n"
"v_mfma_f32_32x32x16_bf16 v[48:63], v[92:95], a[156:159], v[48:63]\n"
"s_waitcnt lgkmcnt(5)\n"
"v_mfma_f32_32x32x16_bf16 v[32:47], v[64:67], a[32:35], v[32:47]\n"
"ds_read_b128 v[88:91], v126 offset:256\n"
"v_mfma_f32_32x32x16_bf16 v[48:63], v[64:67], a[160:163], v[48:63]\n"
"s_waitcnt lgkmcnt(5)\n"
"v_mfma_f32_32x32x16_bf16 v[32:47], v[68:71], a[36:39], v[32:47]\n"
"ds_read_b128 v[92:95], v127 offset:256\n"
"v_mfma_f32_32x32x16_bf16 v[48:63], v[68:71], a[164:167], v[48:63]\n"
"s_waitcnt lgkmcnt(5)\n"
"v_mfma_f32_32x32x16_bf16 v[32:47], v[72:75], a[40:43], v[32:47]\n"
"ds_read_b128 v[64:67], v120 offset:512\n"
"v_mfma_f32_32x32x16_bf16 v[48:63], v[72:75], a[168:171], v[48:63]\n"
"s_waitcnt lgkmcnt(5)\n"
"v_mfma_f32_32x32x16_bf16 v[32:47], v[76:79], a[44:47], v[32:47]\n"
"ds_read_b128 v[68:71], v121 offset:512\n"
"v_mfma_f32_32x32x16_bf16 v[48:63], v[76:79], a[172:175], v[48:63]\n"
"s_waitcnt lgkmcnt(5)\n"
"v_mfma_f32_32x32x16_bf16 v[32:47], v[80:83], a[48:51], v[32:47]\n"
"ds_read_b128 v[72:75], v122 offset:512\n"
"v_mfma_f32_32x32x16_bf16 v[48:63], v[80:83], a[176:179], v[48:63]\n"
"s_waitcnt lgkmcnt(5)\n"
"v_mfma_f32_32x32x16_bf16 v[32:47], v[84:87], a[52:55], v[32:47]\n"
"ds_read_b128 v[76:79], v123 offset:512\n"
"s_add_u32 m0, s35, 0x8000\n"
"s_add_u32 s36, s29, 0x0\n"
"buffer_load_dwordx4 v128, s[20:23], s36 offen offset:0 lds\n"
"v_mfma_f32_32x32x16_bf16 v[48:63], v[84:87], a[180:183], v[48:63]\n"
"s_waitcnt lgkmcnt(5)\n"
"v_mfma_f32_32x32x16_bf16 v[32:47], v[88:91], a[56:59], v[32:47]\n"
"ds_read_b128 v[80:83], v124 offset:512\n"
"buffer_load_dwordx4 v129, s[20:23], s36 offen offset:1024 lds\n"
"v_mfma_f32_32x32x16_bf16 v[48:63], v[88:91], a[184:187], v[48:63]\n"
"s_waitcnt lgkmcnt(5)\n"
"v_mfma_f32_32x32x16_bf16 v[32:47], v[92:95], a[60:63], v[32:47]\n"
"ds_read_b128 v[84:87], v125 offset:512\n"
"buffer_load_dwordx4 v130, s[20:23], s36 offen offset:2048 lds\n"
"v_mfma_f32_32x32x16_bf16 v[48:63], v[92:95], a[188:191], v[48:63]\n"
"s_waitcnt lgkmcnt(5)\n"
"v_mfma_f32_32x32x16_bf16 v[32:47], v[64:67], a[64:67], v[32:47]\n"
"ds_read_b128 v[88:91], v126 offset:512\n"
"buffer_load_dwordx4 v131, s[20:23], s36 offen offset:3072 lds\n"
"v_mfma_f32_32x32x16_bf16 v[48:63], v[64:67], a[192:195], v[48:63]\n"
"s_waitcnt lgkmcnt(5)\n"
"v_mfma_f32_32x32x16_bf16 v[32:47], v[68:71], a[68:71], v[32:47]\n"
"ds_read_b128 v[92:95], v127 offset:512\n"
"s_add_u32 m0, s35, 0x9000\n"
"s_add_u32 s36, s29, 0x1000\n"
"buffer_load_dwordx4 v132, s[20:23], s36 offen offset:0 lds\n"
"v_mfma_f32_32x32x16_bf16 v[48:63], v[68:71], a[196:199], v[48:63]\n"
"s_waitcnt lgkmcnt(5)\n"
"v_mfma_f32_32x32x16_bf16 v[32:47], v[72:75], a[72:75], v[32:47]\n"
"ds_read_b128 v[64:67], v120 offset:768\n"
"buffer_load_dwordx4 v133, s[20:23], s36 offen offset:1024 lds\n"
"v_mfma_f32_32x32x16_bf16 v[48:63], v[72:75], a[200:203], v[48:63]\n"
"s_waitcnt lgkmcnt(5)\n"
"v_mfma_f32_32x32x16_bf16 v[32:47], v[76:79], a[76:79], v[32:47]\n"
"ds_read_b128 v[68:71], v121 offset:768\n"
"buffer_load_dwordx4 v134, s[20:23], s36 offen offset:2048 lds\n"
"v_mfma_f32_32x32x16_bf16 v[48:63], v[76:79], a[204:207], v[48:63]\n"
"s_waitcnt lgkmcnt(5)\n"
"v_mfma_f32_32x32x16_bf16 v[32:47], v[80:83], a[80:83], v[32:47]\n"
"ds_read_b128 v[72:75], v122 offset:768\n"
"buffer_load_dwordx4 v135, s[20:23], s36 offen offset:3072 lds\n"
"v_mfma_f32_32x32x16_bf16 v[48:63], v[80:83], a[208:211], v[48:63]\n"
"s_waitcnt lgkmcnt(5)\n"
"v_mfma_f32_32x32x16_bf16 v[32:47], v[84:87], a[84:87], v[32:47]\n"
"ds_read_b128 v[76:79], v123 offset:768\n"
"v_mfma_f32_32x32x16_bf16 v[48:63], v[84:87], a[212:215], v[48:63]\n"
"s_add_u32 s29, s29, 0x8000\n"
"s_add_u32 s28, s28, 0x2000\n"
"s_waitcnt lgkmcnt(5)\n"
"v_mfma_f32_32x32x16_bf16 v[32:47], v[88:91], a[88:91], v[32:47]\n"
"ds_read_b128 v[80:83], v124 offset:768\n"
"v_mov_b32 v96, s34\n"
"v_mfma_f32_32x32x16_bf16 v[48:63], v[88:91], a[216:219], v[48:63]\n"
"s_waitcnt lgkmcnt(5)\n"
"v_mfma_f32_32x32x16_bf16 v[32:47], v[92:95], a[92:95], v[32:47]\n"
"ds_read_b128 v[84:87], v125 offset:768\n"
"s_add_u32 s34, s34, 1\n"
"v_mfma_f32_32x32x16_bf16 v[48:63], v[92:95], a[220:223], v[48:63]\n"
"s_waitcnt lgkmcnt(5)\n"
"v_mfma_f32_32x32x16_bf16 v[32:47], v[64:67], a[96:99], v[32:47]\n"
"ds_read_b128 v[88:91], v126 offset:768\n"
"v_mfma_f32_32x32x16_bf16 v[48:63], v[64:67], a[224:227], v[48:63]\n"
"s_waitcnt lgkmcnt(5)\n"
"v_mfma_f32_32x32x16_bf16 v[32:47], v[68:71], a[100:103], v[32:47]\n"
"ds_read_b128 v[92:95], v127 offset:768\n"
"v_mfma_f32_32x32x16_bf16 v[48:63], v[68:71], a[228:231], v[48:63]\n"
"s_waitcnt vmcnt(8)\n"
"s_barrier\n"
"s_waitcnt lgkmcnt(5)\n"
"v_mfma_f32_32x32x16_bf16 v[32:47], v[72:75], a[104:107], v[32:47]\n"
"ds_read_b128 v[64:67], v112 offset:32768\n"
"v_mfma_f32_32x32x16_bf16 v[48:63], v[72:75], a[232:235], v[48:63]\n"
"s_mov_b64 exec, 1\n"
"buffer_store_dword v96, off, s[12:15], s31 sc1\n"
"s_mov_b64 exec, -1\n"
"s_waitcnt lgkmcnt(5)\n"
"v_mfma_f32_32x32x16_bf16 v[32:47], v[76:79], a[108:111], v[32:47]\n"
"ds_read_b128 v[68:71], v113 offset:32768\n"
"v_mfma_f32_32x32x16_bf16 v[48:63], v[76:79], a[236:239], v[48:63]\n"
"s_waitcnt lgkmcnt(5)\n"
"v_mfma_f32_32x32x16_bf16 v[32:47], v[80:83], a[112:115], v[32:47]\n"
"ds_read_b128 v[72:75], v114 offset:32768\n"
"v_mfma_f32_32x32x16_bf16 v[48:63], v[80:83], a[240:243], v[48:63]\n"
"s_waitcnt lgkmcnt(5)\n"
"v_mfma_f32_32x32x16_bf16 v[32:47], v[84:87], a[116:119], v[32:47]\n"
"ds_read_b128 v[76:79], v115 offset:32768\n"
"v_mfma_f32_32x32x16_bf16 v[48:63], v[84:87], a[244:247], v[48:63]\n"
"s_waitcnt lgkmcnt(5)\n"
"v_mfma_f32_32x32x16_bf16 v[32:47], v[88:91], a[120:123], v[32:47]\n"
"ds_read_b128 v[80:83], v116 offset:32768\n"
"v_mfma_f32_32x32x16_bf16 v[48:63], v[88:91], a[248:251], v[48:63]\n"
"s_waitcnt lgkmcnt(5)\n"
"v_mfma_f32_32x32x16_bf16 v[32:47], v[92:95], a[124:127], v[32:47]\n"
"ds_read_b128 v[84:87], v117 offset:32768\n"
"v_mfma_f32_32x32x16_bf16 v[48:63], v[92:95], a[252:255], v[48:63]\n"
"s_waitcnt lgkmcnt(5)\n"
"v_mfma_f32_32x32x16_bf16 v[0:15], v[64:67], a[0:3], 0\n"
"ds_read_b128 v[88:91], v118 offset:32768\n"
"v_mfma_f32_32x32x16_bf16 v[16:31], v[64:67], a[128:131], 0\n"
"s_waitcnt lgkmcnt(5)\n"
"v_mfma_f32_32x32x16_bf16 v[0:15], v[68:71], a[4:7], v[0:15]\n"
"ds_read_b128 v[92:95], v119 offset:32768\n"
"v_mfma_f32_32x32x16_bf16 v[16:31], v[68:71], a[132:135], v[16:31]\n"
"s_waitcnt lgkmcnt(5)\n"
"v_mfma_f32_32x32x16_bf16 v[0:15], v[72:75], a[8:11], v[0:15]\n"
"ds_read_b128 v[64:67], v112 offset:33024\n"
"v_mfma_f32_32x32x16_bf16 v[16:31], v[72:75], a[136:139], v[16:31]\n"
"v_cvt_pk_bf16_f32 v96, v32, v33\n"
"v_cvt_pk_bf16_f32 v97, v34, v35\n"
"s_waitcnt lgkmcnt(5)\n"
"v_mfma_f32_32x32x16_bf16 v[0:15], v[76:79], a[12:15], v[0:15]\n"
"ds_read_b128 v[68:71], v113 offset:33024\n"
"v_cvt_pk_bf16_f32 v98, v36, v37\n"
"v_cvt_pk_bf16_f32 v99, v38, v39\n"
"v_mfma_f32_32x32x16_bf16 v[16:31], v[76:79], a[140:143], v[16:31]\n"
"buffer_store_dwordx4 v[96:99], v136, s[24:27], s28 offen offset:0 sc1\n"
"v_cvt_pk_bf16_f32 v100, v40, v41\n"
"s_waitcnt lgkmcnt(5)\n"
"v_mfma_f32_32x32x16_bf16 v[0:15], v[80:83], a[16:19], v[0:15]\n"
"ds_read_b128 v[72:75], v114 offset:33024\n"
"v_cvt_pk_bf16_f32 v101, v42, v43\n"
"v_cvt_pk_bf16_f32 v102, v44, v45\n"
"v_mfma_f32_32x32x16_bf16 v[16:31], v[80:83], a[144:147], v[16:31]\n"
"v_cvt_pk_bf16_f32 v103, v46, v47\n"
"buffer_store_dwordx4 v[100:103], v136, s[24:27], s28 offen offset:1024 sc1\n"
"s_waitcnt lgkmcnt(5)\n"
"v_mfma_f32_32x32x16_bf16 v[0:15], v[84:87], a[20:23], v[0:15]\n"
"ds_read_b128 v[76:79], v115 offset:33024\n"
"v_cvt_pk_bf16_f32 v104, v48, v49\n"
"v_cvt_pk_bf16_f32 v105, v50, v51\n"
"v_mfma_f32_32x32x16_bf16 v[16:31], v[84:87], a[148:151], v[16:31]\n"
"v_cvt_pk_bf16_f32 v106, v52, v53\n"
"v_cvt_pk_bf16_f32 v107, v54, v55\n"
"s_waitcnt lgkmcnt(5)\n"
"v_mfma_f32_32x32x16_bf16 v[0:15], v[88:91], a[24:27], v[0:15]\n"
"ds_read_b128 v[80:83], v116 offset:33024\n"
"buffer_store_dwordx4 v[104:107], v136, s[24:27], s28 offen offset:2048 sc1\n"
"v_cvt_pk_bf16_f32 v108, v56, v57\n"
"v_mfma_f32_32x32x16_bf16 v[16:31], v[88:91], a[152:155], v[16:31]\n"
"v_cvt_pk_bf16_f32 v109, v58, v59\n"
"v_cvt_pk_bf16_f32 v110, v60, v61\n"
"s_waitcnt lgkmcnt(5)\n"
"v_mfma_f32_32x32x16_bf16 v[0:15], v[92:95], a[28:31], v[0:15]\n"
"ds_read_b128 v[84:87], v117 offset:33024\n"
"v_cvt_pk_bf16_f32 v111, v62, v63\n"
"buffer_store_dwordx4 v[108:111], v136, s[24:27], s28 offen offset:3072 sc1\n"
"v_mfma_f32_32x32x16_bf16 v[16:31], v[92:95], a[156:159], v[16:31]\n"
"s_waitcnt lgkmcnt(5)\n"
"v_mfma_f32_32x32x16_bf16 v[0:15], v[64:67], a[32:35], v[0:15]\n"
"ds_read_b128 v[88:91], v118 offset:33024\n"
"v_mfma_f32_32x32x16_bf16 v[16:31], v[64:67], a[160:163], v[16:31]\n"
"s_waitcnt lgkmcnt(5)\n"
"v_mfma_f32_32x32x16_bf16 v[0:15], v[68:71], a[36:39], v[0:15]\n"
"ds_read_b128 v[92:95], v119 offset:33024\n"
"v_mfma_f32_32x32x16_bf16 v[16:31], v[68:71], a[164:167], v[16:31]\n"
"s_waitcnt lgkmcnt(5)\n"
"v_mfma_f32_32x32x16_bf16 v[0:15], v[72:75], a[40:43], v[0:15]\n"
"ds_read_b128 v[64:67], v112 offset:33280\n"
"v_mfma_f32_32x32x16_bf16 v[16:31], v[72:75], a[168:171], v[16:31]\n"
"s_waitcnt lgkmcnt(5)\n"
"v_mfma_f32_32x32x16_bf16 v[0:15], v[76:79], a[44:47], v[0:15]\n"
"ds_read_b128 v[68:71], v113 offset:33280\n"
"v_mfma_f32_32x32x16_bf16 v[16:31], v[76:79], a[172:175], v[16:31]\n"
"s_waitcnt lgkmcnt(5)\n"
"v_mfma_f32_32x32x16_bf16 v[0:15], v[80:83], a[48:51], v[0:15]\n"
"ds_read_b128 v[72:75], v114 offset:33280\n"
"v_mfma_f32_32x32x16_bf16 v[16:31], v[80:83], a[176:179], v[16:31]\n"
"s_waitcnt lgkmcnt(5)\n"
"v_mfma_f32_32x32x16_bf16 v[0:15], v[84:87], a[52:55], v[0:15]\n"
"ds_read_b128 v[76:79], v115 offset:33280\n"
"s_add_u32 m0, s35, 0x20000\n"
"s_add_u32 s36, s29, 0x0\n"
"buffer_load_dwordx4 v128, s[20:23], s36 offen offset:0 lds\n"
"v_mfma_f32_32x32x16_bf16 v[16:31], v[84:87], a[180:183], v[16:31]\n"
"s_waitcnt lgkmcnt(5)\n"
"v_mfma_f32_32x32x16_bf16 v[0:15], v[88:91], a[56:59], v[0:15]\n"
"ds_read_b128 v[80:83], v116 offset:33280\n"
"buffer_load_dwordx4 v129, s[20:23], s36 offen offset:1024 lds\n"
"v_mfma_f32_32x32x16_bf16 v[16:31], v[88:91], a[184:187], v[16:31]\n"
"s_waitcnt lgkmcnt(5)\n"
"v_mfma_f32_32x32x16_bf16 v[0:15], v[92:95], a[60:63], v[0:15]\n"
"ds_read_b128 v[84:87], v117 offset:33280\n"
"buffer_load_dwordx4 v130, s[20:23], s36 offen offset:2048 lds\n"
"v_mfma_f32_32x32x16_bf16 v[16:31], v[92:95], a[188:191], v[16:31]\n"
"s_waitcnt lgkmcnt(5)\n"
"v_mfma_f32_32x32x16_bf16 v[0:15], v[64:67], a[64:67], v[0:15]\n"
"ds_read_b128 v[88:91], v118 offset:33280\n"
"buffer_load_dwordx4 v131, s[20:23], s36 offen offset:3072 lds\n"
"v_mfma_f32_32x32x16_bf16 v[16:31], v[64:67], a[192:195], v[16:31]\n"
"s_waitcnt lgkmcnt(5)\n"
"v_mfma_f32_32x32x16_bf16 v[0:15], v[68:71], a[68:71], v[0:15]\n"
"ds_read_b128 v[92:95], v119 offset:33280\n"
"s_add_u32 m0, s35, 0x21000\n"
"s_add_u32 s36, s29, 0x1000\n"
"buffer_load_dwordx4 v132, s[20:23], s36 offen offset:0 lds\n"
"v_mfma_f32_32x32x16_bf16 v[16:31], v[68:71], a[196:199], v[16:31]\n"
"s_waitcnt lgkmcnt(5)\n"
"v_mfma_f32_32x32x16_bf16 v[0:15], v[72:75], a[72:75], v[0:15]\n"
"ds_read_b128 v[64:67], v112 offset:33536\n"
"buffer_load_dwordx4 v133, s[20:23], s36 offen offset:1024 lds\n"
"v_mfma_f32_32x32x16_bf16 v[16:31], v[72:75], a[200:203], v[16:31]\n"
"s_waitcnt lgkmcnt(5)\n"
"v_mfma_f32_32x32x16_bf16 v[0:15], v[76:79], a[76:79], v[0:15]\n"
"ds_read_b128 v[68:71], v113 offset:33536\n"
"buffer_load_dwordx4 v134, s[20:23], s36 offen offset:2048 lds\n"
"v_mfma_f32_32x32x16_bf16 v[16:31], v[76:79], a[204:207], v[16:31]\n"
"s_waitcnt lgkmcnt(5)\n"
"v_mfma_f32_32x32x16_bf16 v[0:15], v[80:83], a[80:83], v[0:15]\n"
"ds_read_b128 v[72:75], v114 offset:33536\n"
"buffer_load_dwordx4 v135, s[20:23], s36 offen offset:3072 lds\n"
"v_mfma_f32_32x32x16_bf16 v[16:31], v[80:83], a[208:211], v[16:31]\n"
"s_waitcnt lgkmcnt(5)\n"
"v_mfma_f32_32x32x16_bf16 v[0:15], v[84:87], a[84:87], v[0:15]\n"
"ds_read_b128 v[76:79], v115 offset:33536\n"
"v_mfma_f32_32x32x16_bf16 v[16:31], v[84:87], a[212:215], v[16:31]\n"
"s_add_u32 s29, s29, 0x8000\n"
"s_add_u32 s28, s28, 0x2000\n"
"s_waitcnt lgkmcnt(5)\n"
"v_mfma_f32_32x32x16_bf16 v[0:15], v[88:91], a[88:91], v[0:15]\n"
"ds_read_b128 v[80:83], v116 offset:33536\n"
"v_mov_b32 v96, s34\n"
"v_mfma_f32_32x32x16_bf16 v[16:31], v[88:91], a[216:219], v[16:31]\n"
"s_waitcnt lgkmcnt(5)\n"
"v_mfma_f32_32x32x16_bf16 v[0:15], v[92:95], a[92:95], v[0:15]\n"
"ds_read_b128 v[84:87], v117 offset:33536\n"
"s_add_u32 s34, s34, 1\n"
"v_mfma_f32_32x32x16_bf16 v[16:31], v[92:95], a[220:223], v[16:31]\n"
"s_waitcnt lgkmcnt(5)\n"
"v_mfma_f32_32x32x16_bf16 v[0:15], v[64:67], a[96:99], v[0:15]\n"
"ds_read_b128 v[88:91], v118 offset:33536\n"
"v_mfma_f32_32x32x16_bf16 v[16:31], v[64:67], a[224:227], v[16:31]\n"
"s_waitcnt lgkmcnt(5)\n"
"v_mfma_f32_32x32x16_bf16 v[0:15], v[68:71], a[100:103], v[0:15]\n"
"ds_read_b128 v[92:95], v119 offset:33536\n"
"v_mfma_f32_32x32x16_bf16 v[16:31], v[68:71], a[228:231], v[16:31]\n"
"s_waitcnt vmcnt(8)\n"
"s_barrier\n"
"s_waitcnt lgkmcnt(5)\n"
"v_mfma_f32_32x32x16_bf16 v[0:15], v[72:75], a[104:107], v[0:15]\n"
"ds_read_b128 v[64:67], v112 offset:0\n"
"v_mfma_f32_32x32x16_bf16 v[16:31], v[72:75], a[232:235], v[16:31]\n"
"s_mov_b64 exec, 1\n"
"buffer_store_dword v96, off, s[12:15], s31 sc1\n"
"s_mov_b64 exec, -1\n"
"s_waitcnt lgkmcnt(5)\n"
"v_mfma_f32_32x32x16_bf16 v[0:15], v[76:79], a[108:111], v[0:15]\n"
"ds_read_b128 v[68:71], v113 offset:0\n"
"v_mfma_f32_32x32x16_bf16 v[16:31], v[76:79], a[236:239], v[16:31]\n"
"s_waitcnt lgkmcnt(5)\n"
"v_mfma_f32_32x32x16_bf16 v[0:15], v[80:83], a[112:115], v[0:15]\n"
"ds_read_b128 v[72:75], v114 offset:0\n"
"v_mfma_f32_32x32x16_bf16 v[16:31], v[80:83], a[240:243], v[16:31]\n"
"s_waitcnt lgkmcnt(5)\n"
"v_mfma_f32_32x32x16_bf16 v[0:15], v[84:87], a[116:119], v[0:15]\n"
"ds_read_b128 v[76:79], v115 offset:0\n"
"v_mfma_f32_32x32x16_bf16 v[16:31], v[84:87], a[244:247], v[16:31]\n"
"s_waitcnt lgkmcnt(5)\n"
"v_mfma_f32_32x32x16_bf16 v[0:15], v[88:91], a[120:123], v[0:15]\n"
"ds_read_b128 v[80:83], v116 offset:0\n"
"v_mfma_f32_32x32x16_bf16 v[16:31], v[88:91], a[248:251], v[16:31]\n"
"s_waitcnt lgkmcnt(5)\n"
"v_mfma_f32_32x32x16_bf16 v[0:15], v[92:95], a[124:127], v[0:15]\n"
"ds_read_b128 v[84:87], v117 offset:0\n"
"v_mfma_f32_32x32x16_bf16 v[16:31], v[92:95], a[252:255], v[16:31]\n"
"s_waitcnt lgkmcnt(5)\n"
"v_mfma_f32_32x32x16_bf16 v[32:47], v[64:67], a[0:3], 0\n"
"ds_read_b128 v[88:91], v118 offset:0\n"
"v_mfma_f32_32x32x16_bf16 v[48:63], v[64:67], a[128:131], 0\n"
"s_waitcnt lgkmcnt(5)\n"
"v_mfma_f32_32x32x16_bf16 v[32:47], v[68:71], a[4:7], v[32:47]\n"
"ds_read_b128 v[92:95], v119 offset:0\n"
"v_mfma_f32_32x32x16_bf16 v[48:63], v[68:71], a[132:135], v[48:63]\n"
"s_waitcnt lgkmcnt(5)\n"
"v_mfma_f32_32x32x16_bf16 v[32:47], v[72:75], a[8:11], v[32:47]\n"
"ds_read_b128 v[64:67], v112 offset:256\n"
"v_mfma_f32_32x32x16_bf16 v[48:63], v[72:75], a[136:139], v[48:63]\n"
"v_cvt_pk_bf16_f32 v96, v0, v1\n"
"v_cvt_pk_bf16_f32 v97, v2, v3\n"
"s_waitcnt lgkmcnt(5)\n"
"v_mfma_f32_32x32x16_bf16 v[32:47], v[76:79], a[12:15], v[32:47]\n"
"ds_read_b128 v[68:71], v113 offset:256\n"
"v_cvt_pk_bf16_f32 v98, v4, v5\n"
"v_cvt_pk_bf16_f32 v99, v6, v7\n"
"v_mfma_f32_32x32x16_bf16 v[48:63], v[76:79], a[140:143], v[48:63]\n"
"buffer_store_dwordx4 v[96:99], v136, s[24:27], s28 offen offset:0 sc1\n"
"v_cvt_pk_bf16_f32 v100, v8, v9\n"
"s_waitcnt lgkmcnt(5)\n"
"v_mfma_f32_32x32x16_bf16 v[32:47], v[80:83], a[16:19], v[32:47]\n"
"ds_read_b128 v[72:75], v114 offset:256\n"
"v_cvt_pk_bf16_f32 v101, v10, v11\n"
"v_cvt_pk_bf16_f32 v102, v12, v13\n"
"v_mfma_f32_32x32x16_bf16 v[48:63], v[80:83], a[144:147], v[48:63]\n"
"v_cvt_pk_bf16_f32 v103, v14, v15\n"
"buffer_store_dwordx4 v[100:103], v136, s[24:27], s28 offen offset:1024 sc1\n"
"s_waitcnt lgkmcnt(5)\n"
"v_mfma_f32_32x32x16_bf16 v[32:47], v[84:87], a[20:23], v[32:47]\n"
"ds_read_b128 v[76:79], v115 offset:256\n"
"v_cvt_pk_bf16_f32 v104, v16, v17\n"
"v_cvt_pk_bf16_f32 v105, v18, v19\n"
"v_mfma_f32_32x32x16_bf16 v[48:63], v[84:87], a[148:151], v[48:63]\n"
"v_cvt_pk_bf16_f32 v106, v20, v21\n"
"v_cvt_pk_bf16_f32 v107, v22, v23\n"
"s_waitcnt lgkmcnt(5)\n"
"v_mfma_f32_32x32x16_bf16 v[32:47], v[88:91], a[24:27], v[32:47]\n"
"ds_read_b128 v[80:83], v116 offset:256\n"
"buffer_store_dwordx4 v[104:107], v136, s[24:27], s28 offen offset:2048 sc1\n"
"v_cvt_pk_bf16_f32 v108, v24, v25\n"
"v_mfma_f32_32x32x16_bf16 v[48:63], v[88:91], a[152:155], v[48:63]\n"
"v_cvt_pk_bf16_f32 v109, v26, v27\n"
"v_cvt_pk_bf16_f32 v110, v28, v29\n"
"s_waitcnt lgkmcnt(5)\n"
"v_mfma_f32_32x32x16_bf16 v[32:47], v[92:95], a[28:31], v[32:47]\n"
"ds_read_b128 v[84:87], v117 offset:256\n"
"v_cvt_pk_bf16_f32 v111, v30, v31\n"
"buffer_store_dwordx4 v[108:111], v136, s[24:27], s28 offen offset:3072 sc1\n"
"v_mfma_f32_32x32x16_bf16 v[48:63], v[92:95], a[156:159], v[48:63]\n"
"s_waitcnt lgkmcnt(5)\n"
"v_mfma_f32_32x32x16_bf16 v[32:47], v[64:67], a[32:35], v[32:47]\n"
"ds_read_b128 v[88:91], v118 offset:256\n"
"v_mfma_f32_32x32x16_bf16 v[48:63], v[64:67], a[160:163], v[48:63]\n"
"s_waitcnt lgkmcnt(5)\n"
"v_mfma_f32_32x32x16_bf16 v[32:47], v[68:71], a[36:39], v[32:47]\n"
"ds_read_b128 v[92:95], v119 offset:256\n"
"v_mfma_f32_32x32x16_bf16 v[48:63], v[68:71], a[164:167], v[48:63]\n"
"s_waitcnt lgkmcnt(5)\n"
"v_mfma_f32_32x32x16_bf16 v[32:47], v[72:75], a[40:43], v[32:47]\n"
"ds_read_b128 v[64:67], v112 offset:512\n"
"v_mfma_f32_32x32x16_bf16 v[48:63], v[72:75], a[168:171], v[48:63]\n"
"s_waitcnt lgkmcnt(5)\n"
"v_mfma_f32_32x32x16_bf16 v[32:47], v[76:79], a[44:47], v[32:47]\n"
"ds_read_b128 v[68:71], v113 offset:512\n"
"v_mfma_f32_32x32x16_bf16 v[48:63], v[76:79], a[172:175], v[48:63]\n"
"s_waitcnt lgkmcnt(5)\n"
"v_mfma_f32_32x32x16_bf16 v[32:47], v[80:83], a[48:51], v[32:47]\n"
"ds_read_b128 v[72:75], v114 offset:512\n"
"v_mfma_f32_32x32x16_bf16 v[48:63], v[80:83], a[176:179], v[48:63]\n"
"s_waitcnt lgkmcnt(5)\n"
"v_mfma_f32_32x32x16_bf16 v[32:47], v[84:87], a[52:55], v[32:47]\n"
"ds_read_b128 v[76:79], v115 offset:512\n"
"s_add_u32 m0, s35, 0x18000\n"
"s_add_u32 s36, s29, 0x0\n"
"buffer_load_dwordx4 v128, s[20:23], s36 offen offset:0 lds\n"
"v_mfma_f32_32x32x16_bf16 v[48:63], v[84:87], a[180:183], v[48:63]\n"
"s_waitcnt lgkmcnt(5)\n"
"v_mfma_f32_32x32x16_bf16 v[32:47], v[88:91], a[56:59], v[32:47]\n"
"ds_read_b128 v[80:83], v116 offset:512\n"
"buffer_load_dwordx4 v129, s[20:23], s36 offen offset:1024 lds\n"
"v_mfma_f32_32x32x16_bf16 v[48:63], v[88:91], a[184:187], v[48:63]\n"
"s_waitcnt lgkmcnt(5)\n"
"v_mfma_f32_32x32x16_bf16 v[32:47], v[92:95], a[60:63], v[32:47]\n"
"ds_read_b128 v[84:87], v117 offset:512\n"
"buffer_load_dwordx4 v130, s[20:23], s36 offen offset:2048 lds\n"
"v_mfma_f32_32x32x16_bf16 v[48:63], v[92:95], a[188:191], v[48:63]\n"
"s_waitcnt lgkmcnt(5)\n"
"v_mfma_f32_32x32x16_bf16 v[32:47], v[64:67], a[64:67], v[32:47]\n"
"ds_read_b128 v[88:91], v118 offset:512\n"
"buffer_load_dwordx4 v131, s[20:23], s36 offen offset:3072 lds\n"
"v_mfma_f32_32x32x16_bf16 v[48:63], v[64:67], a[192:195], v[48:63]\n"
"s_waitcnt lgkmcnt(5)\n"
"v_mfma_f32_32x32x16_bf16 v[32:47], v[68:71], a[68:71], v[32:47]\n"
"ds_read_b128 v[92:95], v119 offset:512\n"
"s_add_u32 m0, s35, 0x19000\n"
"s_add_u32 s36, s29, 0x1000\n"
"buffer_load_dwordx4 v132, s[20:23], s36 offen offset:0 lds\n"
"v_mfma_f32_32x32x16_bf16 v[48:63], v[68:71], a[196:199], v[48:63]\n"
"s_waitcnt lgkmcnt(5)\n"
"v_mfma_f32_32x32x16_bf16 v[32:47], v[72:75], a[72:75], v[32:47]\n"
"ds_read_b128 v[64:67], v112 offset:768\n"
"buffer_load_dwordx4 v133, s[20:23], s36 offen offset:1024 lds\n"
"v_mfma_f32_32x32x16_bf16 v[48:63], v[72:75], a[200:203], v[48:63]\n"
"s_waitcnt lgkmcnt(5)\n"
"v_mfma_f32_32x32x16_bf16 v[32:47], v[76:79], a[76:79], v[32:47]\n"
"ds_read_b128 v[68:71], v113 offset:768\n"
"buffer_load_dwordx4 v134, s[20:23], s36 offen offset:2048 lds\n"
"v_mfma_f32_32x32x16_bf16 v[48:63], v[76:79], a[204:207], v[48:63]\n"
"s_waitcnt lgkmcnt(5)\n"
"v_mfma_f32_32x32x16_bf16 v[32:47], v[80:83], a[80:83], v[32:47]\n"
"ds_read_b128 v[72:75], v114 offset:768\n"
"buffer_load_dwordx4 v135, s[20:23], s36 offen offset:3072 lds\n"
"v_mfma_f32_32x32x16_bf16 v[48:63], v[80:83], a[208:211], v[48:63]\n"
"s_waitcnt lgkmcnt(5)\n"
"v_mfma_f32_32x32x16_bf16 v[32:47], v[84:87], a[84:87], v[32:47]\n"
"ds_read_b128 v[76:79], v115 offset:768\n"
"v_mfma_f32_32x32x16_bf16 v[48:63], v[84:87], a[212:215], v[48:63]\n"
"s_add_u32 s29, s29, 0x8000\n"
"s_add_u32 s28, s28, 0x2000\n"
"s_waitcnt lgkmcnt(5)\n"
"v_mfma_f32_32x32x16_bf16 v[32:47], v[88:91], a[88:91], v[32:47]\n"
"ds_read_b128 v[80:83], v116 offset:768\n"
"v_mov_b32 v96, s34\n"
"v_mfma_f32_32x32x16_bf16 v[48:63], v[88:91], a[216:219], v[48:63]\n"
"s_waitcnt lgkmcnt(5)\n"
"v_mfma_f32_32x32x16_bf16 v[32:47], v[92:95], a[92:95], v[32:47]\n"
"ds_read_b128 v[84:87], v117 offset:768\n"
"s_add_u32 s34, s34, 1\n"
"v_mfma_f32_32x32x16_bf16 v[48:63], v[92:95], a[220:223], v[48:63]\n"
"s_waitcnt lgkmcnt(5)\n"
"v_mfma_f32_32x32x16_bf16 v[32:47], v[64:67], a[96:99], v[32:47]\n"
"ds_read_b128 v[88:91], v118 offset:768\n"
"v_mfma_f32_32x32x16_bf16 v[48:63], v[64:67], a[224:227], v[48:63]\n"
"s_waitcnt lgkmcnt(5)\n"
"v_mfma_f32_32x32x16_bf16 v[32:47], v[68:71], a[100:103], v[32:47]\n"
"ds_read_b128 v[92:95], v119 offset:768\n"
"v_mfma_f32_32x32x16_bf16 v[48:63], v[68:71], a[228:231], v[48:63]\n"
"s_waitcnt vmcnt(8)\n"
"s_barrier\n"
"s_waitcnt lgkmcnt(5)\n"
"v_mfma_f32_32x32x16_bf16 v[32:47], v[72:75], a[104:107], v[32:47]\n"
"ds_read_b128 v[64:67], v120 offset:32768\n"
"v_mfma_f32_32x32x16_bf16 v[48:63], v[72:75], a[232:235], v[48:63]\n"
"s_mov_b64 exec, 1\n"
"buffer_store_dword v96, off, s[12:15], s31 sc1\n"
"s_mov_b64 exec, -1\n"
"s_waitcnt lgkmcnt(5)\n"
"v_mfma_f32_32x32x16_bf16 v[32:47], v[76:79], a[108:111], v[32:47]\n"
"ds_read_b128 v[68:71], v121 offset:32768\n"
"v_mfma_f32_32x32x16_bf16 v[48:63], v[76:79], a[236:239], v[48:63]\n"
"s_waitcnt lgkmcnt(5)\n"
"v_mfma_f32_32x32x16_bf16 v[32:47], v[80:83], a[112:115], v[32:47]\n"
"ds_read_b128 v[72:75], v122 offset:32768\n"
"v_mfma_f32_32x32x16_bf16 v[48:63], v[80:83], a[240:243], v[48:63]\n"
"s_waitcnt lgkmcnt(5)\n"
"v_mfma_f32_32x32x16_bf16 v[32:47], v[84:87], a[116:119], v[32:47]\n"
"ds_read_b128 v[76:79], v123 offset:32768\n"
"v_mfma_f32_32x32x16_bf16 v[48:63], v[84:87], a[244:247], v[48:63]\n"
"s_waitcnt lgkmcnt(5)\n"
"v_mfma_f32_32x32x16_bf16 v[32:47], v[88:91], a[120:123], v[32:47]\n"
"ds_read_b128 v[80:83], v124 offset:32768\n"
"v_mfma_f32_32x32x16_bf16 v[48:63], v[88:91], a[248:251], v[48:63]\n"
"s_waitcnt lgkmcnt(5)\n"
"v_mfma_f32_32x32x16_bf16 v[32:47], v[92:95], a[124:127], v[32:47]\n"
"ds_read_b128 v[84:87], v125 offset:32768\n"
"v_mfma_f32_32x32x16_bf16 v[48:63], v[92:95], a[252:255], v[48:63]\n"
"s_sub_u32 s30, s30, 1\n"
"s_cmp_lg_u32 s30, 0\n"
"s_cbranch_scc1 L_pv_loop\n"
"s_nop 7\n"
"s_nop 3\n"
"v_cvt_pk_bf16_f32 v96, v32, v33\n"
"v_cvt_pk_bf16_f32 v97, v34, v35\n"
"v_cvt_pk_bf16_f32 v98, v36, v37\n"
"v_cvt_pk_bf16_f32 v99, v38, v39\n"
"buffer_store_dwordx4 v[96:99], v136, s[24:27], s28 offen offset:0 sc1\n"
"v_cvt_pk_bf16_f32 v100, v40, v41\n"
"v_cvt_pk_bf16_f32 v101, v42, v43\n"
"v_cvt_pk_bf16_f32 v102, v44, v45\n"
"v_cvt_pk_bf16_f32 v103, v46, v47\n"
"buffer_store_dwordx4 v[100:103], v136, s[24:27], s28 offen offset:1024 sc1\n"
"v_cvt_pk_bf16_f32 v104, v48, v49\n"
"v_cvt_pk_bf16_f32 v105, v50, v51\n"
"v_cvt_pk_bf16_f32 v106, v52, v53\n"
"v_cvt_pk_bf16_f32 v107, v54, v55\n"
"buffer_store_dwordx4 v[104:107], v136, s[24:27], s28 offen offset:2048 sc1\n"
"v_cvt_pk_bf16_f32 v108, v56, v57\n"
"v_cvt_pk_bf16_f32 v109, v58, v59\n"
"v_cvt_pk_bf16_f32 v110, v60, v61\n"
"v_cvt_pk_bf16_f32 v111, v62, v63\n"
"buffer_store_dwordx4 v[108:111], v136, s[24:27], s28 offen offset:3072 sc1\n"
"v_mov_b32 v0, s34\n"
"s_waitcnt vmcnt(0) lgkmcnt(0)\n"
"s_barrier\n"
"s_mov_b64 exec, 1\n"
"buffer_store_dword v0, off, s[12:15], s31 sc1\n"
"s_mov_b64 exec, -1\n"
"s_branch L_next\n"
"L_att:\n"
"s_add_u32 s43, s66, 1\n"
"s_lshl_b32 s43, s43, 3\n"
"v_and_b32 v0, 3, v255\n"
"v_lshlrev_b32 v0, 3, v0\n"
"s_lshr_b32 s3, s67, 8\n"
"s_or_b32 s3, s3, 0x40000000\n"
"v_bfe_u32 v1, s67, v0, 8\n"
"v_bfe_u32 v2, s3, v0, 8\n"
"v_min_u32 v2, s43, v2\n"
"v_sub_u32 v4, v2, v1\n"
"v_mov_b32 v3, -2\n"
"v_cmp_lt_i32 vcc, 0, v4\n"
"s_nop 1\n"
"v_cndmask_b32 v4, v3, v4, vcc\n"
"v_cmp_gt_u32 vcc, 12, v255\n"
"s_nop 1\n"
"v_cndmask_b32 v4, v3, v4, vcc\n"
"v_min_u32 v5, 11, v255\n"
"v_lshlrev_b32 v5, 7, v5\n"
"s_lshr_b32 s3, s65, 1\n"
"s_mul_i32 s3, s3, 1536\n"
"s_add_u32 s46, s3, 0x7000\n"
"v_lshlrev_b32 v176, 4, v255\n"
"s_add_u32 s35, s66, 1\n"
"s_lshl_b32 s35, s35, 2\n"
"s_lshl_b32 s47, s65, 19\n"
"s_add_u32 s16, s10, 0x4000000\n"
"s_addc_u32 s17, s11, 0\n"
"s_add_u32 s16, s16, s47\n"
"s_addc_u32 s17, s17, 0\n"
"s_and_b32 s17, s17, 0xffff\n"
"s_mov_b32 s18, 0x0\n"
"s_mov_b32 s19, 0x20000\n"
"s_add_u32 s20, s10, 0x5000000\n"
"s_addc_u32 s21, s11, 0\n"
"s_add_u32 s20, s20, s47\n"
"s_addc_u32 s21, s21, 0\n"
"s_and_b32 s21, s21, 0xffff\n"
"s_mov_b32 s22, 0x0\n"
"s_mov_b32 s23, 0x20000\n"
"s_lshl_b32 s18, s35, 14\n"
"s_lshl_b32 s22, s35, 14\n"
"s_lshl_b32 s48, s66, 16\n"
"s_lshl_b32 s3, s5, 14\n"
"s_add_u32 s48, s48, s3\n"
"s_add_u32 s47, s47, s48\n"
"s_add_u32 s24, s10, 0x3000000\n"
"s_addc_u32 s25, s11, 0\n"
"s_add_u32 s24, s24, s47\n"
"s_addc_u32 s25, s25, 0\n"
"s_and_b32 s25, s25, 0xffff\n"
"s_mov_b32 s26, 0x4000\n"
"s_mov_b32 s27, 0x20000\n"
"s_lshl_b32 s48, s48, 5\n"
"s_lshl_b32 s3, s65, 13\n"
"s_add_u32 s48, s48, s3\n"
"s_add_u32 s28, s10, 0x7000000\n"
"s_addc_u32 s29, s11, 0\n"
"s_add_u32 s28, s28, s48\n"
"s_addc_u32 s29, s29, 0\n"
"s_and_b32 s29, s29, 0xffff\n"
"s_mov_b32 s30, 0x42000\n"
"s_mov_b32 s31, 0x20000\n"
"Lw_9:\n"
"buffer_load_dword v6, v5, s[12:15], s46 offen sc1\n"
"s_waitcnt vmcnt(0)\n"
"v_cmp_lt_i32 vcc, v6, v4\n"
"s_cbranch_vccz Lwok_10\n"
"s_sub_u32 s8, s8, 1\n"
"s_cbranch_scc1 L_bulk_fail\n"
"s_sleep 4\n"
"s_branch Lw_9\n"
"Lwok_10:\n"
"s_barrier\n"
"s_lshl_b32 s36, s5, 12\n"
"s_mov_b32 s37, s36\n"
"s_mov_b32 s38, s36\n"
"s_or_b32 s39, s36, 0x10000\n"
"s_mov_b32 m0, s38\n"
"s_nop 0\n"
"buffer_load_dwordx4 v176, s[16:19], s36 offen offset:0 sc1 lds\n"
"buffer_load_dwordx4 v176, s[16:19], s36 offen offset:1024 sc1 lds\n"
"buffer_load_dwordx4 v176, s[16:19], s36 offen offset:2048 sc1 lds\n"
"buffer_load_dwordx4 v176, s[16:19], s36 offen offset:3072 sc1 lds\n"
"s_mov_b32 s3, 0\n"
"buffer_load_dwordx4 a[128:131], v176, s[24:27], s3 offen offset:0 sc1\n"
"s_mov_b32 s3, 0\n"
"buffer_load_dwordx4 a[132:135], v176, s[24:27], s3 offen offset:1024 sc1\n"
"s_mov_b32 s3, 0\n"
"buffer_load_dwordx4 a[136:139], v176, s[24:27], s3 offen offset:2048 sc1\n"
"s_mov_b32 s3, 0\n"
"buffer_load_dwordx4 a[140:143], v176, s[24:27], s3 offen offset:3072 sc1\n"
"s_mov_b32 s3, 4096\n"
"buffer_load_dwordx4 a[144:147], v176, s[24:27], s3 offen offset:0 sc1\n"
"s_mov_b32 s3, 4096\n"
"buffer_load_dwordx4 a[148:151], v176, s[24:27], s3 offen offset:1024 sc1\n"
"s_mov_b32 s3, 4096\n"
"buffer_load_dwordx4 a[152:155], v176, s[24:27], s3 offen offset:2048 sc1\n"
"s_mov_b32 s3, 4096\n"
"buffer_load_dwordx4 a[156:159], v176, s[24:27], s3 offen offset:3072 sc1\n"
"s_mov_b32 s3, 8192\n"
"buffer_load_dwordx4 a[160:163], v176, s[24:27], s3 offen offset:0 sc1\n"
"s_mov_b32 s3, 8192\n"
"buffer_load_dwordx4 a[164:167], v176, s[24:27], s3 offen offset:1024 sc1\n"
"s_mov_b32 s3, 8192\n"
"buffer_load_dwordx4 a[168:171], v176, s[24:27], s3 offen offset:2048 sc1\n"
"s_mov_b32 s3, 8192\n"
"buffer_load_dwordx4 a[172:175], v176, s[24:27], s3 offen offset:3072 sc1\n"
"s_mov_b32 s3, 12288\n"
"buffer_load_dwordx4 a[176:179], v176, s[24:27], s3 offen offset:0 sc1\n"
"s_mov_b32 s3, 12288\n"
"buffer_load_dwordx4 a[180:183], v176, s[24:27], s3 offen offset:1024 sc1\n"
"s_mov_b32 s3, 12288\n"
"buffer_load_dwordx4 a[184:187], v176, s[24:27], s3 offen offset:2048 sc1\n"
"s_mov_b32 s3, 12288\n"
"buffer_load_dwordx4 a[188:191], v176, s[24:27], s3 offen offset:3072 sc1\n"
"s_add_u32 s36, s36, 0x4000\n"
"s_add_u32 s38, s38, 0x4000\n"
"s_and_b32 s38, s38, 0xffff\n"
"v_accvgpr_write_b32 a0, 0\n"
"v_accvgpr_write_b32 a1, 0\n"
"v_accvgpr_write_b32 a2, 0\n"
"v_accvgpr_write_b32 a3, 0\n"
"v_accvgpr_write_b32 a4, 0\n"
"v_accvgpr_write_b32 a5, 0\n"
"v_accvgpr_write_b32 a6, 0\n"
"v_accvgpr_write_b32 a7, 0\n"
"v_accvgpr_write_b32 a8, 0\n"
"v_accvgpr_write_b32 a9, 0\n"
"v_accvgpr_write_b32 a10, 0\n"
"v_accvgpr_write_b32 a11, 0\n"
"v_accvgpr_write_b32 a12, 0\n"
"v_accvgpr_write_b32 a13, 0\n"
"v_accvgpr_write_b32 a14, 0\n"
"v_accvgpr_write_b32 a15, 0\n"
"v_accvgpr_write_b32 a16, 0\n"
"v_accvgpr_write_b32 a17, 0\n"
"v_accvgpr_write_b32 a18, 0\n"
"v_accvgpr_write_b32 a19, 0\n"
"v_accvgpr_write_b32 a20, 0\n"
"v_accvgpr_write_b32 a21, 0\n"
"v_accvgpr_write_b32 a22, 0\n"
"v_accvgpr_write_b32 a23, 0\n"
"v_accvgpr_write_b32 a24, 0\n"
"v_accvgpr_write_b32 a25, 0\n"
"v_accvgpr_write_b32 a26, 0\n"
"v_accvgpr_write_b32 a27, 0\n"
"v_accvgpr_write_b32 a28, 0\n"
"v_accvgpr_write_b32 a29, 0\n"
"v_accvgpr_write_b32 a30, 0\n"
"v_accvgpr_write_b32 a31, 0\n"
"v_accvgpr_write_b32 a32, 0\n"
"v_accvgpr_write_b32 a33, 0\n"
"v_accvgpr_write_b32 a34, 0\n"
"v_accvgpr_write_b32 a35, 0\n"
"v_accvgpr_write_b32 a36, 0\n"
"v_accvgpr_write_b32 a37, 0\n"
"v_accvgpr_write_b32 a38, 0\n"
"v_accvgpr_write_b32 a39, 0\n"
"v_accvgpr_write_b32 a40, 0\n"
"v_accvgpr_write_b32 a41, 0\n"
"v_accvgpr_write_b32 a42, 0\n"
"v_accvgpr_write_b32 a43, 0\n"
"v_accvgpr_write_b32 a44, 0\n"
"v_accvgpr_write_b32 a45, 0\n"
"v_accvgpr_write_b32 a46, 0\n"
"v_accvgpr_write_b32 a47, 0\n"
"v_accvgpr_write_b32 a48, 0\n"
"v_accvgpr_write_b32 a49, 0\n"
"v_accvgpr_write_b32 a50, 0\n"
"v_accvgpr_write_b32 a51, 0\n"
"v_accvgpr_write_b32 a52, 0\n"
"v_accvgpr_write_b32 a53, 0\n"
"v_accvgpr_write_b32 a54, 0\n"
"v_accvgpr_write_b32 a55, 0\n"
"v_accvgpr_write_b32 a56, 0\n"
"v_accvgpr_write_b32 a57, 0\n"
"v_accvgpr_write_b32 a58, 0\n"
"v_accvgpr_write_b32 a59, 0\n"
"v_accvgpr_write_b32 a60, 0\n"
"v_accvgpr_write_b32 a61, 0\n"
"v_accvgpr_write_b32 a62, 0\n"
"v_accvgpr_write_b32 a63, 0\n"
"v_accvgpr_write_b32 a64, 0\n"
"v_accvgpr_write_b32 a65, 0\n"
"v_accvgpr_write_b32 a66, 0\n"
"v_accvgpr_write_b32 a67, 0\n"
"v_accvgpr_write_b32 a68, 0\n"
"v_accvgpr_write_b32 a69, 0\n"
"v_accvgpr_write_b32 a70, 0\n"
"v_accvgpr_write_b32 a71, 0\n"
"v_accvgpr_write_b32 a72, 0\n"
"v_accvgpr_write_b32 a73, 0\n"
"v_accvgpr_write_b32 a74, 0\n"
"v_accvgpr_write_b32 a75, 0\n"
"v_accvgpr_write_b32 a76, 0\n"
"v_accvgpr_write_b32 a77, 0\n"
"v_accvgpr_write_b32 a78, 0\n"
"v_accvgpr_write_b32 a79, 0\n"
"v_accvgpr_write_b32 a80, 0\n"
"v_accvgpr_write_b32 a81, 0\n"
"v_accvgpr_write_b32 a82, 0\n"
"v_accvgpr_write_b32 a83, 0\n"
"v_accvgpr_write_b32 a84, 0\n"
"v_accvgpr_write_b32 a85, 0\n"
"v_accvgpr_write_b32 a86, 0\n"
"v_accvgpr_write_b32 a87, 0\n"
"v_accvgpr_write_b32 a88, 0\n"
"v_accvgpr_write_b32 a89, 0\n"
"v_accvgpr_write_b32 a90, 0\n"
"v_accvgpr_write_b32 a91, 0\n"
"v_accvgpr_write_b32 a92, 0\n"
"v_accvgpr_write_b32 a93, 0\n"
"v_accvgpr_write_b32 a94, 0\n"
"v_accvgpr_write_b32 a95, 0\n"
"v_accvgpr_write_b32 a96, 0\n"
"v_accvgpr_write_b32 a97, 0\n"
"v_accvgpr_write_b32 a98, 0\n"
"v_accvgpr_write_b32 a99, 0\n"
"v_accvgpr_write_b32 a100, 0\n"
"v_accvgpr_write_b32 a101, 0\n"
"v_accvgpr_write_b32 a102, 0\n"
"v_accvgpr_write_b32 a103, 0\n"
"v_accvgpr_write_b32 a104, 0\n"
"v_accvgpr_write_b32 a105, 0\n"
"v_accvgpr_write_b32 a106, 0\n"
"v_accvgpr_write_b32 a107, 0\n"
"v_accvgpr_write_b32 a108, 0\n"
"v_accvgpr_write_b32 a109, 0\n"
"v_accvgpr_write_b32 a110, 0\n"
"v_accvgpr_write_b32 a111, 0\n"
"v_accvgpr_write_b32 a112, 0\n"
"v_accvgpr_write_b32 a113, 0\n"
"v_accvgpr_write_b32 a114, 0\n"
"v_accvgpr_write_b32 a115, 0\n"
"v_accvgpr_write_b32 a116, 0\n"
"v_accvgpr_write_b32 a117, 0\n"
"v_accvgpr_write_b32 a118, 0\n"
"v_accvgpr_write_b32 a119, 0\n"
"v_accvgpr_write_b32 a120, 0\n"
"v_accvgpr_write_b32 a121, 0\n"
"v_accvgpr_write_b32 a122, 0\n"
"v_accvgpr_write_b32 a123, 0\n"
"v_accvgpr_write_b32 a124, 0\n"
"v_accvgpr_write_b32 a125, 0\n"
"v_accvgpr_write_b32 a126, 0\n"
"v_accvgpr_write_b32 a127, 0\n"
"v_mov_b32 v179, 0xff800000\n"
"v_mov_b32 v64, 0\n"
"v_mov_b32 v65, 0\n"
"v_mov_b32 v66, 0\n"
"v_mov_b32 v67, 0\n"
"v_mov_b32 v68, 0\n"
"v_mov_b32 v69, 0\n"
"v_mov_b32 v70, 0\n"
"v_mov_b32 v71, 0\n"
"v_mov_b32 v80, 0\n"
"v_mov_b32 v81, 0\n"
"v_mov_b32 v82, 0\n"
"v_mov_b32 v83, 0\n"
"v_mov_b32 v84, 0\n"
"v_mov_b32 v85, 0\n"
"v_mov_b32 v86, 0\n"
"v_mov_b32 v87, 0\n"
"v_mov_b32 v72, 0\n"
"v_mov_b32 v73, 0\n"
"v_mov_b32 v74, 0\n"
"v_mov_b32 v75, 0\n"
"v_mov_b32 v76, 0\n"
"v_mov_b32 v77, 0\n"
"v_mov_b32 v78, 0\n"
"v_mov_b32 v79, 0\n"
"v_mov_b32 v88, 0\n"
"v_mov_b32 v89, 0\n"
"v_mov_b32 v90, v179\n"
"v_mov_b32 v91, v179\n"
"v_mov_b32 v92, v179\n"
"v_mov_b32 v93, v179\n"
"v_mov_b32 v94, v179\n"
"v_mov_b32 v95, v179\n"
"v_mov_b32 v96, v179\n"
"v_mov_b32 v97, v179\n"
"v_mov_b32 v98, v179\n"
"v_mov_b32 v99, v179\n"
"v_mov_b32 v100, v179\n"
"v_mov_b32 v101, v179\n"
"v_mov_b32 v102, v179\n"
"v_mov_b32 v103, v179\n"
"v_mov_b32 v112, v179\n"
"v_mov_b32 v113, v179\n"
"v_mov_b32 v114, v179\n"
"v_mov_b32 v115, v179\n"
"v_mov_b32 v116, v179\n"
"v_mov_b32 v117, v179\n"
"v_mov_b32 v118, v179\n"
"v_mov_b32 v119, v179\n"
"v_mov_b32 v104, v179\n"
"v_mov_b32 v105, v179\n"
"v_mov_b32 v106, v179\n"
"v_mov_b32 v107, v179\n"
"v_mov_b32 v108, v179\n"
"v_mov_b32 v109, v179\n"
"v_mov_b32 v110, v179\n"
"v_mov_b32 v111, v179\n"
"v_mov_b32 v120, v179\n"
"v_mov_b32 v121, v179\n"
"v_mov_b32 v122, v179\n"
"v_mov_b32 v123, v179\n"
"v_mov_b32 v124, v179\n"
"v_mov_b32 v125, v179\n"
"v_mov_b32 v126, v179\n"
"v_mov_b32 v127, v179\n"
"v_mov_b32 v128, 0\n"
"v_mov_b32 v129, 0\n"
"v_mov_b32 v130, 0\n"
"v_mov_b32 v131, 0\n"
"v_mov_b32 v132, 0\n"
"v_mov_b32 v133, 0\n"
"v_mov_b32 v134, 0\n"
"v_mov_b32 v135, 0\n"
"v_mov_b32 v136, 0\n"
"v_mov_b32 v137, 0\n"
"v_mov_b32 v138, 0\n"
"v_mov_b32 v139, 0\n"
"v_mov_b32 v140, 0\n"
"v_mov_b32 v141, 0\n"
"v_mov_b32 v142, 0\n"
"v_mov_b32 v143, 0\n"
"v_mov_b32 v144, 0\n"
"v_mov_b32 v145, 0\n"
"v_mov_b32 v146, 0\n"
"v_mov_b32 v147, 0\n"
"v_mov_b32 v148, 0\n"
"v_mov_b32 v149, 0\n"
"v_mov_b32 v150, 0\n"
"v_mov_b32 v151, 0\n"
"v_mov_b32 v152, 0\n"
"v_mov_b32 v153, 0\n"
"v_mov_b32 v154, 0\n"
"v_mov_b32 v155, 0\n"
"v_mov_b32 v156, 0\n"
"v_mov_b32 v157, 0\n"
"v_mov_b32 v158, 0\n"
"v_mov_b32 v159, 0\n"
"v_mov_b32 v160, 0\n"
"v_mov_b32 v161, 0\n"
"v_mov_b32 v162, 0\n"
"v_mov_b32 v163, 0\n"
"v_mov_b32 v164, 0\n"
"v_mov_b32 v165, 0\n"
"v_mov_b32 v166, 0\n"
"v_mov_b32 v167, 0\n"
"v_mov_b32 v177, 0x8\n"
"v_bfe_i32 v178, v255, 0, 1\n"
"v_and_b32 v178, 0x1, v178\n"
"v_add_u32 v177, v177, v178\n"
"v_bfe_i32 v178, v255, 1, 1\n"
"v_and_b32 v178, 0x2, v178\n"
"v_add_u32 v177, v177, v178\n"
"v_bfe_i32 v178, v255, 2, 1\n"
"v_and_b32 v178, 0x4, v178\n"
"v_add_u32 v177, v177, v178\n"
"v_bfe_i32 v178, v255, 3, 1\n"
"v_and_b32 v178, 0x8, v178\n"
"v_add_u32 v177, v177, v178\n"
"v_bfe_i32 v178, v255, 4, 1\n"
"v_and_b32 v178, 0x10, v178\n"
"v_add_u32 v177, v177, v178\n"
"v_lshrrev_b32 v178, 5, v255\n"
"v_lshlrev_b32 v178, 2, v178\n"
"v_sub_u32 v177, v177, v178\n"
"v_subrev_u32 v177, 8, v177\n"
"s_mov_b32 s33, 0\n"
"s_sub_u32 s34, s35, 4\n"
"s_add_u32 s34, s34, s5\n"
"s_mov_b32 s40, 0\n"
"s_mov_b32 s41, 0x10000\n"
"s_mov_b32 s42, 0\n"
"s_mov_b32 s44, 0xff800000\n"
"s_mov_b32 s45, 0xff800000\n"
"v_mov_b32 v174, v176\n"
"s_waitcnt vmcnt(16)\n"
"s_barrier\n"
"ds_read_b128 a[192:195], v174 offset:0\n"
"ds_read_b128 a[196:199], v174 offset:1024\n"
"ds_read_b128 a[200:203], v174 offset:2048\n"
"ds_read_b128 a[204:207], v174 offset:3072\n"
"s_mov_b32 m0, s39\n"
"s_nop 0\n"
"buffer_load_dwordx4 v176, s[20:23], s37 offen offset:0 sc1 lds\n"
"buffer_load_dwordx4 v176, s[20:23], s37 offen offset:1024 sc1 lds\n"
"buffer_load_dwordx4 v176, s[20:23], s37 offen offset:2048 sc1 lds\n"
"buffer_load_dwordx4 v176, s[20:23], s37 offen offset:3072 sc1 lds\n"
"s_mov_b32 m0, s38\n"
"s_nop 0\n"
"buffer_load_dwordx4 v176, s[16:19], s36 offen offset:0 sc1 lds\n"
"buffer_load_dwordx4 v176, s[16:19], s36 offen offset:1024 sc1 lds\n"
"buffer_load_dwordx4 v176, s[16:19], s36 offen offset:2048 sc1 lds\n"
"buffer_load_dwordx4 v176, s[16:19], s36 offen offset:3072 sc1 lds\n"
"s_add_u32 s36, s36, 0x4000\n"
"s_add_u32 s37, s37, 0x4000\n"
"s_add_u32 s38, s38, 0x4000\n"
"s_and_b32 s38, s38, 0xffff\n"
"s_add_u32 s39, s39, 0x4000\n"
"s_and_b32 s39, s39, 0xffff\n"
"s_or_b32 s39, s39, 0x10000\n"
"s_mov_b32 m0, s39\n"
"s_nop 0\n"
"buffer_load_dwordx4 v176, s[20:23], s37 offen offset:0 sc1 lds\n"
"buffer_load_dwordx4 v176, s[20:23], s37 offen offset:1024 sc1 lds\n"
"buffer_load_dwordx4 v176, s[20:23], s37 offen offset:2048 sc1 lds\n"
"buffer_load_dwordx4 v176, s[20:23], s37 offen offset:3072 sc1 lds\n"
"s_mov_b32 m0, s38\n"
"s_nop 0\n"
"buffer_load_dwordx4 v176, s[16:19], s36 offen offset:0 sc1 lds\n"
"buffer_load_dwordx4 v176, s[16:19], s36 offen offset:1024 sc1 lds\n"
"buffer_load_dwordx4 v176, s[16:19], s36 offen offset:2048 sc1 lds\n"
"buffer_load_dwordx4 v176, s[16:19], s36 offen offset:3072 sc1 lds\n"
"s_add_u32 s36, s36, 0x4000\n"
"s_add_u32 s37, s37, 0x4000\n"
"s_add_u32 s38, s38, 0x4000\n"
"s_and_b32 s38, s38, 0xffff\n"
"s_add_u32 s39, s39, 0x4000\n"
"s_and_b32 s39, s39, 0xffff\n"
"s_or_b32 s39, s39, 0x10000\n"
"s_waitcnt vmcnt(16)\n"
".p2align 6\n"
"L_att_loop:\n"
"s_waitcnt lgkmcnt(3)\n"
"v_mfma_f32_32x32x16_bf16 v[0:15], a[192:195], a[128:131], v[128:143]\n"
"ds_read_b128 a[208:211], v174 offset:4096\n"
"v_exp_f32 v90, v90\n"
"v_cvt_pk_bf16_f32 v64, v64, v65\n"
"v_exp_f32 v91, v91\n"
"v_mfma_f32_32x32x16_bf16 v[16:31], a[192:195], a[160:163], v[144:159]\n"
"v_add_u32 v175, s41, v176\n"
"v_cvt_pk_bf16_f32 v65, v66, v67\n"
"v_add_f32 v166, v166, v90\n"
"v_exp_f32 v92, v92\n"
"v_cvt_pk_bf16_f32 v66, v68, v69\n"
"s_waitcnt lgkmcnt(3)\n"
"v_mfma_f32_32x32x16_bf16 v[0:15], a[196:199], a[132:135], v[0:15]\n"
"ds_read_b128 a[212:215], v174 offset:5120\n"
"v_add_f32 v167, v167, v91\n"
"v_exp_f32 v93, v93\n"
"v_cvt_pk_bf16_f32 v67, v70, v71\n"
"v_add_f32 v166, v166, v92\n"
"v_mfma_f32_32x32x16_bf16 v[16:31], a[196:199], a[164:167], v[16:31]\n"
"v_exp_f32 v94, v94\n"
"v_cvt_pk_bf16_f32 v80, v80, v81\n"
"v_add_f32 v167, v167, v93\n"
"v_exp_f32 v95, v95\n"
"s_waitcnt lgkmcnt(3)\n"
"v_mfma_f32_32x32x16_bf16 v[0:15], a[200:203], a[136:139], v[0:15]\n"
"ds_read_b128 a[216:219], v174 offset:6144\n"
"v_cvt_pk_bf16_f32 v81, v82, v83\n"
"v_add_f32 v166, v166, v94\n"
"v_exp_f32 v96, v96\n"
"v_cvt_pk_bf16_f32 v82, v84, v85\n"
"v_mfma_f32_32x32x16_bf16 v[16:31], a[200:203], a[168:171], v[16:31]\n"
"v_add_f32 v167, v167, v95\n"
"v_exp_f32 v97, v97\n"
"v_cvt_pk_bf16_f32 v83, v86, v87\n"
"v_add_f32 v164, v164, v96\n"
"s_waitcnt lgkmcnt(3)\n"
"v_mfma_f32_32x32x16_bf16 v[0:15], a[204:207], a[140:143], v[0:15]\n"
"ds_read_b128 a[220:223], v174 offset:7168\n"
"v_exp_f32 v98, v98\n"
"v_cvt_pk_bf16_f32 v72, v72, v73\n"
"v_add_f32 v165, v165, v97\n"
"v_mfma_f32_32x32x16_bf16 v[16:31], a[204:207], a[172:175], v[16:31]\n"
"v_exp_f32 v99, v99\n"
"v_cvt_pk_bf16_f32 v73, v74, v75\n"
"v_add_f32 v164, v164, v98\n"
"v_exp_f32 v100, v100\n"
"s_waitcnt lgkmcnt(3)\n"
"v_mfma_f32_32x32x16_bf16 v[0:15], a[208:211], a[144:147], v[0:15]\n"
"ds_read_b128 a[192:195], v174 offset:8192\n"
"v_cvt_pk_bf16_f32 v74, v76, v77\n"
"v_add_f32 v165, v165, v99\n"
"v_exp_f32 v101, v101\n"
"v_cvt_pk_bf16_f32 v75, v78, v79\n"
"v_mfma_f32_32x32x16_bf16 v[16:31], a[208:211], a[176:179], v[16:31]\n"
"v_add_f32 v164, v164, v100\n"
"v_exp_f32 v102, v102\n"
"v_cvt_pk_bf16_f32 v88, v88, v89\n"
"v_add_f32 v165, v165, v101\n"
"s_waitcnt lgkmcnt(3)\n"
"v_mfma_f32_32x32x16_bf16 v[0:15], a[212:215], a[148:151], v[0:15]\n"
"ds_read_b128 a[196:199], v174 offset:9216\n"
"v_exp_f32 v103, v103\n"
"v_cvt_pk_bf16_f32 v89, v90, v91\n"
"v_add_f32 v164, v164, v102\n"
"v_mfma_f32_32x32x16_bf16 v[16:31], a[212:215], a[180:183], v[16:31]\n"
"v_exp_f32 v112, v112\n"
"v_cvt_pk_bf16_f32 v90, v92, v93\n"
"v_add_f32 v165, v165, v103\n"
"v_exp_f32 v113, v113\n"
"s_waitcnt lgkmcnt(3)\n"
"v_mfma_f32_32x32x16_bf16 v[0:15], a[216:219], a[152:155], v[0:15]\n"
"ds_read_b128 a[200:203], v174 offset:10240\n"
"v_cvt_pk_bf16_f32 v91, v94, v95\n"
"v_add_f32 v166, v166, v112\n"
"v_exp_f32 v114, v114\n"
"v_cvt_pk_bf16_f32 v96, v96, v97\n"
"v_mfma_f32_32x32x16_bf16 v[16:31], a[216:219], a[184:187], v[16:31]\n"
"v_add_f32 v167, v167, v113\n"
"v_exp_f32 v115, v115\n"
"v_cvt_pk_bf16_f32 v97, v98, v99\n"
"v_add_f32 v166, v166, v114\n"
"s_waitcnt lgkmcnt(3)\n"
"v_mfma_f32_32x32x16_bf16 v[0:15], a[220:223], a[156:159], v[0:15]\n"
"ds_read_b128 a[204:207], v174 offset:11264\n"
"v_exp_f32 v116, v116\n"
"v_cvt_pk_bf16_f32 v98, v100, v101\n"
"v_add_f32 v167, v167, v115\n"
"v_mfma_f32_32x32x16_bf16 v[16:31], a[220:223], a[188:191], v[16:31]\n"
"v_exp_f32 v117, v117\n"
"v_cvt_pk_bf16_f32 v99, v102, v103\n"
"v_add_f32 v166, v166, v116\n"
"v_exp_f32 v118, v118\n"
"s_waitcnt lgkmcnt(3)\n"
"v_mfma_f32_32x32x16_bf16 v[32:47], a[192:195], a[128:131], v[128:143]\n"
"ds_read_b128 a[208:211], v174 offset:12288\n"
"v_add_f32 v167, v167, v117\n"
"v_exp_f32 v119, v119\n"
"v_add_f32 v166, v166, v118\n"
"v_mfma_f32_32x32x16_bf16 v[48:63], a[192:195], a[160:163], v[144:159]\n"
"v_exp_f32 v104, v104\n"
"v_add_f32 v167, v167, v119\n"
"v_exp_f32 v105, v105\n"
"v_add_f32 v164, v164, v104\n"
"s_waitcnt lgkmcnt(3)\n"
"v_mfma_f32_32x32x16_bf16 v[32:47], a[196:199], a[132:135], v[32:47]\n"
"ds_read_b128 a[212:215], v174 offset:13312\n"
"v_exp_f32 v106, v106\n"
"v_cvt_pk_bf16_f32 v112, v112, v113\n"
"v_add_f32 v165, v165, v105\n"
"v_mfma_f32_32x32x16_bf16 v[48:63], a[196:199], a[164:167], v[48:63]\n"
"v_exp_f32 v107, v107\n"
"v_cvt_pk_bf16_f32 v113, v114, v115\n"
"v_add_f32 v164, v164, v106\n"
"v_exp_f32 v108, v108\n"
"s_waitcnt lgkmcnt(3)\n"
"v_mfma_f32_32x32x16_bf16 v[32:47], a[200:203], a[136:139], v[32:47]\n"
"ds_read_b128 a[216:219], v174 offset:14336\n"
"v_cvt_pk_bf16_f32 v114, v116, v117\n"
"v_add_f32 v165, v165, v107\n"
"v_exp_f32 v109, v109\n"
"v_cvt_pk_bf16_f32 v115, v118, v119\n"
"v_mfma_f32_32x32x16_bf16 v[48:63], a[200:203], a[168:171], v[48:63]\n"
"v_add_f32 v164, v164, v108\n"
"v_exp_f32 v110, v110\n"
"v_add_f32 v165, v165, v109\n"
"v_exp_f32 v111, v111\n"
"s_waitcnt lgkmcnt(3)\n"
"v_mfma_f32_32x32x16_bf16 v[32:47], a[204:207], a[140:143], v[32:47]\n"
"ds_read_b128 a[220:223], v174 offset:15360\n"
"v_add_f32 v164, v164, v110\n"
"v_exp_f32 v120, v120\n"
"v_add_f32 v165, v165, v111\n"
"v_mfma_f32_32x32x16_bf16 v[48:63], a[204:207], a[172:175], v[48:63]\n"
"v_exp_f32 v121, v121\n"
"v_add_f32 v166, v166, v120\n"
"v_exp_f32 v122, v122\n"
"v_cvt_pk_bf16_f32 v104, v104, v105\n"
"s_waitcnt lgkmcnt(3)\n"
"v_mfma_f32_32x32x16_bf16 v[32:47], a[208:211], a[144:147], v[32:47]\n"
"ds_read_b128 a[192:195], v175 offset:0\n"
"v_add_f32 v167, v167, v121\n"
"v_exp_f32 v123, v123\n"
"v_cvt_pk_bf16_f32 v105, v106, v107\n"
"v_add_f32 v166, v166, v122\n"
"v_mfma_f32_32x32x16_bf16 v[48:63], a[208:211], a[176:179], v[48:63]\n"
"v_exp_f32 v124, v124\n"
"v_cvt_pk_bf16_f32 v106, v108, v109\n"
"v_add_f32 v167, v167, v123\n"
"v_exp_f32 v125, v125\n"
"s_waitcnt lgkmcnt(3)\n"
"v_mfma_f32_32x32x16_bf16 v[32:47], a[212:215], a[148:151], v[32:47]\n"
"ds_read_b128 a[196:199], v175 offset:2048\n"
"v_cvt_pk_bf16_f32 v107, v110, v111\n"
"v_add_f32 v166, v166, v124\n"
"v_exp_f32 v126, v126\n"
"v_add_f32 v167, v167, v125\n"
"v_mfma_f32_32x32x16_bf16 v[48:63], a[212:215], a[180:183], v[48:63]\n"
"v_exp_f32 v127, v127\n"
"v_add_f32 v166, v166, v126\n"
"v_add_f32 v167, v167, v127\n"
"v_cvt_pk_bf16_f32 v120, v120, v121\n"
"v_cvt_pk_bf16_f32 v121, v122, v123\n"
"s_waitcnt lgkmcnt(3)\n"
"v_mfma_f32_32x32x16_bf16 v[32:47], a[216:219], a[152:155], v[32:47]\n"
"ds_read_b128 a[200:203], v175 offset:4096\n"
"v_cvt_pk_bf16_f32 v122, v124, v125\n"
"v_cvt_pk_bf16_f32 v123, v126, v127\n"
"v_add_f32 v162, v162, v164\n"
"v_add_f32 v162, v162, v165\n"
"v_add_f32 v163, v163, v166\n"
"v_mfma_f32_32x32x16_bf16 v[48:63], a[216:219], a[184:187], v[48:63]\n"
"v_add_f32 v163, v163, v167\n"
"s_waitcnt lgkmcnt(3)\n"
"v_mfma_f32_32x32x16_bf16 v[32:47], a[220:223], a[156:159], v[32:47]\n"
"ds_read_b128 a[204:207], v175 offset:6144\n"
"v_mfma_f32_32x32x16_bf16 v[48:63], a[220:223], a[188:191], v[48:63]\n"
"s_waitcnt vmcnt(8)\n"
"s_barrier\n"
"s_waitcnt lgkmcnt(3)\n"
"v_mfma_f32_32x32x16_bf16 a[0:15], a[192:195], v[64:67], a[0:15]\n"
"ds_read_b128 a[208:211], v175 offset:1024\n"
"v_mfma_f32_32x32x16_bf16 a[16:31], a[192:195], v[80:83], a[16:31]\n"
"s_mov_b32 m0, s38\n"
"s_nop 0\n"
"buffer_load_dwordx4 v176, s[16:19], s36 offen offset:0 sc1 lds\n"
"s_waitcnt lgkmcnt(3)\n"
"v_mfma_f32_32x32x16_bf16 a[32:47], a[196:199], v[64:67], a[32:47]\n"
"ds_read_b128 a[212:215], v175 offset:3072\n"
"buffer_load_dwordx4 v176, s[16:19], s36 offen offset:1024 sc1 lds\n"
"s_cmp_eq_u32 s33, s34\n"
"s_cbranch_scc1 L_att_mask0\n"
"L_att_masked0:\n"
"v_mfma_f32_32x32x16_bf16 a[48:63], a[196:199], v[80:83], a[48:63]\n"
"buffer_load_dwordx4 v176, s[16:19], s36 offen offset:2048 sc1 lds\n"
"v_max3_f32 v168, v0, v1, v2\n"
"v_max3_f32 v169, v33, v34, v35\n"
"v_max3_f32 v168, v168, v3, v4\n"
"v_max3_f32 v169, v169, v36, v37\n"
"s_waitcnt lgkmcnt(3)\n"
"v_mfma_f32_32x32x16_bf16 a[64:79], a[200:203], v[64:67], a[64:79]\n"
"ds_read_b128 a[216:219], v175 offset:5120\n"
"buffer_load_dwordx4 v176, s[16:19], s36 offen offset:3072 sc1 lds\n"
"v_max3_f32 v168, v168, v5, v6\n"
"v_max3_f32 v169, v169, v38, v39\n"
"v_max3_f32 v168, v168, v7, v8\n"
"v_mfma_f32_32x32x16_bf16 a[80:95], a[200:203], v[80:83], a[80:95]\n"
"s_mov_b32 m0, s39\n"
"s_nop 0\n"
"buffer_load_dwordx4 v176, s[20:23], s37 offen offset:0 sc1 lds\n"
"v_max3_f32 v169, v169, v40, v41\n"
"v_max3_f32 v168, v168, v9, v10\n"
"v_max3_f32 v169, v169, v42, v43\n"
"s_waitcnt lgkmcnt(3)\n"
"v_mfma_f32_32x32x16_bf16 a[96:111], a[204:207], v[64:67], a[96:111]\n"
"ds_read_b128 a[220:223], v175 offset:7168\n"
"buffer_load_dwordx4 v176, s[20:23], s37 offen offset:1024 sc1 lds\n"
"v_max3_f32 v168, v168, v11, v12\n"
"v_max3_f32 v169, v169, v44, v45\n"
"v_max3_f32 v168, v168, v13, v14\n"
"v_mfma_f32_32x32x16_bf16 a[112:127], a[204:207], v[80:83], a[112:127]\n"
"buffer_load_dwordx4 v176, s[20:23], s37 offen offset:2048 sc1 lds\n"
"v_max3_f32 v169, v169, v46, v47\n"
"v_max3_f32 v168, v168, v15, v32\n"
"v_max_f32 v168, v168, v169\n"
"v_max_f32 v169, v168, v169\n"
"s_waitcnt lgkmcnt(3)\n"
"v_mfma_f32_32x32x16_bf16 a[0:15], a[208:211], v[72:75], a[0:15]\n"
"ds_read_b128 a[192:195], v175 offset:8192\n"
"buffer_load_dwordx4 v176, s[20:23], s37 offen offset:3072 sc1 lds\n"
"v_max3_f32 v170, v16, v17, v18\n"
"v_max3_f32 v171, v49, v50, v51\n"
"v_max3_f32 v170, v170, v19, v20\n"
"v_mfma_f32_32x32x16_bf16 a[16:31], a[208:211], v[88:91], a[16:31]\n"
"v_max3_f32 v171, v171, v52, v53\n"
"v_max3_f32 v170, v170, v21, v22\n"
"v_max3_f32 v171, v171, v54, v55\n"
"v_max3_f32 v170, v170, v23, v24\n"
"v_max3_f32 v171, v171, v56, v57\n"
"v_max3_f32 v170, v170, v25, v26\n"
"s_waitcnt lgkmcnt(3)\n"
"v_mfma_f32_32x32x16_bf16 a[32:47], a[212:215], v[72:75], a[32:47]\n"
"ds_read_b128 a[196:199], v175 offset:10240\n"
"s_add_u32 s36, s36, 0x4000\n"
"s_add_u32 s37, s37, 0x4000\n"
"s_add_u32 s38, s38, 0x4000\n"
"s_and_b32 s38, s38, 0xffff\n"
"v_max3_f32 v171, v171, v58, v59\n"
"v_max3_f32 v170, v170, v27, v28\n"
"v_max3_f32 v171, v171, v60, v61\n"
"v_mfma_f32_32x32x16_bf16 a[48:63], a[212:215], v[88:91], a[48:63]\n"
"s_add_u32 s39, s39, 0x4000\n"
"s_and_b32 s39, s39, 0xffff\n"
"s_or_b32 s39, s39, 0x10000\n"
"v_max3_f32 v170, v170, v29, v30\n"
"v_max3_f32 v171, v171, v62, v63\n"
"v_max3_f32 v170, v170, v31, v48\n"
"s_waitcnt lgkmcnt(3)\n"
"v_mfma_f32_32x32x16_bf16 a[64:79], a[216:219], v[72:75], a[64:79]\n"
"ds_read_b128 a[200:203], v175 offset:12288\n"
"s_add_u32 s40, s40, 0x4000\n"
"s_and_b32 s40, s40, 0xffff\n"
"v_max_f32 v170, v170, v171\n"
"v_max_f32 v171, v170, v171\n"
"v_mov_b32 v164, 0\n"
"v_mov_b32 v165, 0\n"
"v_mfma_f32_32x32x16_bf16 a[80:95], a[216:219], v[88:91], a[80:95]\n"
"v_add_u32 v174, s40, v176\n"
"s_lshl_b32 s41, s33, 14\n"
"s_and_b32 s41, s41, 0xffff\n"
"s_or_b32 s41, s41, 0x10000\n"
"v_permlane32_swap_b32 v168, v169\n"
"v_permlane32_swap_b32 v170, v171\n"
"s_waitcnt lgkmcnt(3)\n"
"v_mfma_f32_32x32x16_bf16 a[96:111], a[220:223], v[72:75], a[96:111]\n"
"ds_read_b128 a[204:207], v175 offset:14336\n"
"v_max_f32 v168, v168, v169\n"
"v_max_f32 v170, v170, v171\n"
"v_mfma_f32_32x32x16_bf16 a[112:127], a[220:223], v[88:91], a[112:127]\n"
"v_max_f32 v178, v168, v170\n"
"v_cmp_lt_f32 vcc, s45, v178\n"
"v_mov_b32 v166, 0\n"
"v_mov_b32 v167, 0\n"
"s_waitcnt lgkmcnt(3)\n"
"v_mfma_f32_32x32x16_bf16 a[0:15], a[192:195], v[96:99], a[0:15]\n"
"ds_read_b128 a[208:211], v175 offset:9216\n"
"s_cbranch_vccnz L_att_rare0\n"
"L_att_rared0:\n"
"v_exp_f32 v0, v0\n"
"v_mfma_f32_32x32x16_bf16 a[16:31], a[192:195], v[112:115], a[16:31]\n"
"v_exp_f32 v1, v1\n"
"v_add_f32 v164, v164, v0\n"
"v_exp_f32 v2, v2\n"
"v_add_f32 v165, v165, v1\n"
"s_waitcnt lgkmcnt(3)\n"
"v_mfma_f32_32x32x16_bf16 a[32:47], a[196:199], v[96:99], a[32:47]\n"
"ds_read_b128 a[212:215], v175 offset:11264\n"
"v_exp_f32 v3, v3\n"
"v_add_f32 v164, v164, v2\n"
"v_exp_f32 v4, v4\n"
"v_mfma_f32_32x32x16_bf16 a[48:63], a[196:199], v[112:115], a[48:63]\n"
"v_add_f32 v165, v165, v3\n"
"v_exp_f32 v5, v5\n"
"v_add_f32 v164, v164, v4\n"
"v_exp_f32 v6, v6\n"
"s_waitcnt lgkmcnt(3)\n"
"v_mfma_f32_32x32x16_bf16 a[64:79], a[200:203], v[96:99], a[64:79]\n"
"ds_read_b128 a[216:219], v175 offset:13312\n"
"v_add_f32 v165, v165, v5\n"
"v_exp_f32 v7, v7\n"
"v_add_f32 v164, v164, v6\n"
"v_mfma_f32_32x32x16_bf16 a[80:95], a[200:203], v[112:115], a[80:95]\n"
"v_exp_f32 v16, v16\n"
"v_add_f32 v165, v165, v7\n"
"v_exp_f32 v17, v17\n"
"v_add_f32 v166, v166, v16\n"
"s_waitcnt lgkmcnt(3)\n"
"v_mfma_f32_32x32x16_bf16 a[96:111], a[204:207], v[96:99], a[96:111]\n"
"ds_read_b128 a[220:223], v175 offset:15360\n"
"v_exp_f32 v18, v18\n"
"v_add_f32 v167, v167, v17\n"
"v_exp_f32 v19, v19\n"
"v_mfma_f32_32x32x16_bf16 a[112:127], a[204:207], v[112:115], a[112:127]\n"
"v_add_f32 v166, v166, v18\n"
"v_exp_f32 v20, v20\n"
"v_add_f32 v167, v167, v19\n"
"v_exp_f32 v21, v21\n"
"s_waitcnt lgkmcnt(3)\n"
"v_mfma_f32_32x32x16_bf16 a[0:15], a[208:211], v[104:107], a[0:15]\n"
"ds_read_b128 a[192:195], v174 offset:0\n"
"v_add_f32 v166, v166, v20\n"
"v_exp_f32 v22, v22\n"
"v_add_f32 v167, v167, v21\n"
"v_mfma_f32_32x32x16_bf16 a[16:31], a[208:211], v[120:123], a[16:31]\n"
"v_exp_f32 v23, v23\n"
"v_add_f32 v166, v166, v22\n"
"v_exp_f32 v8, v8\n"
"v_add_f32 v167, v167, v23\n"
"s_waitcnt lgkmcnt(3)\n"
"v_mfma_f32_32x32x16_bf16 a[32:47], a[212:215], v[104:107], a[32:47]\n"
"ds_read_b128 a[196:199], v174 offset:1024\n"
"v_exp_f32 v9, v9\n"
"v_add_f32 v164, v164, v8\n"
"v_exp_f32 v10, v10\n"
"v_mfma_f32_32x32x16_bf16 a[48:63], a[212:215], v[120:123], a[48:63]\n"
"v_add_f32 v165, v165, v9\n"
"v_exp_f32 v11, v11\n"
"v_add_f32 v164, v164, v10\n"
"v_exp_f32 v12, v12\n"
"s_waitcnt lgkmcnt(3)\n"
"v_mfma_f32_32x32x16_bf16 a[64:79], a[216:219], v[104:107], a[64:79]\n"
"ds_read_b128 a[200:203], v174 offset:2048\n"
"v_add_f32 v165, v165, v11\n"
"v_exp_f32 v13, v13\n"
"v_add_f32 v164, v164, v12\n"
"v_mfma_f32_32x32x16_bf16 a[80:95], a[216:219], v[120:123], a[80:95]\n"
"v_exp_f32 v14, v14\n"
"v_add_f32 v165, v165, v13\n"
"v_exp_f32 v15, v15\n"
"v_add_f32 v164, v164, v14\n"
"s_waitcnt lgkmcnt(3)\n"
"v_mfma_f32_32x32x16_bf16 a[96:111], a[220:223], v[104:107], a[96:111]\n"
"ds_read_b128 a[204:207], v174 offset:3072\n"
"v_exp_f32 v24, v24\n"
"v_add_f32 v165, v165, v15\n"
"v_exp_f32 v25, v25\n"
"v_mfma_f32_32x32x16_bf16 a[112:127], a[220:223], v[120:123], a[112:127]\n"
"v_add_f32 v166, v166, v24\n"
"s_nop 0\n"
"v_add_f32 v167, v167, v25\n"
"s_cmp_lg_u32 s42, 0\n"
"s_cbranch_scc1 L_att_resc0\n"
"L_att_rescd0:\n"
"s_cmp_eq_u32 s33, s34\n"
"s_nop 0\n"
"s_cbranch_scc1 L_att_drain0\n"
"s_add_u32 s33, s33, 1\n"
"s_waitcnt lgkmcnt(3)\n"
"v_mfma_f32_32x32x16_bf16 v[64:79], a[192:195], a[128:131], v[128:143]\n"
"ds_read_b128 a[208:211], v174 offset:4096\n"
"v_exp_f32 v26, v26\n"
"v_cvt_pk_bf16_f32 v0, v0, v1\n"
"v_exp_f32 v27, v27\n"
"v_mfma_f32_32x32x16_bf16 v[80:95], a[192:195], a[160:163], v[144:159]\n"
"v_add_u32 v175, s41, v176\n"
"v_cvt_pk_bf16_f32 v1, v2, v3\n"
"v_add_f32 v166, v166, v26\n"
"v_exp_f32 v28, v28\n"
"v_cvt_pk_bf16_f32 v2, v4, v5\n"
"s_waitcnt lgkmcnt(3)\n"
"v_mfma_f32_32x32x16_bf16 v[64:79], a[196:199], a[132:135], v[64:79]\n"
"ds_read_b128 a[212:215], v174 offset:5120\n"
"v_add_f32 v167, v167, v27\n"
"v_exp_f32 v29, v29\n"
"v_cvt_pk_bf16_f32 v3, v6, v7\n"
"v_add_f32 v166, v166, v28\n"
"v_mfma_f32_32x32x16_bf16 v[80:95], a[196:199], a[164:167], v[80:95]\n"
"v_exp_f32 v30, v30\n"
"v_cvt_pk_bf16_f32 v16, v16, v17\n"
"v_add_f32 v167, v167, v29\n"
"v_exp_f32 v31, v31\n"
"s_waitcnt lgkmcnt(3)\n"
"v_mfma_f32_32x32x16_bf16 v[64:79], a[200:203], a[136:139], v[64:79]\n"
"ds_read_b128 a[216:219], v174 offset:6144\n"
"v_cvt_pk_bf16_f32 v17, v18, v19\n"
"v_add_f32 v166, v166, v30\n"
"v_exp_f32 v32, v32\n"
"v_cvt_pk_bf16_f32 v18, v20, v21\n"
"v_mfma_f32_32x32x16_bf16 v[80:95], a[200:203], a[168:171], v[80:95]\n"
"v_add_f32 v167, v167, v31\n"
"v_exp_f32 v33, v33\n"
"v_cvt_pk_bf16_f32 v19, v22, v23\n"
"v_add_f32 v164, v164, v32\n"
"s_waitcnt lgkmcnt(3)\n"
"v_mfma_f32_32x32x16_bf16 v[64:79], a[204:207], a[140:143], v[64:79]\n"
"ds_read_b128 a[220:223], v174 offset:7168\n"
"v_exp_f32 v34, v34\n"
"v_cvt_pk_bf16_f32 v8, v8, v9\n"
"v_add_f32 v165, v165, v33\n"
"v_mfma_f32_32x32x16_bf16 v[80:95], a[204:207], a[172:175], v[80:95]\n"
"v_exp_f32 v35, v35\n"
"v_cvt_pk_bf16_f32 v9, v10, v11\n"
"v_add_f32 v164, v164, v34\n"
"v_exp_f32 v36, v36\n"
"s_waitcnt lgkmcnt(3)\n"
"v_mfma_f32_32x32x16_bf16 v[64:79], a[208:211], a[144:147], v[64:79]\n"
"ds_read_b128 a[192:195], v174 offset:8192\n"
"v_cvt_pk_bf16_f32 v10, v12, v13\n"
"v_add_f32 v165, v165, v35\n"
"v_exp_f32 v37, v37\n"
"v_cvt_pk_bf16_f32 v11, v14, v15\n"
"v_mfma_f32_32x32x16_bf16 v[80:95], a[208:211], a[176:179], v[80:95]\n"
"v_add_f32 v164, v164, v36\n"
"v_exp_f32 v38, v38\n"
"v_cvt_pk_bf16_f32 v24, v24, v25\n"
"v_add_f32 v165, v165, v37\n"
"s_waitcnt lgkmcnt(3)\n"
"v_mfma_f32_32x32x16_bf16 v[64:79], a[212:215], a[148:151], v[64:79]\n"
"ds_read_b128 a[196:199], v174 offset:9216\n"
"v_exp_f32 v39, v39\n"
"v_cvt_pk_bf16_f32 v25, v26, v27\n"
"v_add_f32 v164, v164, v38\n"
"v_mfma_f32_32x32x16_bf16 v[80:95], a[212:215], a[180:183], v[80:95]\n"
"v_exp_f32 v48, v48\n"
"v_cvt_pk_bf16_f32 v26, v28, v29\n"
"v_add_f32 v165, v165, v39\n"
"v_exp_f32 v49, v49\n"
"s_waitcnt lgkmcnt(3)\n"
"v_mfma_f32_32x32x16_bf16 v[64:79], a[216:219], a[152:155], v[64:79]\n"
"ds_read_b128 a[200:203], v174 offset:10240\n"
"v_cvt_pk_bf16_f32 v27, v30, v31\n"
"v_add_f32 v166, v166, v48\n"
"v_exp_f32 v50, v50\n"
"v_cvt_pk_bf16_f32 v32, v32, v33\n"
"v_mfma_f32_32x32x16_bf16 v[80:95], a[216:219], a[184:187], v[80:95]\n"
"v_add_f32 v167, v167, v49\n"
"v_exp_f32 v51, v51\n"
"v_cvt_pk_bf16_f32 v33, v34, v35\n"
"v_add_f32 v166, v166, v50\n"
"s_waitcnt lgkmcnt(3)\n"
"v_mfma_f32_32x32x16_bf16 v[64:79], a[220:223], a[156:159], v[64:79]\n"
"ds_read_b128 a[204:207], v174 offset:11264\n"
"v_exp_f32 v52, v52\n"
"v_cvt_pk_bf16_f32 v34, v36, v37\n"
"v_add_f32 v167, v167, v51\n"
"v_mfma_f32_32x32x16_bf16 v[80:95], a[220:223], a[188:191], v[80:95]\n"
"v_exp_f32 v53, v53\n"
"v_cvt_pk_bf16_f32 v35, v38, v39\n"
"v_add_f32 v166, v166, v52\n"
"v_exp_f32 v54, v54\n"
"s_waitcnt lgkmcnt(3)\n"
"v_mfma_f32_32x32x16_bf16 v[96:111], a[192:195], a[128:131], v[128:143]\n"
"ds_read_b128 a[208:211], v174 offset:12288\n"
"v_add_f32 v167, v167, v53\n"
"v_exp_f32 v55, v55\n"
"v_add_f32 v166, v166, v54\n"
"v_mfma_f32_32x32x16_bf16 v[112:127], a[192:195], a[160:163], v[144:159]\n"
"v_exp_f32 v40, v40\n"
"v_add_f32 v167, v167, v55\n"
"v_exp_f32 v41, v41\n"
"v_add_f32 v164, v164, v40\n"
"s_waitcnt lgkmcnt(3)\n"
"v_mfma_f32_32x32x16_bf16 v[96:111], a[196:199], a[132:135], v[96:111]\n"
"ds_read_b128 a[212:215], v174 offset:13312\n"
"v_exp_f32 v42, v42\n"
"v_cvt_pk_bf16_f32 v48, v48, v49\n"
"v_add_f32 v165, v165, v41\n"
"v_mfma_f32_32x32x16_bf16 v[112:127], a[196:199], a[164:167], v[112:127]\n"
"v_exp_f32 v43, v43\n"
"v_cvt_pk_bf16_f32 v49, v50, v51\n"
"v_add_f32 v164, v164, v42\n"
"v_exp_f32 v44, v44\n"
"s_waitcnt lgkmcnt(3)\n"
"v_mfma_f32_32x32x16_bf16 v[96:111], a[200:203], a[136:139], v[96:111]\n"
"ds_read_b128 a[216:219], v174 offset:14336\n"
"v_cvt_pk_bf16_f32 v50, v52, v53\n"
"v_add_f32 v165, v165, v43\n"
"v_exp_f32 v45, v45\n"
"v_cvt_pk_bf16_f32 v51, v54, v55\n"
"v_mfma_f32_32x32x16_bf16 v[112:127], a[200:203], a[168:171], v[112:127]\n"
"v_add_f32 v164, v164, v44\n"
"v_exp_f32 v46, v46\n"
"v_add_f32 v165, v165, v45\n"
"v_exp_f32 v47, v47\n"
"s_waitcnt lgkmcnt(3)\n"
"v_mfma_f32_32x32x16_bf16 v[96:111], a[204:207], a[140:143], v[96:111]\n"
"ds_read_b128 a[220:223], v174 offset:15360\n"
"v_add_f32 v164, v164, v46\n"
"v_exp_f32 v56, v56\n"
"v_add_f32 v165, v165, v47\n"
"v_mfma_f32_32x32x16_bf16 v[112:127], a[204:207], a[172:175], v[112:127]\n"
"v_exp_f32 v57, v57\n"
"v_add_f32 v166, v166, v56\n"
"v_exp_f32 v58, v58\n"
"v_cvt_pk_bf16_f32 v40, v40, v41\n"
"s_waitcnt lgkmcnt(3)\n"
"v_mfma_f32_32x32x16_bf16 v[96:111], a[208:211], a[144:147], v[96:111]\n"
"ds_read_b128 a[192:195], v175 offset:0\n"
"v_add_f32 v167, v167, v57\n"
"v_exp_f32 v59, v59\n"
"v_cvt_pk_bf16_f32 v41, v42, v43\n"
"v_add_f32 v166, v166, v58\n"
"v_mfma_f32_32x32x16_bf16 v[112:127], a[208:211], a[176:179], v[112:127]\n"
"v_exp_f32 v60, v60\n"
"v_cvt_pk_bf16_f32 v42, v44, v45\n"
"v_add_f32 v167, v167, v59\n"
"v_exp_f32 v61, v61\n"
"s_waitcnt lgkmcnt(3)\n"
"v_mfma_f32_32x32x16_bf16 v[96:111], a[212:215], a[148:151], v[96:111]\n"
"ds_read_b128 a[196:199], v175 offset:2048\n"
"v_cvt_pk_bf16_f32 v43, v46, v47\n"
"v_add_f32 v166, v166, v60\n"
"v_exp_f32 v62, v62\n"
"v_add_f32 v167, v167, v61\n"
"v_mfma_f32_32x32x16_bf16 v[112:127], a[212:215], a[180:183], v[112:127]\n"
"v_exp_f32 v63, v63\n"
"v_add_f32 v166, v166, v62\n"
"v_add_f32 v167, v167, v63\n"
"v_cvt_pk_bf16_f32 v56, v56, v57\n"
"v_cvt_pk_bf16_f32 v57, v58, v59\n"
"s_waitcnt lgkmcnt(3)\n"
"v_mfma_f32_32x32x16_bf16 v[96:111], a[216:219], a[152:155], v[96:111]\n"
"ds_read_b128 a[200:203], v175 offset:4096\n"
"v_cvt_pk_bf16_f32 v58, v60, v61\n"
"v_cvt_pk_bf16_f32 v59, v62, v63\n"
"v_add_f32 v162, v162, v164\n"
"v_add_f32 v162, v162, v165\n"
"v_add_f32 v163, v163, v166\n"
"v_mfma_f32_32x32x16_bf16 v[112:127], a[216:219], a[184:187], v[112:127]\n"
"v_add_f32 v163, v163, v167\n"
"s_waitcnt lgkmcnt(3)\n"
"v_mfma_f32_32x32x16_bf16 v[96:111], a[220:223], a[156:159], v[96:111]\n"
"ds_read_b128 a[204:207], v175 offset:6144\n"
"v_mfma_f32_32x32x16_bf16 v[112:127], a[220:223], a[188:191], v[112:127]\n"
"s_waitcnt vmcnt(8)\n"
"s_barrier\n"
"s_waitcnt lgkmcnt(3)\n"
"v_mfma_f32_32x32x16_bf16 a[0:15], a[192:195], v[0:3], a[0:15]\n"
"ds_read_b128 a[208:211], v175 offset:1024\n"
"v_mfma_f32_32x32x16_bf16 a[16:31], a[192:195], v[16:19], a[16:31]\n"
"s_mov_b32 m0, s38\n"
"s_nop 0\n"
"buffer_load_dwordx4 v176, s[16:19], s36 offen offset:0 sc1 lds\n"
"s_waitcnt lgkmcnt(3)\n"
"v_mfma_f32_32x32x16_bf16 a[32:47], a[196:199], v[0:3], a[32:47]\n"
"ds_read_b128 a[212:215], v175 offset:3072\n"
"buffer_load_dwordx4 v176, s[16:19], s36 offen offset:1024 sc1 lds\n"
"s_cmp_eq_u32 s33, s34\n"
"s_cbranch_scc1 L_att_mask1\n"
"L_att_masked1:\n"
"v_mfma_f32_32x32x16_bf16 a[48:63], a[196:199], v[16:19], a[48:63]\n"
"buffer_load_dwordx4 v176, s[16:19], s36 offen offset:2048 sc1 lds\n"
"v_max3_f32 v168, v64, v65, v66\n"
"v_max3_f32 v169, v97, v98, v99\n"
"v_max3_f32 v168, v168, v67, v68\n"
"v_max3_f32 v169, v169, v100, v101\n"
"s_waitcnt lgkmcnt(3)\n"
"v_mfma_f32_32x32x16_bf16 a[64:79], a[200:203], v[0:3], a[64:79]\n"
"ds_read_b128 a[216:219], v175 offset:5120\n"
"buffer_load_dwordx4 v176, s[16:19], s36 offen offset:3072 sc1 lds\n"
"v_max3_f32 v168, v168, v69, v70\n"
"v_max3_f32 v169, v169, v102, v103\n"
"v_max3_f32 v168, v168, v71, v72\n"
"v_mfma_f32_32x32x16_bf16 a[80:95], a[200:203], v[16:19], a[80:95]\n"
"s_mov_b32 m0, s39\n"
"s_nop 0\n"
"buffer_load_dwordx4 v176, s[20:23], s37 offen offset:0 sc1 lds\n"
"v_max3_f32 v169, v169, v104, v105\n"
"v_max3_f32 v168, v168, v73, v74\n"
"v_max3_f32 v169, v169, v106, v107\n"
"s_waitcnt lgkmcnt(3)\n"
"v_mfma_f32_32x32x16_bf16 a[96:111], a[204:207], v[0:3], a[96:111]\n"
"ds_read_b128 a[220:223], v175 offset:7168\n"
"buffer_load_dwordx4 v176, s[20:23], s37 offen offset:1024 sc1 lds\n"
"v_max3_f32 v168, v168, v75, v76\n"
"v_max3_f32 v169, v169, v108, v109\n"
"v_max3_f32 v168, v168, v77, v78\n"
"v_mfma_f32_32x32x16_bf16 a[112:127], a[204:207], v[16:19], a[112:127]\n"
"buffer_load_dwordx4 v176, s[20:23], s37 offen offset:2048 sc1 lds\n"
"v_max3_f32 v169, v169, v110, v111\n"
"v_max3_f32 v168, v168, v79, v96\n"
"v_max_f32 v168, v168, v169\n"
"v_max_f32 v169, v168, v169\n"
"s_waitcnt lgkmcnt(3)\n"
"v_mfma_f32_32x32x16_bf16 a[0:15], a[208:211], v[8:11], a[0:15]\n"
"ds_read_b128 a[192:195], v175 offset:8192\n"
"buffer_load_dwordx4 v176, s[20:23], s37 offen offset:3072 sc1 lds\n"
"v_max3_f32 v170, v80, v81, v82\n"
"v_max3_f32 v171, v113, v114, v115\n"
"v_max3_f32 v170, v170, v83, v84\n"
"v_mfma_f32_32x32x16_bf16 a[16:31], a[208:211], v[24:27], a[16:31]\n"
"v_max3_f32 v171, v171, v116, v117\n"
"v_max3_f32 v170, v170, v85, v86\n"
"v_max3_f32 v171, v171, v118, v119\n"
"v_max3_f32 v170, v170, v87, v88\n"
"v_max3_f32 v171, v171, v120, v121\n"
"v_max3_f32 v170, v170, v89, v90\n"
"s_waitcnt lgkmcnt(3)\n"
"v_mfma_f32_32x32x16_bf16 a[32:47], a[212:215], v[8:11], a[32:47]\n"
"ds_read_b128 a[196:199], v175 offset:10240\n"
"s_add_u32 s36, s36, 0x4000\n"
"s_add_u32 s37, s37, 0x4000\n"
"s_add_u32 s38, s38, 0x4000\n"
"s_and_b32 s38, s38, 0xffff\n"
"v_max3_f32 v171, v171, v122, v123\n"
"v_max3_f32 v170, v170, v91, v92\n"
"v_max3_f32 v171, v171, v124, v125\n"
"v_mfma_f32_32x32x16_bf16 a[48:63], a[212:215], v[24:27], a[48:63]\n"
"s_add_u32 s39, s39, 0x4000\n"
"s_and_b32 s39, s39, 0xffff\n"
"s_or_b32 s39, s39, 0x10000\n"
"v_max3_f32 v170, v170, v93, v94\n"
"v_max3_f32 v171, v171, v126, v127\n"
"v_max3_f32 v170, v170, v95, v112\n"
"s_waitcnt lgkmcnt(3)\n"
"v_mfma_f32_32x32x16_bf16 a[64:79], a[216:219], v[8:11], a[64:79]\n"
"ds_read_b128 a[200:203], v175 offset:12288\n"
"s_add_u32 s40, s40, 0x4000\n"
"s_and_b32 s40, s40, 0xffff\n"
"v_max_f32 v170, v170, v171\n"
"v_max_f32 v171, v170, v171\n"
"v_mov_b32 v164, 0\n"
"v_mov_b32 v165, 0\n"
"v_mfma_f32_32x32x16_bf16 a[80:95], a[216:219], v[24:27], a[80:95]\n"
"v_add_u32 v174, s40, v176\n"
"s_lshl_b32 s41, s33, 14\n"
"s_and_b32 s41, s41, 0xffff\n"
"s_or_b32 s41, s41, 0x10000\n"
"v_permlane32_swap_b32 v168, v169\n"
"v_permlane32_swap_b32 v170, v171\n"
"s_waitcnt lgkmcnt(3)\n"
"v_mfma_f32_32x32x16_bf16 a[96:111], a[220:223], v[8:11], a[96:111]\n"
"ds_read_b128 a[204:207], v175 offset:14336\n"
"v_max_f32 v168, v168, v169\n"
"v_max_f32 v170, v170, v171\n"
"v_mfma_f32_32x32x16_bf16 a[112:127], a[220:223], v[24:27], a[112:127]\n"
"v_max_f32 v178, v168, v170\n"
"v_cmp_lt_f32 vcc, s45, v178\n"
"v_mov_b32 v166, 0\n"
"v_mov_b32 v167, 0\n"
"s_waitcnt lgkmcnt(3)\n"
"v_mfma_f32_32x32x16_bf16 a[0:15], a[192:195], v[32:35], a[0:15]\n"
"ds_read_b128 a[208:211], v175 offset:9216\n"
"s_cbranch_vccnz L_att_rare1\n"
"L_att_rared1:\n"
"v_exp_f32 v64, v64\n"
"v_mfma_f32_32x32x16_bf16 a[16:31], a[192:195], v[48:51], a[16:31]\n"
"v_exp_f32 v65, v65\n"
"v_add_f32 v164, v164, v64\n"
"v_exp_f32 v66, v66\n"
"v_add_f32 v165, v165, v65\n"
"s_waitcnt lgkmcnt(3)\n"
"v_mfma_f32_32x32x16_bf16 a[32:47], a[196:199], v[32:35], a[32:47]\n"
"ds_read_b128 a[212:215], v175 offset:11264\n"
"v_exp_f32 v67, v67\n"
"v_add_f32 v164, v164, v66\n"
"v_exp_f32 v68, v68\n"
"v_mfma_f32_32x32x16_bf16 a[48:63], a[196:199], v[48:51], a[48:63]\n"
"v_add_f32 v165, v165, v67\n"
"v_exp_f32 v69, v69\n"
"v_add_f32 v164, v164, v68\n"
"v_exp_f32 v70, v70\n"
"s_waitcnt lgkmcnt(3)\n"
"v_mfma_f32_32x32x16_bf16 a[64:79], a[200:203], v[32:35], a[64:79]\n"
"ds_read_b128 a[216:219], v175 offset:13312\n"
"v_add_f32 v165, v165, v69\n"
"v_exp_f32 v71, v71\n"
"v_add_f32 v164, v164, v70\n"
"v_mfma_f32_32x32x16_bf16 a[80:95], a[200:203], v[48:51], a[80:95]\n"
"v_exp_f32 v80, v80\n"
"v_add_f32 v165, v165, v71\n"
"v_exp_f32 v81, v81\n"
"v_add_f32 v166, v166, v80\n"
"s_waitcnt lgkmcnt(3)\n"
"v_mfma_f32_32x32x16_bf16 a[96:111], a[204:207], v[32:35], a[96:111]\n"
"ds_read_b128 a[220:223], v175 offset:15360\n"
"v_exp_f32 v82, v82\n"
"v_add_f32 v167, v167, v81\n"
"v_exp_f32 v83, v83\n"
"v_mfma_f32_32x32x16_bf16 a[112:127], a[204:207], v[48:51], a[112:127]\n"
"v_add_f32 v166, v166, v82\n"
"v_exp_f32 v84, v84\n"
"v_add_f32 v167, v167, v83\n"
"v_exp_f32 v85, v85\n"
"s_waitcnt lgkmcnt(3)\n"
"v_mfma_f32_32x32x16_bf16 a[0:15], a[208:211], v[40:43], a[0:15]\n"
"ds_read_b128 a[192:195], v174 offset:0\n"
"v_add_f32 v166, v166, v84\n"
"v_exp_f32 v86, v86\n"
"v_add_f32 v167, v167, v85\n"
"v_mfma_f32_32x32x16_bf16 a[16:31], a[208:211], v[56:59], a[16:31]\n"
"v_exp_f32 v87, v87\n"
"v_add_f32 v166, v166, v86\n"
"v_exp_f32 v72, v72\n"
"v_add_f32 v167, v167, v87\n"
"s_waitcnt lgkmcnt(3)\n"
"v_mfma_f32_32x32x16_bf16 a[32:47], a[212:215], v[40:43], a[32:47]\n"
"ds_read_b128 a[196:199], v174 offset:1024\n"
"v_exp_f32 v73, v73\n"
"v_add_f32 v164, v164, v72\n"
"v_exp_f32 v74, v74\n"
"v_mfma_f32_32x32x16_bf16 a[48:63], a[212:215], v[56:59], a[48:63]\n"
"v_add_f32 v165, v165, v73\n"
"v_exp_f32 v75, v75\n"
"v_add_f32 v164, v164, v74\n"
"v_exp_f32 v76, v76\n"
"s_waitcnt lgkmcnt(3)\n"
"v_mfma_f32_32x32x16_bf16 a[64:79], a[216:219], v[40:43], a[64:79]\n"
"ds_read_b128 a[200:203], v174 offset:2048\n"
"v_add_f32 v165, v165, v75\n"
"v_exp_f32 v77, v77\n"
"v_add_f32 v164, v164, v76\n"
"v_mfma_f32_32x32x16_bf16 a[80:95], a[216:219], v[56:59], a[80:95]\n"
"v_exp_f32 v78, v78\n"
"v_add_f32 v165, v165, v77\n"
"v_exp_f32 v79, v79\n"
"v_add_f32 v164, v164, v78\n"
"s_waitcnt lgkmcnt(3)\n"
"v_mfma_f32_32x32x16_bf16 a[96:111], a[220:223], v[40:43], a[96:111]\n"
"ds_read_b128 a[204:207], v174 offset:3072\n"
"v_exp_f32 v88, v88\n"
"v_add_f32 v165, v165, v79\n"
"v_exp_f32 v89, v89\n"
"v_mfma_f32_32x32x16_bf16 a[112:127], a[220:223], v[56:59], a[112:127]\n"
"v_add_f32 v166, v166, v88\n"
"s_nop 0\n"
"v_add_f32 v167, v167, v89\n"
"s_cmp_lg_u32 s42, 0\n"
"s_cbranch_scc1 L_att_resc1\n"
"L_att_rescd1:\n"
"s_cmp_eq_u32 s33, s34\n"
"s_nop 0\n"
"s_cbranch_scc1 L_att_drain1\n"
"s_add_u32 s33, s33, 1\n"
"s_branch L_att_loop\n"
"L_att_drain0:\n"
"v_add_u32 v175, s41, v176\n"
"s_waitcnt lgkmcnt(0)\n"
"ds_read_b128 a[192:195], v175 offset:0\n"
"ds_read_b128 a[196:199], v175 offset:2048\n"
"ds_read_b128 a[200:203], v175 offset:4096\n"
"ds_read_b128 a[204:207], v175 offset:6144\n"
"s_waitcnt vmcnt(8)\n"
"s_barrier\n"
"v_cvt_pk_bf16_f32 v0, v0, v1\n"
"v_cvt_pk_bf16_f32 v1, v2, v3\n"
"v_cvt_pk_bf16_f32 v2, v4, v5\n"
"v_cvt_pk_bf16_f32 v3, v6, v7\n"
"v_cvt_pk_bf16_f32 v16, v16, v17\n"
"v_cvt_pk_bf16_f32 v17, v18, v19\n"
"v_cvt_pk_bf16_f32 v18, v20, v21\n"
"v_cvt_pk_bf16_f32 v19, v22, v23\n"
"s_waitcnt lgkmcnt(3)\n"
"v_mfma_f32_32x32x16_bf16 a[0:15], a[192:195], v[0:3], a[0:15]\n"
"ds_read_b128 a[208:211], v175 offset:1024\n"
"v_exp_f32 v26, v26\n"
"v_exp_f32 v27, v27\n"
"v_add_f32 v166, v166, v26\n"
"v_mfma_f32_32x32x16_bf16 a[16:31], a[192:195], v[16:19], a[16:31]\n"
"s_mov_b32 m0, s38\n"
"s_nop 0\n"
"buffer_load_dwordx4 v176, s[16:19], s36 offen offset:0 sc1 lds\n"
"v_exp_f32 v28, v28\n"
"v_add_f32 v167, v167, v27\n"
"s_waitcnt lgkmcnt(3)\n"
"v_mfma_f32_32x32x16_bf16 a[32:47], a[196:199], v[0:3], a[32:47]\n"
"ds_read_b128 a[212:215], v175 offset:3072\n"
"buffer_load_dwordx4 v176, s[16:19], s36 offen offset:1024 sc1 lds\n"
"v_exp_f32 v29, v29\n"
"v_add_f32 v166, v166, v28\n"
"v_mfma_f32_32x32x16_bf16 a[48:63], a[196:199], v[16:19], a[48:63]\n"
"buffer_load_dwordx4 v176, s[16:19], s36 offen offset:2048 sc1 lds\n"
"v_exp_f32 v30, v30\n"
"v_add_f32 v167, v167, v29\n"
"s_waitcnt lgkmcnt(3)\n"
"v_mfma_f32_32x32x16_bf16 a[64:79], a[200:203], v[0:3], a[64:79]\n"
"ds_read_b128 a[216:219], v175 offset:5120\n"
"buffer_load_dwordx4 v176, s[16:19], s36 offen offset:3072 sc1 lds\n"
"v_exp_f32 v31, v31\n"
"v_add_f32 v166, v166, v30\n"
"v_mfma_f32_32x32x16_bf16 a[80:95], a[200:203], v[16:19], a[80:95]\n"
"s_mov_b32 m0, s39\n"
"s_nop 0\n"
"buffer_load_dwordx4 v176, s[20:23], s37 offen offset:0 sc1 lds\n"
"s_nop 0\n"
"v_add_f32 v167, v167, v31\n"
"v_cvt_pk_bf16_f32 v8, v8, v9\n"
"s_waitcnt lgkmcnt(3)\n"
"v_mfma_f32_32x32x16_bf16 a[96:111], a[204:207], v[0:3], a[96:111]\n"
"ds_read_b128 a[220:223], v175 offset:7168\n"
"buffer_load_dwordx4 v176, s[20:23], s37 offen offset:1024 sc1 lds\n"
"v_cvt_pk_bf16_f32 v9, v10, v11\n"
"v_cvt_pk_bf16_f32 v10, v12, v13\n"
"v_cvt_pk_bf16_f32 v11, v14, v15\n"
"v_mfma_f32_32x32x16_bf16 a[112:127], a[204:207], v[16:19], a[112:127]\n"
"buffer_load_dwordx4 v176, s[20:23], s37 offen offset:2048 sc1 lds\n"
"v_cvt_pk_bf16_f32 v24, v24, v25\n"
"v_cvt_pk_bf16_f32 v25, v26, v27\n"
"v_cvt_pk_bf16_f32 v26, v28, v29\n"
"v_cvt_pk_bf16_f32 v27, v30, v31\n"
"s_nop 1\n"
"s_waitcnt lgkmcnt(3)\n"
"v_mfma_f32_32x32x16_bf16 a[0:15], a[208:211], v[8:11], a[0:15]\n"
"ds_read_b128 a[192:195], v175 offset:8192\n"
"buffer_load_dwordx4 v176, s[20:23], s37 offen offset:3072 sc1 lds\n"
"v_exp_f32 v32, v32\n"
"v_mfma_f32_32x32x16_bf16 a[16:31], a[208:211], v[24:27], a[16:31]\n"
"v_exp_f32 v33, v33\n"
"v_add_f32 v164, v164, v32\n"
"v_exp_f32 v34, v34\n"
"v_add_f32 v165, v165, v33\n"
"s_waitcnt lgkmcnt(3)\n"
"v_mfma_f32_32x32x16_bf16 a[32:47], a[212:215], v[8:11], a[32:47]\n"
"ds_read_b128 a[196:199], v175 offset:10240\n"
"s_add_u32 s36, s36, 0x4000\n"
"s_add_u32 s37, s37, 0x4000\n"
"s_add_u32 s38, s38, 0x4000\n"
"s_and_b32 s38, s38, 0xffff\n"
"v_exp_f32 v35, v35\n"
"v_add_f32 v164, v164, v34\n"
"v_mfma_f32_32x32x16_bf16 a[48:63], a[212:215], v[24:27], a[48:63]\n"
"s_add_u32 s39, s39, 0x4000\n"
"s_and_b32 s39, s39, 0xffff\n"
"s_or_b32 s39, s39, 0x10000\n"
"v_exp_f32 v36, v36\n"
"v_add_f32 v165, v165, v35\n"
"s_waitcnt lgkmcnt(3)\n"
"v_mfma_f32_32x32x16_bf16 a[64:79], a[216:219], v[8:11], a[64:79]\n"
"ds_read_b128 a[200:203], v175 offset:12288\n"
"v_exp_f32 v37, v37\n"
"v_add_f32 v164, v164, v36\n"
"v_exp_f32 v38, v38\n"
"v_mfma_f32_32x32x16_bf16 a[80:95], a[216:219], v[24:27], a[80:95]\n"
"v_add_f32 v165, v165, v37\n"
"v_exp_f32 v39, v39\n"
"v_add_f32 v164, v164, v38\n"
"v_exp_f32 v48, v48\n"
"s_waitcnt lgkmcnt(3)\n"
"v_mfma_f32_32x32x16_bf16 a[96:111], a[220:223], v[8:11], a[96:111]\n"
"ds_read_b128 a[204:207], v175 offset:14336\n"
"v_add_f32 v165, v165, v39\n"
"v_exp_f32 v49, v49\n"
"v_add_f32 v166, v166, v48\n"
"v_mfma_f32_32x32x16_bf16 a[112:127], a[220:223], v[24:27], a[112:127]\n"
"v_exp_f32 v50, v50\n"
"v_add_f32 v167, v167, v49\n"
"v_exp_f32 v51, v51\n"
"v_add_f32 v166, v166, v50\n"
"v_exp_f32 v52, v52\n"
"v_add_f32 v167, v167, v51\n"
"v_exp_f32 v53, v53\n"
"v_add_f32 v166, v166, v52\n"
"v_exp_f32 v54, v54\n"
"v_add_f32 v167, v167, v53\n"
"v_exp_f32 v55, v55\n"
"v_add_f32 v166, v166, v54\n"
"s_nop 0\n"
"v_add_f32 v167, v167, v55\n"
"v_cvt_pk_bf16_f32 v32, v32, v33\n"
"v_cvt_pk_bf16_f32 v33, v34, v35\n"
"v_cvt_pk_bf16_f32 v34, v36, v37\n"
"v_cvt_pk_bf16_f32 v35, v38, v39\n"
"v_cvt_pk_bf16_f32 v48, v48, v49\n"
"v_cvt_pk_bf16_f32 v49, v50, v51\n"
"v_cvt_pk_bf16_f32 v50, v52, v53\n"
"v_cvt_pk_bf16_f32 v51, v54, v55\n"
"s_nop 1\n"
"s_waitcnt lgkmcnt(3)\n"
"v_mfma_f32_32x32x16_bf16 a[0:15], a[192:195], v[32:35], a[0:15]\n"
"ds_read_b128 a[208:211], v175 offset:9216\n"
"v_exp_f32 v40, v40\n"
"v_exp_f32 v41, v41\n"
"v_add_f32 v164, v164, v40\n"
"v_mfma_f32_32x32x16_bf16 a[16:31], a[192:195], v[48:51], a[16:31]\n"
"v_exp_f32 v42, v42\n"
"v_add_f32 v165, v165, v41\n"
"v_exp_f32 v43, v43\n"
"v_add_f32 v164, v164, v42\n"
"s_waitcnt lgkmcnt(3)\n"
"v_mfma_f32_32x32x16_bf16 a[32:47], a[196:199], v[32:35], a[32:47]\n"
"ds_read_b128 a[212:215], v175 offset:11264\n"
"v_exp_f32 v44, v44\n"
"v_add_f32 v165, v165, v43\n"
"v_exp_f32 v45, v45\n"
"v_mfma_f32_32x32x16_bf16 a[48:63], a[196:199], v[48:51], a[48:63]\n"
"v_add_f32 v164, v164, v44\n"
"v_exp_f32 v46, v46\n"
"v_add_f32 v165, v165, v45\n"
"v_exp_f32 v47, v47\n"
"s_waitcnt lgkmcnt(3)\n"
"v_mfma_f32_32x32x16_bf16 a[64:79], a[200:203], v[32:35], a[64:79]\n"
"ds_read_b128 a[216:219], v175 offset:13312\n"
"v_add_f32 v164, v164, v46\n"
"v_exp_f32 v56, v56\n"
"v_add_f32 v165, v165, v47\n"
"v_mfma_f32_32x32x16_bf16 a[80:95], a[200:203], v[48:51], a[80:95]\n"
"v_exp_f32 v57, v57\n"
"v_add_f32 v166, v166, v56\n"
"v_exp_f32 v58, v58\n"
"v_add_f32 v167, v167, v57\n"
"s_waitcnt lgkmcnt(3)\n"
"v_mfma_f32_32x32x16_bf16 a[96:111], a[204:207], v[32:35], a[96:111]\n"
"ds_read_b128 a[220:223], v175 offset:15360\n"
"v_exp_f32 v59, v59\n"
"v_add_f32 v166, v166, v58\n"
"v_exp_f32 v60, v60\n"
"v_mfma_f32_32x32x16_bf16 a[112:127], a[204:207], v[48:51], a[112:127]\n"
"v_add_f32 v167, v167, v59\n"
"v_exp_f32 v61, v61\n"
"v_add_f32 v166, v166, v60\n"
"v_exp_f32 v62, v62\n"
"v_add_f32 v167, v167, v61\n"
"v_exp_f32 v63, v63\n"
"v_add_f32 v166, v166, v62\n"
"s_nop 0\n"
"v_add_f32 v167, v167, v63\n"
"v_cvt_pk_bf16_f32 v40, v40, v41\n"
"v_cvt_pk_bf16_f32 v41, v42, v43\n"
"v_cvt_pk_bf16_f32 v42, v44, v45\n"
"v_cvt_pk_bf16_f32 v43, v46, v47\n"
"v_cvt_pk_bf16_f32 v56, v56, v57\n"
"v_cvt_pk_bf16_f32 v57, v58, v59\n"
"v_cvt_pk_bf16_f32 v58, v60, v61\n"
"v_cvt_pk_bf16_f32 v59, v62, v63\n"
"s_nop 1\n"
"s_waitcnt lgkmcnt(3)\n"
"v_mfma_f32_32x32x16_bf16 a[0:15], a[208:211], v[40:43], a[0:15]\n"
"v_mfma_f32_32x32x16_bf16 a[16:31], a[208:211], v[56:59], a[16:31]\n"
"s_waitcnt lgkmcnt(2)\n"
"v_mfma_f32_32x32x16_bf16 a[32:47], a[212:215], v[40:43], a[32:47]\n"
"v_add_f32 v162, v162, v164\n"
"v_add_f32 v162, v162, v165\n"
"v_mfma_f32_32x32x16_bf16 a[48:63], a[212:215], v[56:59], a[48:63]\n"
"v_add_f32 v163, v163, v166\n"
"v_add_f32 v163, v163, v167\n"
"s_waitcnt lgkmcnt(1)\n"
"v_mfma_f32_32x32x16_bf16 a[64:79], a[216:219], v[40:43], a[64:79]\n"
"v_mfma_f32_32x32x16_bf16 a[80:95], a[216:219], v[56:59], a[80:95]\n"
"s_waitcnt lgkmcnt(0)\n"
"v_mfma_f32_32x32x16_bf16 a[96:111], a[220:223], v[40:43], a[96:111]\n"
"v_mfma_f32_32x32x16_bf16 a[112:127], a[220:223], v[56:59], a[112:127]\n"
"s_branch L_att_epi\n"
"L_att_drain1:\n"
"v_add_u32 v175, s41, v176\n"
"s_waitcnt lgkmcnt(0)\n"
"ds_read_b128 a[192:195], v175 offset:0\n"
"ds_read_b128 a[196:199], v175 offset:2048\n"
"ds_read_b128 a[200:203], v175 offset:4096\n"
"ds_read_b128 a[204:207], v175 offset:6144\n"
"s_waitcnt vmcnt(8)\n"
"s_barrier\n"
"v_cvt_pk_bf16_f32 v64, v64, v65\n"
"v_cvt_pk_bf16_f32 v65, v66, v67\n"
"v_cvt_pk_bf16_f32 v66, v68, v69\n"
"v_cvt_pk_bf16_f32 v67, v70, v71\n"
"v_cvt_pk_bf16_f32 v80, v80, v81\n"
"v_cvt_pk_bf16_f32 v81, v82, v83\n"
"v_cvt_pk_bf16_f32 v82, v84, v85\n"
"v_cvt_pk_bf16_f32 v83, v86, v87\n"
"s_waitcnt lgkmcnt(3)\n"
"v_mfma_f32_32x32x16_bf16 a[0:15], a[192:195], v[64:67], a[0:15]\n"
"ds_read_b128 a[208:211], v175 offset:1024\n"
"v_exp_f32 v90, v90\n"
"v_exp_f32 v91, v91\n"
"v_add_f32 v166, v166, v90\n"
"v_mfma_f32_32x32x16_bf16 a[16:31], a[192:195], v[80:83], a[16:31]\n"
"s_mov_b32 m0, s38\n"
"s_nop 0\n"
"buffer_load_dwordx4 v176, s[16:19], s36 offen offset:0 sc1 lds\n"
"v_exp_f32 v92, v92\n"
"v_add_f32 v167, v167, v91\n"
"s_waitcnt lgkmcnt(3)\n"
"v_mfma_f32_32x32x16_bf16 a[32:47], a[196:199], v[64:67], a[32:47]\n"
"ds_read_b128 a[212:215], v175 offset:3072\n"
"buffer_load_dwordx4 v176, s[16:19], s36 offen offset:1024 sc1 lds\n"
"v_exp_f32 v93, v93\n"
"v_add_f32 v166, v166, v92\n"
"v_mfma_f32_32x32x16_bf16 a[48:63], a[196:199], v[80:83], a[48:63]\n"
"buffer_load_dwordx4 v176, s[16:19], s36 offen offset:2048 sc1 lds\n"
"v_exp_f32 v94, v94\n"
"v_add_f32 v167, v167, v93\n"
"s_waitcnt lgkmcnt(3)\n"
"v_mfma_f32_32x32x16_bf16 a[64:79], a[200:203], v[64:67], a[64:79]\n"
"ds_read_b128 a[216:219], v175 offset:5120\n"
"buffer_load_dwordx4 v176, s[16:19], s36 offen offset:3072 sc1 lds\n"
"v_exp_f32 v95, v95\n"
"v_add_f32 v166, v166, v94\n"
"v_mfma_f32_32x32x16_bf16 a[80:95], a[200:203], v[80:83], a[80:95]\n"
"s_mov_b32 m0, s39\n"
"s_nop 0\n"
"buffer_load_dwordx4 v176, s[20:23], s37 offen offset:0 sc1 lds\n"
"s_nop 0\n"
"v_add_f32 v167, v167, v95\n"
"v_cvt_pk_bf16_f32 v72, v72, v73\n"
"s_waitcnt lgkmcnt(3)\n"
"v_mfma_f32_32x32x16_bf16 a[96:111], a[204:207], v[64:67], a[96:111]\n"
"ds_read_b128 a[220:223], v175 offset:7168\n"
"buffer_load_dwordx4 v176, s[20:23], s37 offen offset:1024 sc1 lds\n"
"v_cvt_pk_bf16_f32 v73, v74, v75\n"
"v_cvt_pk_bf16_f32 v74, v76, v77\n"
"v_cvt_pk_bf16_f32 v75, v78, v79\n"
"v_mfma_f32_32x32x16_bf16 a[112:127], a[204:207], v[80:83], a[112:127]\n"
"buffer_load_dwordx4 v176, s[20:23], s37 offen offset:2048 sc1 lds\n"
"v_cvt_pk_bf16_f32 v88, v88, v89\n"
"v_cvt_pk_bf16_f32 v89, v90, v91\n"
"v_cvt_pk_bf16_f32 v90, v92, v93\n"
"v_cvt_pk_bf16_f32 v91, v94, v95\n"
"s_nop 1\n"
"s_waitcnt lgkmcnt(3)\n"
"v_mfma_f32_32x32x16_bf16 a[0:15], a[208:211], v[72:75], a[0:15]\n"
"ds_read_b128 a[192:195], v175 offset:8192\n"
"buffer_load_dwordx4 v176, s[20:23], s37 offen offset:3072 sc1 lds\n"
"v_exp_f32 v96, v96\n"
"v_mfma_f32_32x32x16_bf16 a[16:31], a[208:211], v[88:91], a[16:31]\n"
"v_exp_f32 v97, v97\n"
"v_add_f32 v164, v164, v96\n"
"v_exp_f32 v98, v98\n"
"v_add_f32 v165, v165, v97\n"
"s_waitcnt lgkmcnt(3)\n"
"v_mfma_f32_32x32x16_bf16 a[32:47], a[212:215], v[72:75], a[32:47]\n"
"ds_read_b128 a[196:199], v175 offset:10240\n"
"s_add_u32 s36, s36, 0x4000\n"
"s_add_u32 s37, s37, 0x4000\n"
"s_add_u32 s38, s38, 0x4000\n"
"s_and_b32 s38, s38, 0xffff\n"
"v_exp_f32 v99, v99\n"
"v_add_f32 v164, v164, v98\n"
"v_mfma_f32_32x32x16_bf16 a[48:63], a[212:215], v[88:91], a[48:63]\n"
"s_add_u32 s39, s39, 0x4000\n"
"s_and_b32 s39, s39, 0xffff\n"
"s_or_b32 s39, s39, 0x10000\n"
"v_exp_f32 v100, v100\n"
"v_add_f32 v165, v165, v99\n"
"s_waitcnt lgkmcnt(3)\n"
"v_mfma_f32_32x32x16_bf16 a[64:79], a[216:219], v[72:75], a[64:79]\n"
"ds_read_b128 a[200:203], v175 offset:12288\n"
"v_exp_f32 v101, v101\n"
"v_add_f32 v164, v164, v100\n"
"v_exp_f32 v102, v102\n"
"v_mfma_f32_32x32x16_bf16 a[80:95], a[216:219], v[88:91], a[80:95]\n"
"v_add_f32 v165, v165, v101\n"
"v_exp_f32 v103, v103\n"
"v_add_f32 v164, v164, v102\n"
"v_exp_f32 v112, v112\n"
"s_waitcnt lgkmcnt(3)\n"
"v_mfma_f32_32x32x16_bf16 a[96:111], a[220:223], v[72:75], a[96:111]\n"
"ds_read_b128 a[204:207], v175 offset:14336\n"
"v_add_f32 v165, v165, v103\n"
"v_exp_f32 v113, v113\n"
"v_add_f32 v166, v166, v112\n"
"v_mfma_f32_32x32x16_bf16 a[112:127], a[220:223], v[88:91], a[112:127]\n"
"v_exp_f32 v114, v114\n"
"v_add_f32 v167, v167, v113\n"
"v_exp_f32 v115, v115\n"
"v_add_f32 v166, v166, v114\n"
"v_exp_f32 v116, v116\n"
"v_add_f32 v167, v167, v115\n"
"v_exp_f32 v117, v117\n"
"v_add_f32 v166, v166, v116\n"
"v_exp_f32 v118, v118\n"
"v_add_f32 v167, v167, v117\n"
"v_exp_f32 v119, v119\n"
"v_add_f32 v166, v166, v118\n"
"s_nop 0\n"
"v_add_f32 v167, v167, v119\n"
"v_cvt_pk_bf16_f32 v96, v96, v97\n"
"v_cvt_pk_bf16_f32 v97, v98, v99\n"
"v_cvt_pk_bf16_f32 v98, v100, v101\n"
"v_cvt_pk_bf16_f32 v99, v102, v103\n"
"v_cvt_pk_bf16_f32 v112, v112, v113\n"
"v_cvt_pk_bf16_f32 v113, v114, v115\n"
"v_cvt_pk_bf16_f32 v114, v116, v117\n"
"v_cvt_pk_bf16_f32 v115, v118, v119\n"
"s_nop 1\n"
"s_waitcnt lgkmcnt(3)\n"
"v_mfma_f32_32x32x16_bf16 a[0:15], a[192:195], v[96:99], a[0:15]\n"
"ds_read_b128 a[208:211], v175 offset:9216\n"
"v_exp_f32 v104, v104\n"
"v_exp_f32 v105, v105\n"
"v_add_f32 v164, v164, v104\n"
"v_mfma_f32_32x32x16_bf16 a[16:31], a[192:195], v[112:115], a[16:31]\n"
"v_exp_f32 v106, v106\n"
"v_add_f32 v165, v165, v105\n"
"v_exp_f32 v107, v107\n"
"v_add_f32 v164, v164, v106\n"
"s_waitcnt lgkmcnt(3)\n"
"v_mfma_f32_32x32x16_bf16 a[32:47], a[196:199], v[96:99], a[32:47]\n"
"ds_read_b128 a[212:215], v175 offset:11264\n"
"v_exp_f32 v108, v108\n"
"v_add_f32 v165, v165, v107\n"
"v_exp_f32 v109, v109\n"
"v_mfma_f32_32x32x16_bf16 a[48:63], a[196:199], v[112:115], a[48:63]\n"
"v_add_f32 v164, v164, v108\n"
"v_exp_f32 v110, v110\n"
"v_add_f32 v165, v165, v109\n"
"v_exp_f32 v111, v111\n"
"s_waitcnt lgkmcnt(3)\n"
"v_mfma_f32_32x32x16_bf16 a[64:79], a[200:203], v[96:99], a[64:79]\n"
"ds_read_b128 a[216:219], v175 offset:13312\n"
"v_add_f32 v164, v164, v110\n"
"v_exp_f32 v120, v120\n"
"v_add_f32 v165, v165, v111\n"
"v_mfma_f32_32x32x16_bf16 a[80:95], a[200:203], v[112:115], a[80:95]\n"
"v_exp_f32 v121, v121\n"
"v_add_f32 v166, v166, v120\n"
"v_exp_f32 v122, v122\n"
"v_add_f32 v167, v167, v121\n"
"s_waitcnt lgkmcnt(3)\n"
"v_mfma_f32_32x32x16_bf16 a[96:111], a[204:207], v[96:99], a[96:111]\n"
"ds_read_b128 a[220:223], v175 offset:15360\n"
"v_exp_f32 v123, v123\n"
"v_add_f32 v166, v166, v122\n"
"v_exp_f32 v124, v124\n"
"v_mfma_f32_32x32x16_bf16 a[112:127], a[204:207], v[112:115], a[112:127]\n"
"v_add_f32 v167, v167, v123\n"
"v_exp_f32 v125, v125\n"
"v_add_f32 v166, v166, v124\n"
"v_exp_f32 v126, v126\n"
"v_add_f32 v167, v167, v125\n"
"v_exp_f32 v127, v127\n"
"v_add_f32 v166, v166, v126\n"
"s_nop 0\n"
"v_add_f32 v167, v167, v127\n"
"v_cvt_pk_bf16_f32 v104, v104, v105\n"
"v_cvt_pk_bf16_f32 v105, v106, v107\n"
"v_cvt_pk_bf16_f32 v106, v108, v109\n"
"v_cvt_pk_bf16_f32 v107, v110, v111\n"
"v_cvt_pk_bf16_f32 v120, v120, v121\n"
"v_cvt_pk_bf16_f32 v121, v122, v123\n"
"v_cvt_pk_bf16_f32 v122, v124, v125\n"
"v_cvt_pk_bf16_f32 v123, v126, v127\n"
"s_nop 1\n"
"s_waitcnt lgkmcnt(3)\n"
"v_mfma_f32_32x32x16_bf16 a[0:15], a[208:211], v[104:107], a[0:15]\n"
"v_mfma_f32_32x32x16_bf16 a[16:31], a[208:211], v[120:123], a[16:31]\n"
"s_waitcnt lgkmcnt(2)\n"
"v_mfma_f32_32x32x16_bf16 a[32:47], a[212:215], v[104:107], a[32:47]\n"
"v_add_f32 v162, v162, v164\n"
"v_add_f32 v162, v162, v165\n"
"v_mfma_f32_32x32x16_bf16 a[48:63], a[212:215], v[120:123], a[48:63]\n"
"v_add_f32 v163, v163, v166\n"
"v_add_f32 v163, v163, v167\n"
"s_waitcnt lgkmcnt(1)\n"
"v_mfma_f32_32x32x16_bf16 a[64:79], a[216:219], v[104:107], a[64:79]\n"
"v_mfma_f32_32x32x16_bf16 a[80:95], a[216:219], v[120:123], a[80:95]\n"
"s_waitcnt lgkmcnt(0)\n"
"v_mfma_f32_32x32x16_bf16 a[96:111], a[220:223], v[104:107], a[96:111]\n"
"v_mfma_f32_32x32x16_bf16 a[112:127], a[220:223], v[120:123], a[112:127]\n"
"L_att_epi:\n"
"s_add_u32 s34, s34, 1\n"
"s_cmp_ge_u32 s34, s35\n"
"s_cbranch_scc1 L_att_epi2\n"
"s_waitcnt vmcnt(0)\n"
"s_barrier\n"
"L_att_epi2:\n"
"v_mov_b32 v178, v162\n"
"s_nop 1\n"
"v_permlane32_swap_b32 v178, v162\n"
"v_add_f32 v162, v162, v178\n"
"v_rcp_f32 v162, v162\n"
"v_mov_b32 v178, v163\n"
"s_nop 1\n"
"v_permlane32_swap_b32 v178, v163\n"
"v_add_f32 v163, v163, v178\n"
"v_rcp_f32 v163, v163\n"
"s_nop 4\n"
"s_mov_b32 s46, 0\n"
"s_mov_b32 s47, 4096\n"
"v_accvgpr_read_b32 v0, a0\n"
"v_accvgpr_read_b32 v1, a1\n"
"v_accvgpr_read_b32 v2, a2\n"
"v_accvgpr_read_b32 v3, a3\n"
"v_accvgpr_read_b32 v4, a4\n"
"v_accvgpr_read_b32 v5, a5\n"
"v_accvgpr_read_b32 v6, a6\n"
"v_accvgpr_read_b32 v7, a7\n"
"v_pk_mul_f32 v[0:1], v[0:1], v[162:163] op_sel:[0,0] op_sel_hi:[1,0]\n"
"v_pk_mul_f32 v[2:3], v[2:3], v[162:163] op_sel:[0,0] op_sel_hi:[1,0]\n"
"v_pk_mul_f32 v[4:5], v[4:5], v[162:163] op_sel:[0,0] op_sel_hi:[1,0]\n"
"v_pk_mul_f32 v[6:7], v[6:7], v[162:163] op_sel:[0,0] op_sel_hi:[1,0]\n"
"v_cvt_pk_bf16_f32 v8, v0, v1\n"
"v_cvt_pk_bf16_f32 v9, v2, v3\n"
"v_cvt_pk_bf16_f32 v10, v4, v5\n"
"v_cvt_pk_bf16_f32 v11, v6, v7\n"
"buffer_store_dwordx4 v[8:11], v176, s[28:31], s46 offen offset:0 sc1\n"
"v_accvgpr_read_b32 v16, a8\n"
"v_accvgpr_read_b32 v17, a9\n"
"v_accvgpr_read_b32 v18, a10\n"
"v_accvgpr_read_b32 v19, a11\n"
"v_accvgpr_read_b32 v20, a12\n"
"v_accvgpr_read_b32 v21, a13\n"
"v_accvgpr_read_b32 v22, a14\n"
"v_accvgpr_read_b32 v23, a15\n"
"v_pk_mul_f32 v[16:17], v[16:17], v[162:163] op_sel:[0,0] op_sel_hi:[1,0]\n"
"v_pk_mul_f32 v[18:19], v[18:19], v[162:163] op_sel:[0,0] op_sel_hi:[1,0]\n"
"v_pk_mul_f32 v[20:21], v[20:21], v[162:163] op_sel:[0,0] op_sel_hi:[1,0]\n"
"v_pk_mul_f32 v[22:23], v[22:23], v[162:163] op_sel:[0,0] op_sel_hi:[1,0]\n"
"v_cvt_pk_bf16_f32 v24, v16, v17\n"
"v_cvt_pk_bf16_f32 v25, v18, v19\n"
"v_cvt_pk_bf16_f32 v26, v20, v21\n"
"v_cvt_pk_bf16_f32 v27, v22, v23\n"
"buffer_store_dwordx4 v[24:27], v176, s[28:31], s46 offen offset:1024 sc1\n"
"v_accvgpr_read_b32 v32, a32\n"
"v_accvgpr_read_b32 v33, a33\n"
"v_accvgpr_read_b32 v34, a34\n"
"v_accvgpr_read_b32 v35, a35\n"
"v_accvgpr_read_b32 v36, a36\n"
"v_accvgpr_read_b32 v37, a37\n"
"v_accvgpr_read_b32 v38, a38\n"
"v_accvgpr_read_b32 v39, a39\n"
"v_pk_mul_f32 v[32:33], v[32:33], v[162:163] op_sel:[0,0] op_sel_hi:[1,0]\n"
"v_pk_mul_f32 v[34:35], v[34:35], v[162:163] op_sel:[0,0] op_sel_hi:[1,0]\n"
"v_pk_mul_f32 v[36:37], v[36:37], v[162:163] op_sel:[0,0] op_sel_hi:[1,0]\n"
"v_pk_mul_f32 v[38:39], v[38:39], v[162:163] op_sel:[0,0] op_sel_hi:[1,0]\n"
"v_cvt_pk_bf16_f32 v40, v32, v33\n"
"v_cvt_pk_bf16_f32 v41, v34, v35\n"
"v_cvt_pk_bf16_f32 v42, v36, v37\n"
"v_cvt_pk_bf16_f32 v43, v38, v39\n"
"buffer_store_dwordx4 v[40:43], v176, s[28:31], s46 offen offset:2048 sc1\n"
"v_accvgpr_read_b32 v48, a40\n"
"v_accvgpr_read_b32 v49, a41\n"
"v_accvgpr_read_b32 v50, a42\n"
"v_accvgpr_read_b32 v51, a43\n"
"v_accvgpr_read_b32 v52, a44\n"
"v_accvgpr_read_b32 v53, a45\n"
"v_accvgpr_read_b32 v54, a46\n"
"v_accvgpr_read_b32 v55, a47\n"
"v_pk_mul_f32 v[48:49], v[48:49], v[162:163] op_sel:[0,0] op_sel_hi:[1,0]\n"
"v_pk_mul_f32 v[50:51], v[50:51], v[162:163] op_sel:[0,0] op_sel_hi:[1,0]\n"
"v_pk_mul_f32 v[52:53], v[52:53], v[162:163] op_sel:[0,0] op_sel_hi:[1,0]\n"
"v_pk_mul_f32 v[54:55], v[54:55], v[162:163] op_sel:[0,0] op_sel_hi:[1,0]\n"
"v_cvt_pk_bf16_f32 v56, v48, v49\n"
"v_cvt_pk_bf16_f32 v57, v50, v51\n"
"v_cvt_pk_bf16_f32 v58, v52, v53\n"
"v_cvt_pk_bf16_f32 v59, v54, v55\n"
"buffer_store_dwordx4 v[56:59], v176, s[28:31], s46 offen offset:3072 sc1\n"
"v_accvgpr_read_b32 v0, a64\n"
"v_accvgpr_read_b32 v1, a65\n"
"v_accvgpr_read_b32 v2, a66\n"
"v_accvgpr_read_b32 v3, a67\n"
"v_accvgpr_read_b32 v4, a68\n"
"v_accvgpr_read_b32 v5, a69\n"
"v_accvgpr_read_b32 v6, a70\n"
"v_accvgpr_read_b32 v7, a71\n"
"v_pk_mul_f32 v[0:1], v[0:1], v[162:163] op_sel:[0,0] op_sel_hi:[1,0]\n"
"v_pk_mul_f32 v[2:3], v[2:3], v[162:163] op_sel:[0,0] op_sel_hi:[1,0]\n"
"v_pk_mul_f32 v[4:5], v[4:5], v[162:163] op_sel:[0,0] op_sel_hi:[1,0]\n"
"v_pk_mul_f32 v[6:7], v[6:7], v[162:163] op_sel:[0,0] op_sel_hi:[1,0]\n"
"v_cvt_pk_bf16_f32 v8, v0, v1\n"
"v_cvt_pk_bf16_f32 v9, v2, v3\n"
"v_cvt_pk_bf16_f32 v10, v4, v5\n"
"v_cvt_pk_bf16_f32 v11, v6, v7\n"
"buffer_store_dwordx4 v[8:11], v176, s[28:31], s47 offen offset:0 sc1\n"
"v_accvgpr_read_b32 v16, a72\n"
"v_accvgpr_read_b32 v17, a73\n"
"v_accvgpr_read_b32 v18, a74\n"
"v_accvgpr_read_b32 v19, a75\n"
"v_accvgpr_read_b32 v20, a76\n"
"v_accvgpr_read_b32 v21, a77\n"
"v_accvgpr_read_b32 v22, a78\n"
"v_accvgpr_read_b32 v23, a79\n"
"v_pk_mul_f32 v[16:17], v[16:17], v[162:163] op_sel:[0,0] op_sel_hi:[1,0]\n"
"v_pk_mul_f32 v[18:19], v[18:19], v[162:163] op_sel:[0,0] op_sel_hi:[1,0]\n"
"v_pk_mul_f32 v[20:21], v[20:21], v[162:163] op_sel:[0,0] op_sel_hi:[1,0]\n"
"v_pk_mul_f32 v[22:23], v[22:23], v[162:163] op_sel:[0,0] op_sel_hi:[1,0]\n"
"v_cvt_pk_bf16_f32 v24, v16, v17\n"
"v_cvt_pk_bf16_f32 v25, v18, v19\n"
"v_cvt_pk_bf16_f32 v26, v20, v21\n"
"v_cvt_pk_bf16_f32 v27, v22, v23\n"
"buffer_store_dwordx4 v[24:27], v176, s[28:31], s47 offen offset:1024 sc1\n"
"v_accvgpr_read_b32 v32, a96\n"
"v_accvgpr_read_b32 v33, a97\n"
"v_accvgpr_read_b32 v34, a98\n"
"v_accvgpr_read_b32 v35, a99\n"
"v_accvgpr_read_b32 v36, a100\n"
"v_accvgpr_read_b32 v37, a101\n"
"v_accvgpr_read_b32 v38, a102\n"
"v_accvgpr_read_b32 v39, a103\n"
"v_pk_mul_f32 v[32:33], v[32:33], v[162:163] op_sel:[0,0] op_sel_hi:[1,0]\n"
"v_pk_mul_f32 v[34:35], v[34:35], v[162:163] op_sel:[0,0] op_sel_hi:[1,0]\n"
"v_pk_mul_f32 v[36:37], v[36:37], v[162:163] op_sel:[0,0] op_sel_hi:[1,0]\n"
"v_pk_mul_f32 v[38:39], v[38:39], v[162:163] op_sel:[0,0] op_sel_hi:[1,0]\n"
"v_cvt_pk_bf16_f32 v40, v32, v33\n"
"v_cvt_pk_bf16_f32 v41, v34, v35\n"
"v_cvt_pk_bf16_f32 v42, v36, v37\n"
"v_cvt_pk_bf16_f32 v43, v38, v39\n"
"buffer_store_dwordx4 v[40:43], v176, s[28:31], s47 offen offset:2048 sc1\n"
"v_accvgpr_read_b32 v48, a104\n"
"v_accvgpr_read_b32 v49, a105\n"
"v_accvgpr_read_b32 v50, a106\n"
"v_accvgpr_read_b32 v51, a107\n"
"v_accvgpr_read_b32 v52, a108\n"
"v_accvgpr_read_b32 v53, a109\n"
"v_accvgpr_read_b32 v54, a110\n"
"v_accvgpr_read_b32 v55, a111\n"
"v_pk_mul_f32 v[48:49], v[48:49], v[162:163] op_sel:[0,0] op_sel_hi:[1,0]\n"
"v_pk_mul_f32 v[50:51], v[50:51], v[162:163] op_sel:[0,0] op_sel_hi:[1,0]\n"
"v_pk_mul_f32 v[52:53], v[52:53], v[162:163] op_sel:[0,0] op_sel_hi:[1,0]\n"
"v_pk_mul_f32 v[54:55], v[54:55], v[162:163] op_sel:[0,0] op_sel_hi:[1,0]\n"
"v_cvt_pk_bf16_f32 v56, v48, v49\n"
"v_cvt_pk_bf16_f32 v57, v50, v51\n"
"v_cvt_pk_bf16_f32 v58, v52, v53\n"
"v_cvt_pk_bf16_f32 v59, v54, v55\n"
"buffer_store_dwordx4 v[56:59], v176, s[28:31], s47 offen offset:3072 sc1\n"
"s_mov_b32 s46, 262144\n"
"s_mov_b32 s47, 266240\n"
"v_accvgpr_read_b32 v0, a16\n"
"v_accvgpr_read_b32 v1, a17\n"
"v_accvgpr_read_b32 v2, a18\n"
"v_accvgpr_read_b32 v3, a19\n"
"v_accvgpr_read_b32 v4, a20\n"
"v_accvgpr_read_b32 v5, a21\n"
"v_accvgpr_read_b32 v6, a22\n"
"v_accvgpr_read_b32 v7, a23\n"
"v_pk_mul_f32 v[0:1], v[0:1], v[162:163] op_sel:[0,1] op_sel_hi:[1,1]\n"
"v_pk_mul_f32 v[2:3], v[2:3], v[162:163] op_sel:[0,1] op_sel_hi:[1,1]\n"
"v_pk_mul_f32 v[4:5], v[4:5], v[162:163] op_sel:[0,1] op_sel_hi:[1,1]\n"
"v_pk_mul_f32 v[6:7], v[6:7], v[162:163] op_sel:[0,1] op_sel_hi:[1,1]\n"
"v_cvt_pk_bf16_f32 v8, v0, v1\n"
"v_cvt_pk_bf16_f32 v9, v2, v3\n"
"v_cvt_pk_bf16_f32 v10, v4, v5\n"
"v_cvt_pk_bf16_f32 v11, v6, v7\n"
"buffer_store_dwordx4 v[8:11], v176, s[28:31], s46 offen offset:0 sc1\n"
"v_accvgpr_read_b32 v16, a24\n"
"v_accvgpr_read_b32 v17, a25\n"
"v_accvgpr_read_b32 v18, a26\n"
"v_accvgpr_read_b32 v19, a27\n"
"v_accvgpr_read_b32 v20, a28\n"
"v_accvgpr_read_b32 v21, a29\n"
"v_accvgpr_read_b32 v22, a30\n"
"v_accvgpr_read_b32 v23, a31\n"
"v_pk_mul_f32 v[16:17], v[16:17], v[162:163] op_sel:[0,1] op_sel_hi:[1,1]\n"
"v_pk_mul_f32 v[18:19], v[18:19], v[162:163] op_sel:[0,1] op_sel_hi:[1,1]\n"
"v_pk_mul_f32 v[20:21], v[20:21], v[162:163] op_sel:[0,1] op_sel_hi:[1,1]\n"
"v_pk_mul_f32 v[22:23], v[22:23], v[162:163] op_sel:[0,1] op_sel_hi:[1,1]\n"
"v_cvt_pk_bf16_f32 v24, v16, v17\n"
"v_cvt_pk_bf16_f32 v25, v18, v19\n"
"v_cvt_pk_bf16_f32 v26, v20, v21\n"
"v_cvt_pk_bf16_f32 v27, v22, v23\n"
"buffer_store_dwordx4 v[24:27], v176, s[28:31], s46 offen offset:1024 sc1\n"
"v_accvgpr_read_b32 v32, a48\n"
"v_accvgpr_read_b32 v33, a49\n"
"v_accvgpr_read_b32 v34, a50\n"
"v_accvgpr_read_b32 v35, a51\n"
"v_accvgpr_read_b32 v36, a52\n"
"v_accvgpr_read_b32 v37, a53\n"
"v_accvgpr_read_b32 v38, a54\n"
"v_accvgpr_read_b32 v39, a55\n"
"v_pk_mul_f32 v[32:33], v[32:33], v[162:163] op_sel:[0,1] op_sel_hi:[1,1]\n"
"v_pk_mul_f32 v[34:35], v[34:35], v[162:163] op_sel:[0,1] op_sel_hi:[1,1]\n"
"v_pk_mul_f32 v[36:37], v[36:37], v[162:163] op_sel:[0,1] op_sel_hi:[1,1]\n"
"v_pk_mul_f32 v[38:39], v[38:39], v[162:163] op_sel:[0,1] op_sel_hi:[1,1]\n"
"v_cvt_pk_bf16_f32 v40, v32, v33\n"
"v_cvt_pk_bf16_f32 v41, v34, v35\n"
"v_cvt_pk_bf16_f32 v42, v36, v37\n"
"v_cvt_pk_bf16_f32 v43, v38, v39\n"
"buffer_store_dwordx4 v[40:43], v176, s[28:31], s46 offen offset:2048 sc1\n"
"v_accvgpr_read_b32 v48, a56\n"
"v_accvgpr_read_b32 v49, a57\n"
"v_accvgpr_read_b32 v50, a58\n"
"v_accvgpr_read_b32 v51, a59\n"
"v_accvgpr_read_b32 v52, a60\n"
"v_accvgpr_read_b32 v53, a61\n"
"v_accvgpr_read_b32 v54, a62\n"
"v_accvgpr_read_b32 v55, a63\n"
"v_pk_mul_f32 v[48:49], v[48:49], v[162:163] op_sel:[0,1] op_sel_hi:[1,1]\n"
"v_pk_mul_f32 v[50:51], v[50:51], v[162:163] op_sel:[0,1] op_sel_hi:[1,1]\n"
"v_pk_mul_f32 v[52:53], v[52:53], v[162:163] op_sel:[0,1] op_sel_hi:[1,1]\n"
"v_pk_mul_f32 v[54:55], v[54:55], v[162:163] op_sel:[0,1] op_sel_hi:[1,1]\n"
"v_cvt_pk_bf16_f32 v56, v48, v49\n"
"v_cvt_pk_bf16_f32 v57, v50, v51\n"
"v_cvt_pk_bf16_f32 v58, v52, v53\n"
"v_cvt_pk_bf16_f32 v59, v54, v55\n"
"buffer_store_dwordx4 v[56:59], v176, s[28:31], s46 offen offset:3072 sc1\n"
"v_accvgpr_read_b32 v0, a80\n"
"v_accvgpr_read_b32 v1, a81\n"
"v_accvgpr_read_b32 v2, a82\n"
"v_accvgpr_read_b32 v3, a83\n"
"v_accvgpr_read_b32 v4, a84\n"
"v_accvgpr_read_b32 v5, a85\n"
"v_accvgpr_read_b32 v6, a86\n"
"v_accvgpr_read_b32 v7, a87\n"
"v_pk_mul_f32 v[0:1], v[0:1], v[162:163] op_sel:[0,1] op_sel_hi:[1,1]\n"
"v_pk_mul_f32 v[2:3], v[2:3], v[162:163] op_sel:[0,1] op_sel_hi:[1,1]\n"
"v_pk_mul_f32 v[4:5], v[4:5], v[162:163] op_sel:[0,1] op_sel_hi:[1,1]\n"
"v_pk_mul_f32 v[6:7], v[6:7], v[162:163] op_sel:[0,1] op_sel_hi:[1,1]\n"
"v_cvt_pk_bf16_f32 v8, v0, v1\n"
"v_cvt_pk_bf16_f32 v9, v2, v3\n"
"v_cvt_pk_bf16_f32 v10, v4, v5\n"
"v_cvt_pk_bf16_f32 v11, v6, v7\n"
"buffer_store_dwordx4 v[8:11], v176, s[28:31], s47 offen offset:0 sc1\n"
"v_accvgpr_read_b32 v16, a88\n"
"v_accvgpr_read_b32 v17, a89\n"
"v_accvgpr_read_b32 v18, a90\n"
"v_accvgpr_read_b32 v19, a91\n"
"v_accvgpr_read_b32 v20, a92\n"
"v_accvgpr_read_b32 v21, a93\n"
"v_accvgpr_read_b32 v22, a94\n"
"v_accvgpr_read_b32 v23, a95\n"
"v_pk_mul_f32 v[16:17], v[16:17], v[162:163] op_sel:[0,1] op_sel_hi:[1,1]\n"
"v_pk_mul_f32 v[18:19], v[18:19], v[162:163] op_sel:[0,1] op_sel_hi:[1,1]\n"
"v_pk_mul_f32 v[20:21], v[20:21], v[162:163] op_sel:[0,1] op_sel_hi:[1,1]\n"
"v_pk_mul_f32 v[22:23], v[22:23], v[162:163] op_sel:[0,1] op_sel_hi:[1,1]\n"
"v_cvt_pk_bf16_f32 v24, v16, v17\n"
"v_cvt_pk_bf16_f32 v25, v18, v19\n"
"v_cvt_pk_bf16_f32 v26, v20, v21\n"
"v_cvt_pk_bf16_f32 v27, v22, v23\n"
"buffer_store_dwordx4 v[24:27], v176, s[28:31], s47 offen offset:1024 sc1\n"
"v_accvgpr_read_b32 v32, a112\n"
"v_accvgpr_read_b32 v33, a113\n"
"v_accvgpr_read_b32 v34, a114\n"
"v_accvgpr_read_b32 v35, a115\n"
"v_accvgpr_read_b32 v36, a116\n"
"v_accvgpr_read_b32 v37, a117\n"
"v_accvgpr_read_b32 v38, a118\n"
"v_accvgpr_read_b32 v39, a119\n"
"v_pk_mul_f32 v[32:33], v[32:33], v[162:163] op_sel:[0,1] op_sel_hi:[1,1]\n"
"v_pk_mul_f32 v[34:35], v[34:35], v[162:163] op_sel:[0,1] op_sel_hi:[1,1]\n"
"v_pk_mul_f32 v[36:37], v[36:37], v[162:163] op_sel:[0,1] op_sel_hi:[1,1]\n"
"v_pk_mul_f32 v[38:39], v[38:39], v[162:163] op_sel:[0,1] op_sel_hi:[1,1]\n"
"v_cvt_pk_bf16_f32 v40, v32, v33\n"
"v_cvt_pk_bf16_f32 v41, v34, v35\n"
"v_cvt_pk_bf16_f32 v42, v36, v37\n"
"v_cvt_pk_bf16_f32 v43, v38, v39\n"
"buffer_store_dwordx4 v[40:43], v176, s[28:31], s47 offen offset:2048 sc1\n"
"v_accvgpr_read_b32 v48, a120\n"
"v_accvgpr_read_b32 v49, a121\n"
"v_accvgpr_read_b32 v50, a122\n"
"v_accvgpr_read_b32 v51, a123\n"
"v_accvgpr_read_b32 v52, a124\n"
"v_accvgpr_read_b32 v53, a125\n"
"v_accvgpr_read_b32 v54, a126\n"
"v_accvgpr_read_b32 v55, a127\n"
"v_pk_mul_f32 v[48:49], v[48:49], v[162:163] op_sel:[0,1] op_sel_hi:[1,1]\n"
"v_pk_mul_f32 v[50:51], v[50:51], v[162:163] op_sel:[0,1] op_sel_hi:[1,1]\n"
"v_pk_mul_f32 v[52:53], v[52:53], v[162:163] op_sel:[0,1] op_sel_hi:[1,1]\n"
"v_pk_mul_f32 v[54:55], v[54:55], v[162:163] op_sel:[0,1] op_sel_hi:[1,1]\n"
"v_cvt_pk_bf16_f32 v56, v48, v49\n"
"v_cvt_pk_bf16_f32 v57, v50, v51\n"
"v_cvt_pk_bf16_f32 v58, v52, v53\n"
"v_cvt_pk_bf16_f32 v59, v54, v55\n"
"buffer_store_dwordx4 v[56:59], v176, s[28:31], s47 offen offset:3072 sc1\n"
"L_att_service:\n"
"s_add_u32 s34, s34, 1\n"
"s_cmp_ge_u32 s34, s35\n"
"s_cbranch_scc1 L_att_end\n"
"s_barrier\n"
"s_branch L_att_service\n"
"L_att_end:\n"
"s_waitcnt vmcnt(0)\n"
"v_mov_b32 v0, 1\n"
"s_lshl_b32 s3, s66, 7\n"
"s_add_u32 s3, s3, 0x2200\n"
"s_mov_b64 exec, 1\n"
"buffer_atomic_add v0, off, s[12:15], s3 sc1\n"
"s_mov_b64 exec, -1\n"
"s_branch L_next\n"
"L_att_mask0:\n"
"v_mov_b32 v178, 0xff800000\n"
"v_mov_b32 v179, 0xff800000\n"
"v_cmp_le_i32 s[46:47], 0, v177\n"
"v_cmp_le_i32 s[48:49], 1, v177\n"
"v_cmp_le_i32 s[50:51], 2, v177\n"
"v_cmp_le_i32 s[52:53], 3, v177\n"
"v_cmp_le_i32 s[54:55], 8, v177\n"
"v_cmp_le_i32 s[56:57], 9, v177\n"
"v_cmp_le_i32 s[58:59], 10, v177\n"
"v_cmp_le_i32 s[60:61], 11, v177\n"
"v_mov_b64 v[32:33], v[178:179]\n"
"v_mov_b64 v[34:35], v[178:179]\n"
"v_mov_b64 v[36:37], v[178:179]\n"
"v_mov_b64 v[38:39], v[178:179]\n"
"v_mov_b64 v[40:41], v[178:179]\n"
"v_mov_b64 v[42:43], v[178:179]\n"
"v_mov_b64 v[44:45], v[178:179]\n"
"v_mov_b64 v[46:47], v[178:179]\n"
"v_cndmask_b32 v0, v179, v0, s[46:47]\n"
"v_cndmask_b32 v48, v179, v48, s[46:47]\n"
"v_cmp_le_i32 s[46:47], 16, v177\n"
"v_cndmask_b32 v1, v179, v1, s[48:49]\n"
"v_cndmask_b32 v49, v179, v49, s[48:49]\n"
"v_cmp_le_i32 s[48:49], 17, v177\n"
"v_cndmask_b32 v2, v179, v2, s[50:51]\n"
"v_cndmask_b32 v50, v179, v50, s[50:51]\n"
"v_cmp_le_i32 s[50:51], 18, v177\n"
"v_cndmask_b32 v3, v179, v3, s[52:53]\n"
"v_cndmask_b32 v51, v179, v51, s[52:53]\n"
"v_cmp_le_i32 s[52:53], 19, v177\n"
"v_cndmask_b32 v4, v179, v4, s[54:55]\n"
"v_cndmask_b32 v52, v179, v52, s[54:55]\n"
"v_cmp_le_i32 s[54:55], 24, v177\n"
"v_cndmask_b32 v5, v179, v5, s[56:57]\n"
"v_cndmask_b32 v53, v179, v53, s[56:57]\n"
"v_cmp_le_i32 s[56:57], 25, v177\n"
"v_cndmask_b32 v6, v179, v6, s[58:59]\n"
"v_cndmask_b32 v54, v179, v54, s[58:59]\n"
"v_cmp_le_i32 s[58:59], 26, v177\n"
"v_cndmask_b32 v7, v179, v7, s[60:61]\n"
"v_cndmask_b32 v55, v179, v55, s[60:61]\n"
"v_cmp_le_i32 s[60:61], 27, v177\n"
"v_cndmask_b32 v8, v179, v8, s[46:47]\n"
"v_cndmask_b32 v56, v179, v56, s[46:47]\n"
"v_cndmask_b32 v9, v179, v9, s[48:49]\n"
"v_cndmask_b32 v57, v179, v57, s[48:49]\n"
"v_cndmask_b32 v10, v179, v10, s[50:51]\n"
"v_cndmask_b32 v58, v179, v58, s[50:51]\n"
"v_cndmask_b32 v11, v179, v11, s[52:53]\n"
"v_cndmask_b32 v59, v179, v59, s[52:53]\n"
"v_cndmask_b32 v12, v179, v12, s[54:55]\n"
"v_cndmask_b32 v60, v179, v60, s[54:55]\n"
"v_cndmask_b32 v13, v179, v13, s[56:57]\n"
"v_cndmask_b32 v61, v179, v61, s[56:57]\n"
"v_cndmask_b32 v14, v179, v14, s[58:59]\n"
"v_cndmask_b32 v62, v179, v62, s[58:59]\n"
"v_cndmask_b32 v15, v179, v15, s[60:61]\n"
"v_cndmask_b32 v63, v179, v63, s[60:61]\n"
"s_branch L_att_masked0\n"
"L_att_rare0:\n"
"v_max_f32 v168, s44, v168\n"
"v_add_f32 v160, v160, v168\n"
"v_exp_f32 v172, -v168\n"
"v_sub_f32 v0, v0, v168\n"
"v_sub_f32 v1, v1, v168\n"
"v_sub_f32 v2, v2, v168\n"
"v_sub_f32 v3, v3, v168\n"
"v_sub_f32 v4, v4, v168\n"
"v_sub_f32 v5, v5, v168\n"
"v_sub_f32 v6, v6, v168\n"
"v_sub_f32 v7, v7, v168\n"
"v_sub_f32 v8, v8, v168\n"
"v_sub_f32 v9, v9, v168\n"
"v_sub_f32 v10, v10, v168\n"
"v_sub_f32 v11, v11, v168\n"
"v_sub_f32 v12, v12, v168\n"
"v_sub_f32 v13, v13, v168\n"
"v_sub_f32 v14, v14, v168\n"
"v_sub_f32 v15, v15, v168\n"
"v_sub_f32 v32, v32, v168\n"
"v_sub_f32 v33, v33, v168\n"
"v_sub_f32 v34, v34, v168\n"
"v_sub_f32 v35, v35, v168\n"
"v_sub_f32 v36, v36, v168\n"
"v_sub_f32 v37, v37, v168\n"
"v_sub_f32 v38, v38, v168\n"
"v_sub_f32 v39, v39, v168\n"
"v_sub_f32 v40, v40, v168\n"
"v_sub_f32 v41, v41, v168\n"
"v_sub_f32 v42, v42, v168\n"
"v_sub_f32 v43, v43, v168\n"
"v_sub_f32 v44, v44, v168\n"
"v_sub_f32 v45, v45, v168\n"
"v_sub_f32 v46, v46, v168\n"
"v_sub_f32 v47, v47, v168\n"
"v_sub_f32 v128, 0, v160\n"
"v_sub_f32 v129, 0, v160\n"
"v_sub_f32 v130, 0, v160\n"
"v_sub_f32 v131, 0, v160\n"
"v_sub_f32 v132, 0, v160\n"
"v_sub_f32 v133, 0, v160\n"
"v_sub_f32 v134, 0, v160\n"
"v_sub_f32 v135, 0, v160\n"
"v_sub_f32 v136, 0, v160\n"
"v_sub_f32 v137, 0, v160\n"
"v_sub_f32 v138, 0, v160\n"
"v_sub_f32 v139, 0, v160\n"
"v_sub_f32 v140, 0, v160\n"
"v_sub_f32 v141, 0, v160\n"
"v_sub_f32 v142, 0, v160\n"
"v_sub_f32 v143, 0, v160\n"
"v_max_f32 v170, s44, v170\n"
"v_add_f32 v161, v161, v170\n"
"v_exp_f32 v173, -v170\n"
"v_sub_f32 v16, v16, v170\n"
"v_sub_f32 v17, v17, v170\n"
"v_sub_f32 v18, v18, v170\n"
"v_sub_f32 v19, v19, v170\n"
"v_sub_f32 v20, v20, v170\n"
"v_sub_f32 v21, v21, v170\n"
"v_sub_f32 v22, v22, v170\n"
"v_sub_f32 v23, v23, v170\n"
"v_sub_f32 v24, v24, v170\n"
"v_sub_f32 v25, v25, v170\n"
"v_sub_f32 v26, v26, v170\n"
"v_sub_f32 v27, v27, v170\n"
"v_sub_f32 v28, v28, v170\n"
"v_sub_f32 v29, v29, v170\n"
"v_sub_f32 v30, v30, v170\n"
"v_sub_f32 v31, v31, v170\n"
"v_sub_f32 v48, v48, v170\n"
"v_sub_f32 v49, v49, v170\n"
"v_sub_f32 v50, v50, v170\n"
"v_sub_f32 v51, v51, v170\n"
"v_sub_f32 v52, v52, v170\n"
"v_sub_f32 v53, v53, v170\n"
"v_sub_f32 v54, v54, v170\n"
"v_sub_f32 v55, v55, v170\n"
"v_sub_f32 v56, v56, v170\n"
"v_sub_f32 v57, v57, v170\n"
"v_sub_f32 v58, v58, v170\n"
"v_sub_f32 v59, v59, v170\n"
"v_sub_f32 v60, v60, v170\n"
"v_sub_f32 v61, v61, v170\n"
"v_sub_f32 v62, v62, v170\n"
"v_sub_f32 v63, v63, v170\n"
"v_sub_f32 v144, 0, v161\n"
"v_sub_f32 v145, 0, v161\n"
"v_sub_f32 v146, 0, v161\n"
"v_sub_f32 v147, 0, v161\n"
"v_sub_f32 v148, 0, v161\n"
"v_sub_f32 v149, 0, v161\n"
"v_sub_f32 v150, 0, v161\n"
"v_sub_f32 v151, 0, v161\n"
"v_sub_f32 v152, 0, v161\n"
"v_sub_f32 v153, 0, v161\n"
"v_sub_f32 v154, 0, v161\n"
"v_sub_f32 v155, 0, v161\n"
"v_sub_f32 v156, 0, v161\n"
"v_sub_f32 v157, 0, v161\n"
"v_sub_f32 v158, 0, v161\n"
"v_sub_f32 v159, 0, v161\n"
"s_cmp_eq_u32 s44, 0\n"
"s_cselect_b32 s42, 1, 0\n"
"s_mov_b32 s44, 0\n"
"s_mov_b32 s45, 0x41c00000\n"
"s_branch L_att_rared0\n"
"L_att_resc0:\n"
"s_nop 7\n"
"s_nop 3\n"
"v_accvgpr_read_b32 v178, a0\n"
"v_accvgpr_read_b32 v179, a1\n"
"v_mul_f32 v178, v178, v172\n"
"v_mul_f32 v179, v179, v172\n"
"v_accvgpr_write_b32 a0, v178\n"
"v_accvgpr_write_b32 a1, v179\n"
"v_accvgpr_read_b32 v178, a2\n"
"v_accvgpr_read_b32 v179, a3\n"
"v_mul_f32 v178, v178, v172\n"
"v_mul_f32 v179, v179, v172\n"
"v_accvgpr_write_b32 a2, v178\n"
"v_accvgpr_write_b32 a3, v179\n"
"v_accvgpr_read_b32 v178, a4\n"
"v_accvgpr_read_b32 v179, a5\n"
"v_mul_f32 v178, v178, v172\n"
"v_mul_f32 v179, v179, v172\n"
"v_accvgpr_write_b32 a4, v178\n"
"v_accvgpr_write_b32 a5, v179\n"
"v_accvgpr_read_b32 v178, a6\n"
"v_accvgpr_read_b32 v179, a7\n"
"v_mul_f32 v178, v178, v172\n"
"v_mul_f32 v179, v179, v172\n"
"v_accvgpr_write_b32 a6, v178\n"
"v_accvgpr_write_b32 a7, v179\n"
"v_accvgpr_read_b32 v178, a8\n"
"v_accvgpr_read_b32 v179, a9\n"
"v_mul_f32 v178, v178, v172\n"
"v_mul_f32 v179, v179, v172\n"
"v_accvgpr_write_b32 a8, v178\n"
"v_accvgpr_write_b32 a9, v179\n"
"v_accvgpr_read_b32 v178, a10\n"
"v_accvgpr_read_b32 v179, a11\n"
"v_mul_f32 v178, v178, v172\n"
"v_mul_f32 v179, v179, v172\n"
"v_accvgpr_write_b32 a10, v178\n"
"v_accvgpr_write_b32 a11, v179\n"
"v_accvgpr_read_b32 v178, a12\n"
"v_accvgpr_read_b32 v179, a13\n"
"v_mul_f32 v178, v178, v172\n"
"v_mul_f32 v179, v179, v172\n"
"v_accvgpr_write_b32 a12, v178\n"
"v_accvgpr_write_b32 a13, v179\n"
"v_accvgpr_read_b32 v178, a14\n"
"v_accvgpr_read_b32 v179, a15\n"
"v_mul_f32 v178, v178, v172\n"
"v_mul_f32 v179, v179, v172\n"
"v_accvgpr_write_b32 a14, v178\n"
"v_accvgpr_write_b32 a15, v179\n"
"v_accvgpr_read_b32 v178, a16\n"
"v_accvgpr_read_b32 v179, a17\n"
"v_mul_f32 v178, v178, v173\n"
"v_mul_f32 v179, v179, v173\n"
"v_accvgpr_write_b32 a16, v178\n"
"v_accvgpr_write_b32 a17, v179\n"
"v_accvgpr_read_b32 v178, a18\n"
"v_accvgpr_read_b32 v179, a19\n"
"v_mul_f32 v178, v178, v173\n"
"v_mul_f32 v179, v179, v173\n"
"v_accvgpr_write_b32 a18, v178\n"
"v_accvgpr_write_b32 a19, v179\n"
"v_accvgpr_read_b32 v178, a20\n"
"v_accvgpr_read_b32 v179, a21\n"
"v_mul_f32 v178, v178, v173\n"
"v_mul_f32 v179, v179, v173\n"
"v_accvgpr_write_b32 a20, v178\n"
"v_accvgpr_write_b32 a21, v179\n"
"v_accvgpr_read_b32 v178, a22\n"
"v_accvgpr_read_b32 v179, a23\n"
"v_mul_f32 v178, v178, v173\n"
"v_mul_f32 v179, v179, v173\n"
"v_accvgpr_write_b32 a22, v178\n"
"v_accvgpr_write_b32 a23, v179\n"
"v_accvgpr_read_b32 v178, a24\n"
"v_accvgpr_read_b32 v179, a25\n"
"v_mul_f32 v178, v178, v173\n"
"v_mul_f32 v179, v179, v173\n"
"v_accvgpr_write_b32 a24, v178\n"
"v_accvgpr_write_b32 a25, v179\n"
"v_accvgpr_read_b32 v178, a26\n"
"v_accvgpr_read_b32 v179, a27\n"
"v_mul_f32 v178, v178, v173\n"
"v_mul_f32 v179, v179, v173\n"
"v_accvgpr_write_b32 a26, v178\n"
"v_accvgpr_write_b32 a27, v179\n"
"v_accvgpr_read_b32 v178, a28\n"
"v_accvgpr_read_b32 v179, a29\n"
"v_mul_f32 v178, v178, v173\n"
"v_mul_f32 v179, v179, v173\n"
"v_accvgpr_write_b32 a28, v178\n"
"v_accvgpr_write_b32 a29, v179\n"
"v_accvgpr_read_b32 v178, a30\n"
"v_accvgpr_read_b32 v179, a31\n"
"v_mul_f32 v178, v178, v173\n"
"v_mul_f32 v179, v179, v173\n"
"v_accvgpr_write_b32 a30, v178\n"
"v_accvgpr_write_b32 a31, v179\n"
"v_accvgpr_read_b32 v178, a32\n"
"v_accvgpr_read_b32 v179, a33\n"
"v_mul_f32 v178, v178, v172\n"
"v_mul_f32 v179, v179, v172\n"
"v_accvgpr_write_b32 a32, v178\n"
"v_accvgpr_write_b32 a33, v179\n"
"v_accvgpr_read_b32 v178, a34\n"
"v_accvgpr_read_b32 v179, a35\n"
"v_mul_f32 v178, v178, v172\n"
"v_mul_f32 v179, v179, v172\n"
"v_accvgpr_write_b32 a34, v178\n"
"v_accvgpr_write_b32 a35, v179\n"
"v_accvgpr_read_b32 v178, a36\n"
"v_accvgpr_read_b32 v179, a37\n"
"v_mul_f32 v178, v178, v172\n"
"v_mul_f32 v179, v179, v172\n"
"v_accvgpr_write_b32 a36, v178\n"
"v_accvgpr_write_b32 a37, v179\n"
"v_accvgpr_read_b32 v178, a38\n"
"v_accvgpr_read_b32 v179, a39\n"
"v_mul_f32 v178, v178, v172\n"
"v_mul_f32 v179, v179, v172\n"
"v_accvgpr_write_b32 a38, v178\n"
"v_accvgpr_write_b32 a39, v179\n"
"v_accvgpr_read_b32 v178, a40\n"
"v_accvgpr_read_b32 v179, a41\n"
"v_mul_f32 v178, v178, v172\n"
"v_mul_f32 v179, v179, v172\n"
"v_accvgpr_write_b32 a40, v178\n"
"v_accvgpr_write_b32 a41, v179\n"
"v_accvgpr_read_b32 v178, a42\n"
"v_accvgpr_read_b32 v179, a43\n"
"v_mul_f32 v178, v178, v172\n"
"v_mul_f32 v179, v179, v172\n"
"v_accvgpr_write_b32 a42, v178\n"
"v_accvgpr_write_b32 a43, v179\n"
"v_accvgpr_read_b32 v178, a44\n"
"v_accvgpr_read_b32 v179, a45\n"
"v_mul_f32 v178, v178, v172\n"
"v_mul_f32 v179, v179, v172\n"
"v_accvgpr_write_b32 a44, v178\n"
"v_accvgpr_write_b32 a45, v179\n"
"v_accvgpr_read_b32 v178, a46\n"
"v_accvgpr_read_b32 v179, a47\n"
"v_mul_f32 v178, v178, v172\n"
"v_mul_f32 v179, v179, v172\n"
"v_accvgpr_write_b32 a46, v178\n"
"v_accvgpr_write_b32 a47, v179\n"
"v_accvgpr_read_b32 v178, a48\n"
"v_accvgpr_read_b32 v179, a49\n"
"v_mul_f32 v178, v178, v173\n"
"v_mul_f32 v179, v179, v173\n"
"v_accvgpr_write_b32 a48, v178\n"
"v_accvgpr_write_b32 a49, v179\n"
"v_accvgpr_read_b32 v178, a50\n"
"v_accvgpr_read_b32 v179, a51\n"
"v_mul_f32 v178, v178, v173\n"
"v_mul_f32 v179, v179, v173\n"
"v_accvgpr_write_b32 a50, v178\n"
"v_accvgpr_write_b32 a51, v179\n"
"v_accvgpr_read_b32 v178, a52\n"
"v_accvgpr_read_b32 v179, a53\n"
"v_mul_f32 v178, v178, v173\n"
"v_mul_f32 v179, v179, v173\n"
"v_accvgpr_write_b32 a52, v178\n"
"v_accvgpr_write_b32 a53, v179\n"
"v_accvgpr_read_b32 v178, a54\n"
"v_accvgpr_read_b32 v179, a55\n"
"v_mul_f32 v178, v178, v173\n"
"v_mul_f32 v179, v179, v173\n"
"v_accvgpr_write_b32 a54, v178\n"
"v_accvgpr_write_b32 a55, v179\n"
"v_accvgpr_read_b32 v178, a56\n"
"v_accvgpr_read_b32 v179, a57\n"
"v_mul_f32 v178, v178, v173\n"
"v_mul_f32 v179, v179, v173\n"
"v_accvgpr_write_b32 a56, v178\n"
"v_accvgpr_write_b32 a57, v179\n"
"v_accvgpr_read_b32 v178, a58\n"
"v_accvgpr_read_b32 v179, a59\n"
"v_mul_f32 v178, v178, v173\n"
"v_mul_f32 v179, v179, v173\n"
"v_accvgpr_write_b32 a58, v178\n"
"v_accvgpr_write_b32 a59, v179\n"
"v_accvgpr_read_b32 v178, a60\n"
"v_accvgpr_read_b32 v179, a61\n"
"v_mul_f32 v178, v178, v173\n"
"v_mul_f32 v179, v179, v173\n"
"v_accvgpr_write_b32 a60, v178\n"
"v_accvgpr_write_b32 a61, v179\n"
"v_accvgpr_read_b32 v178, a62\n"
"v_accvgpr_read_b32 v179, a63\n"
"v_mul_f32 v178, v178, v173\n"
"v_mul_f32 v179, v179, v173\n"
"v_accvgpr_write_b32 a62, v178\n"
"v_accvgpr_write_b32 a63, v179\n"
"v_accvgpr_read_b32 v178, a64\n"
"v_accvgpr_read_b32 v179, a65\n"
"v_mul_f32 v178, v178, v172\n"
"v_mul_f32 v179, v179, v172\n"
"v_accvgpr_write_b32 a64, v178\n"
"v_accvgpr_write_b32 a65, v179\n"
"v_accvgpr_read_b32 v178, a66\n"
"v_accvgpr_read_b32 v179, a67\n"
"v_mul_f32 v178, v178, v172\n"
"v_mul_f32 v179, v179, v172\n"
"v_accvgpr_write_b32 a66, v178\n"
"v_accvgpr_write_b32 a67, v179\n"
"v_accvgpr_read_b32 v178, a68\n"
"v_accvgpr_read_b32 v179, a69\n"
"v_mul_f32 v178, v178, v172\n"
"v_mul_f32 v179, v179, v172\n"
"v_accvgpr_write_b32 a68, v178\n"
"v_accvgpr_write_b32 a69, v179\n"
"v_accvgpr_read_b32 v178, a70\n"
"v_accvgpr_read_b32 v179, a71\n"
"v_mul_f32 v178, v178, v172\n"
"v_mul_f32 v179, v179, v172\n"
"v_accvgpr_write_b32 a70, v178\n"
"v_accvgpr_write_b32 a71, v179\n"
"v_accvgpr_read_b32 v178, a72\n"
"v_accvgpr_read_b32 v179, a73\n"
"v_mul_f32 v178, v178, v172\n"
"v_mul_f32 v179, v179, v172\n"
"v_accvgpr_write_b32 a72, v178\n"
"v_accvgpr_write_b32 a73, v179\n"
"v_accvgpr_read_b32 v178, a74\n"
"v_accvgpr_read_b32 v179, a75\n"
"v_mul_f32 v178, v178, v172\n"
"v_mul_f32 v179, v179, v172\n"
"v_accvgpr_write_b32 a74, v178\n"
"v_accvgpr_write_b32 a75, v179\n"
"v_accvgpr_read_b32 v178, a76\n"
"v_accvgpr_read_b32 v179, a77\n"
"v_mul_f32 v178, v178, v172\n"
"v_mul_f32 v179, v179, v172\n"
"v_accvgpr_write_b32 a76, v178\n"
"v_accvgpr_write_b32 a77, v179\n"
"v_accvgpr_read_b32 v178, a78\n"
"v_accvgpr_read_b32 v179, a79\n"
"v_mul_f32 v178, v178, v172\n"
"v_mul_f32 v179, v179, v172\n"
"v_accvgpr_write_b32 a78, v178\n"
"v_accvgpr_write_b32 a79, v179\n"
"v_accvgpr_read_b32 v178, a80\n"
"v_accvgpr_read_b32 v179, a81\n"
"v_mul_f32 v178, v178, v173\n"
"v_mul_f32 v179, v179, v173\n"
"v_accvgpr_write_b32 a80, v178\n"
"v_accvgpr_write_b32 a81, v179\n"
"v_accvgpr_read_b32 v178, a82\n"
"v_accvgpr_read_b32 v179, a83\n"
"v_mul_f32 v178, v178, v173\n"
"v_mul_f32 v179, v179, v173\n"
"v_accvgpr_write_b32 a82, v178\n"
"v_accvgpr_write_b32 a83, v179\n"
"v_accvgpr_read_b32 v178, a84\n"
"v_accvgpr_read_b32 v179, a85\n"
"v_mul_f32 v178, v178, v173\n"
"v_mul_f32 v179, v179, v173\n"
"v_accvgpr_write_b32 a84, v178\n"
"v_accvgpr_write_b32 a85, v179\n"
"v_accvgpr_read_b32 v178, a86\n"
"v_accvgpr_read_b32 v179, a87\n"
"v_mul_f32 v178, v178, v173\n"
"v_mul_f32 v179, v179, v173\n"
"v_accvgpr_write_b32 a86, v178\n"
"v_accvgpr_write_b32 a87, v179\n"
"v_accvgpr_read_b32 v178, a88\n"
"v_accvgpr_read_b32 v179, a89\n"
"v_mul_f32 v178, v178, v173\n"
"v_mul_f32 v179, v179, v173\n"
"v_accvgpr_write_b32 a88, v178\n"
"v_accvgpr_write_b32 a89, v179\n"
"v_accvgpr_read_b32 v178, a90\n"
"v_accvgpr_read_b32 v179, a91\n"
"v_mul_f32 v178, v178, v173\n"
"v_mul_f32 v179, v179, v173\n"
"v_accvgpr_write_b32 a90, v178\n"
"v_accvgpr_write_b32 a91, v179\n"
"v_accvgpr_read_b32 v178, a92\n"
"v_accvgpr_read_b32 v179, a93\n"
"v_mul_f32 v178, v178, v173\n"
"v_mul_f32 v179, v179, v173\n"
"v_accvgpr_write_b32 a92, v178\n"
"v_accvgpr_write_b32 a93, v179\n"
"v_accvgpr_read_b32 v178, a94\n"
"v_accvgpr_read_b32 v179, a95\n"
"v_mul_f32 v178, v178, v173\n"
"v_mul_f32 v179, v179, v173\n"
"v_accvgpr_write_b32 a94, v178\n"
"v_accvgpr_write_b32 a95, v179\n"
"v_accvgpr_read_b32 v178, a96\n"
"v_accvgpr_read_b32 v179, a97\n"
"v_mul_f32 v178, v178, v172\n"
"v_mul_f32 v179, v179, v172\n"
"v_accvgpr_write_b32 a96, v178\n"
"v_accvgpr_write_b32 a97, v179\n"
"v_accvgpr_read_b32 v178, a98\n"
"v_accvgpr_read_b32 v179, a99\n"
"v_mul_f32 v178, v178, v172\n"
"v_mul_f32 v179, v179, v172\n"
"v_accvgpr_write_b32 a98, v178\n"
"v_accvgpr_write_b32 a99, v179\n"
"v_accvgpr_read_b32 v178, a100\n"
"v_accvgpr_read_b32 v179, a101\n"
"v_mul_f32 v178, v178, v172\n"
"v_mul_f32 v179, v179, v172\n"
"v_accvgpr_write_b32 a100, v178\n"
"v_accvgpr_write_b32 a101, v179\n"
"v_accvgpr_read_b32 v178, a102\n"
"v_accvgpr_read_b32 v179, a103\n"
"v_mul_f32 v178, v178, v172\n"
"v_mul_f32 v179, v179, v172\n"
"v_accvgpr_write_b32 a102, v178\n"
"v_accvgpr_write_b32 a103, v179\n"
"v_accvgpr_read_b32 v178, a104\n"
"v_accvgpr_read_b32 v179, a105\n"
"v_mul_f32 v178, v178, v172\n"
"v_mul_f32 v179, v179, v172\n"
"v_accvgpr_write_b32 a104, v178\n"
"v_accvgpr_write_b32 a105, v179\n"
"v_accvgpr_read_b32 v178, a106\n"
"v_accvgpr_read_b32 v179, a107\n"
"v_mul_f32 v178, v178, v172\n"
"v_mul_f32 v179, v179, v172\n"
"v_accvgpr_write_b32 a106, v178\n"
"v_accvgpr_write_b32 a107, v179\n"
"v_accvgpr_read_b32 v178, a108\n"
"v_accvgpr_read_b32 v179, a109\n"
"v_mul_f32 v178, v178, v172\n"
"v_mul_f32 v179, v179, v172\n"
"v_accvgpr_write_b32 a108, v178\n"
"v_accvgpr_write_b32 a109, v179\n"
"v_accvgpr_read_b32 v178, a110\n"
"v_accvgpr_read_b32 v179, a111\n"
"v_mul_f32 v178, v178, v172\n"
"v_mul_f32 v179, v179, v172\n"
"v_accvgpr_write_b32 a110, v178\n"
"v_accvgpr_write_b32 a111, v179\n"
"v_accvgpr_read_b32 v178, a112\n"
"v_accvgpr_read_b32 v179, a113\n"
"v_mul_f32 v178, v178, v173\n"
"v_mul_f32 v179, v179, v173\n"
"v_accvgpr_write_b32 a112, v178\n"
"v_accvgpr_write_b32 a113, v179\n"
"v_accvgpr_read_b32 v178, a114\n"
"v_accvgpr_read_b32 v179, a115\n"
"v_mul_f32 v178, v178, v173\n"
"v_mul_f32 v179, v179, v173\n"
"v_accvgpr_write_b32 a114, v178\n"
"v_accvgpr_write_b32 a115, v179\n"
"v_accvgpr_read_b32 v178, a116\n"
"v_accvgpr_read_b32 v179, a117\n"
"v_mul_f32 v178, v178, v173\n"
"v_mul_f32 v179, v179, v173\n"
"v_accvgpr_write_b32 a116, v178\n"
"v_accvgpr_write_b32 a117, v179\n"
"v_accvgpr_read_b32 v178, a118\n"
"v_accvgpr_read_b32 v179, a119\n"
"v_mul_f32 v178, v178, v173\n"
"v_mul_f32 v179, v179, v173\n"
"v_accvgpr_write_b32 a118, v178\n"
"v_accvgpr_write_b32 a119, v179\n"
"v_accvgpr_read_b32 v178, a120\n"
"v_accvgpr_read_b32 v179, a121\n"
"v_mul_f32 v178, v178, v173\n"
"v_mul_f32 v179, v179, v173\n"
"v_accvgpr_write_b32 a120, v178\n"
"v_accvgpr_write_b32 a121, v179\n"
"v_accvgpr_read_b32 v178, a122\n"
"v_accvgpr_read_b32 v179, a123\n"
"v_mul_f32 v178, v178, v173\n"
"v_mul_f32 v179, v179, v173\n"
"v_accvgpr_write_b32 a122, v178\n"
"v_accvgpr_write_b32 a123, v179\n"
"v_accvgpr_read_b32 v178, a124\n"
"v_accvgpr_read_b32 v179, a125\n"
"v_mul_f32 v178, v178, v173\n"
"v_mul_f32 v179, v179, v173\n"
"v_accvgpr_write_b32 a124, v178\n"
"v_accvgpr_write_b32 a125, v179\n"
"v_accvgpr_read_b32 v178, a126\n"
"v_accvgpr_read_b32 v179, a127\n"
"v_mul_f32 v178, v178, v173\n"
"v_mul_f32 v179, v179, v173\n"
"v_accvgpr_write_b32 a126, v178\n"
"v_accvgpr_write_b32 a127, v179\n"
"v_mul_f32 v162, v162, v172\n"
"v_mul_f32 v163, v163, v173\n"
"s_mov_b32 s42, 0\n"
"s_nop 1\n"
"s_branch L_att_rescd0\n"
"L_att_mask1:\n"
"v_mov_b32 v178, 0xff800000\n"
"v_mov_b32 v179, 0xff800000\n"
"v_cmp_le_i32 s[46:47], 0, v177\n"
"v_cmp_le_i32 s[48:49], 1, v177\n"
"v_cmp_le_i32 s[50:51], 2, v177\n"
"v_cmp_le_i32 s[52:53], 3, v177\n"
"v_cmp_le_i32 s[54:55], 8, v177\n"
"v_cmp_le_i32 s[56:57], 9, v177\n"
"v_cmp_le_i32 s[58:59], 10, v177\n"
"v_cmp_le_i32 s[60:61], 11, v177\n"
"v_mov_b64 v[96:97], v[178:179]\n"
"v_mov_b64 v[98:99], v[178:179]\n"
"v_mov_b64 v[100:101], v[178:179]\n"
"v_mov_b64 v[102:103], v[178:179]\n"
"v_mov_b64 v[104:105], v[178:179]\n"
"v_mov_b64 v[106:107], v[178:179]\n"
"v_mov_b64 v[108:109], v[178:179]\n"
"v_mov_b64 v[110:111], v[178:179]\n"
"v_cndmask_b32 v64, v179, v64, s[46:47]\n"
"v_cndmask_b32 v112, v179, v112, s[46:47]\n"
"v_cmp_le_i32 s[46:47], 16, v177\n"
"v_cndmask_b32 v65, v179, v65, s[48:49]\n"
"v_cndmask_b32 v113, v179, v113, s[48:49]\n"
"v_cmp_le_i32 s[48:49], 17, v177\n"
"v_cndmask_b32 v66, v179, v66, s[50:51]\n"
"v_cndmask_b32 v114, v179, v114, s[50:51]\n"
"v_cmp_le_i32 s[50:51], 18, v177\n"
"v_cndmask_b32 v67, v179, v67, s[52:53]\n"
"v_cndmask_b32 v115, v179, v115, s[52:53]\n"
"v_cmp_le_i32 s[52:53], 19, v177\n"
"v_cndmask_b32 v68, v179, v68, s[54:55]\n"
"v_cndmask_b32 v116, v179, v116, s[54:55]\n"
"v_cmp_le_i32 s[54:55], 24, v177\n"
"v_cndmask_b32 v69, v179, v69, s[56:57]\n"
"v_cndmask_b32 v117, v179, v117, s[56:57]\n"
"v_cmp_le_i32 s[56:57], 25, v177\n"
"v_cndmask_b32 v70, v179, v70, s[58:59]\n"
"v_cndmask_b32 v118, v179, v118, s[58:59]\n"
"v_cmp_le_i32 s[58:59], 26, v177\n"
"v_cndmask_b32 v71, v179, v71, s[60:61]\n"
"v_cndmask_b32 v119, v179, v119, s[60:61]\n"
"v_cmp_le_i32 s[60:61], 27, v177\n"
"v_cndmask_b32 v72, v179, v72, s[46:47]\n"
"v_cndmask_b32 v120, v179, v120, s[46:47]\n"
"v_cndmask_b32 v73, v179, v73, s[48:49]\n"
"v_cndmask_b32 v121, v179, v121, s[48:49]\n"
"v_cndmask_b32 v74, v179, v74, s[50:51]\n"
"v_cndmask_b32 v122, v179, v122, s[50:51]\n"
"v_cndmask_b32 v75, v179, v75, s[52:53]\n"
"v_cndmask_b32 v123, v179, v123, s[52:53]\n"
"v_cndmask_b32 v76, v179, v76, s[54:55]\n"
"v_cndmask_b32 v124, v179, v124, s[54:55]\n"
"v_cndmask_b32 v77, v179, v77, s[56:57]\n"
"v_cndmask_b32 v125, v179, v125, s[56:57]\n"
"v_cndmask_b32 v78, v179, v78, s[58:59]\n"
"v_cndmask_b32 v126, v179, v126, s[58:59]\n"
"v_cndmask_b32 v79, v179, v79, s[60:61]\n"
"v_cndmask_b32 v127, v179, v127, s[60:61]\n"
"s_branch L_att_masked1\n"
"L_att_rare1:\n"
"v_max_f32 v168, s44, v168\n"
"v_add_f32 v160, v160, v168\n"
"v_exp_f32 v172, -v168\n"
"v_sub_f32 v64, v64, v168\n"
"v_sub_f32 v65, v65, v168\n"
"v_sub_f32 v66, v66, v168\n"
"v_sub_f32 v67, v67, v168\n"
"v_sub_f32 v68, v68, v168\n"
"v_sub_f32 v69, v69, v168\n"
"v_sub_f32 v70, v70, v168\n"
"v_sub_f32 v71, v71, v168\n"
"v_sub_f32 v72, v72, v168\n"
"v_sub_f32 v73, v73, v168\n"
"v_sub_f32 v74, v74, v168\n"
"v_sub_f32 v75, v75, v168\n"
"v_sub_f32 v76, v76, v168\n"
"v_sub_f32 v77, v77, v168\n"
"v_sub_f32 v78, v78, v168\n"
"v_sub_f32 v79, v79, v168\n"
"v_sub_f32 v96, v96, v168\n"
"v_sub_f32 v97, v97, v168\n"
"v_sub_f32 v98, v98, v168\n"
"v_sub_f32 v99, v99, v168\n"
"v_sub_f32 v100, v100, v168\n"
"v_sub_f32 v101, v101, v168\n"
"v_sub_f32 v102, v102, v168\n"
"v_sub_f32 v103, v103, v168\n"
"v_sub_f32 v104, v104, v168\n"
"v_sub_f32 v105, v105, v168\n"
"v_sub_f32 v106, v106, v168\n"
"v_sub_f32 v107, v107, v168\n"
"v_sub_f32 v108, v108, v168\n"
"v_sub_f32 v109, v109, v168\n"
"v_sub_f32 v110, v110, v168\n"
"v_sub_f32 v111, v111, v168\n"
"v_sub_f32 v128, 0, v160\n"
"v_sub_f32 v129, 0, v160\n"
"v_sub_f32 v130, 0, v160\n"
"v_sub_f32 v131, 0, v160\n"
"v_sub_f32 v132, 0, v160\n"
"v_sub_f32 v133, 0, v160\n"
"v_sub_f32 v134, 0, v160\n"
"v_sub_f32 v135, 0, v160\n"
"v_sub_f32 v136, 0, v160\n"
"v_sub_f32 v137, 0, v160\n"
"v_sub_f32 v138, 0, v160\n"
"v_sub_f32 v139, 0, v160\n"
"v_sub_f32 v140, 0, v160\n"
"v_sub_f32 v141, 0, v160\n"
"v_sub_f32 v142, 0, v160\n"
"v_sub_f32 v143, 0, v160\n"
"v_max_f32 v170, s44, v170\n"
"v_add_f32 v161, v161, v170\n"
"v_exp_f32 v173, -v170\n"
"v_sub_f32 v80, v80, v170\n"
"v_sub_f32 v81, v81, v170\n"
"v_sub_f32 v82, v82, v170\n"
"v_sub_f32 v83, v83, v170\n"
"v_sub_f32 v84, v84, v170\n"
"v_sub_f32 v85, v85, v170\n"
"v_sub_f32 v86, v86, v170\n"
"v_sub_f32 v87, v87, v170\n"
"v_sub_f32 v88, v88, v170\n"
"v_sub_f32 v89, v89, v170\n"
"v_sub_f32 v90, v90, v170\n"
"v_sub_f32 v91, v91, v170\n"
"v_sub_f32 v92, v92, v170\n"
"v_sub_f32 v93, v93, v170\n"
"v_sub_f32 v94, v94, v170\n"
"v_sub_f32 v95, v95, v170\n"
"v_sub_f32 v112, v112, v170\n"
"v_sub_f32 v113, v113, v170\n"
"v_sub_f32 v114, v114, v170\n"
"v_sub_f32 v115, v115, v170\n"
"v_sub_f32 v116, v116, v170\n"
"v_sub_f32 v117, v117, v170\n"
"v_sub_f32 v118, v118, v170\n"
"v_sub_f32 v119, v119, v170\n"
"v_sub_f32 v120, v120, v170\n"
"v_sub_f32 v121, v121, v170\n"
"v_sub_f32 v122, v122, v170\n"
"v_sub_f32 v123, v123, v170\n"
"v_sub_f32 v124, v124, v170\n"
"v_sub_f32 v125, v125, v170\n"
"v_sub_f32 v126, v126, v170\n"
"v_sub_f32 v127, v127, v170\n"
"v_sub_f32 v144, 0, v161\n"
"v_sub_f32 v145, 0, v161\n"
"v_sub_f32 v146, 0, v161\n"
"v_sub_f32 v147, 0, v161\n"
"v_sub_f32 v148, 0, v161\n"
"v_sub_f32 v149, 0, v161\n"
"v_sub_f32 v150, 0, v161\n"
"v_sub_f32 v151, 0, v161\n"
"v_sub_f32 v152, 0, v161\n"
"v_sub_f32 v153, 0, v161\n"
"v_sub_f32 v154, 0, v161\n"
"v_sub_f32 v155, 0, v161\n"
"v_sub_f32 v156, 0, v161\n"
"v_sub_f32 v157, 0, v161\n"
"v_sub_f32 v158, 0, v161\n"
"v_sub_f32 v159, 0, v161\n"
"s_cmp_eq_u32 s44, 0\n"
"s_cselect_b32 s42, 1, 0\n"
"s_mov_b32 s44, 0\n"
"s_mov_b32 s45, 0x41c00000\n"
"s_branch L_att_rared1\n"
"L_att_resc1:\n"
"s_nop 7\n"
"s_nop 3\n"
"v_accvgpr_read_b32 v178, a0\n"
"v_accvgpr_read_b32 v179, a1\n"
"v_mul_f32 v178, v178, v172\n"
"v_mul_f32 v179, v179, v172\n"
"v_accvgpr_write_b32 a0, v178\n"
"v_accvgpr_write_b32 a1, v179\n"
"v_accvgpr_read_b32 v178, a2\n"
"v_accvgpr_read_b32 v179, a3\n"
"v_mul_f32 v178, v178, v172\n"
"v_mul_f32 v179, v179, v172\n"
"v_accvgpr_write_b32 a2, v178\n"
"v_accvgpr_write_b32 a3, v179\n"
"v_accvgpr_read_b32 v178, a4\n"
"v_accvgpr_read_b32 v179, a5\n"
"v_mul_f32 v178, v178, v172\n"
"v_mul_f32 v179, v179, v172\n"
"v_accvgpr_write_b32 a4, v178\n"
"v_accvgpr_write_b32 a5, v179\n"
"v_accvgpr_read_b32 v178, a6\n"
"v_accvgpr_read_b32 v179, a7\n"
"v_mul_f32 v178, v178, v172\n"
"v_mul_f32 v179, v179, v172\n"
"v_accvgpr_write_b32 a6, v178\n"
"v_accvgpr_write_b32 a7, v179\n"
"v_accvgpr_read_b32 v178, a8\n"
"v_accvgpr_read_b32 v179, a9\n"
"v_mul_f32 v178, v178, v172\n"
"v_mul_f32 v179, v179, v172\n"
"v_accvgpr_write_b32 a8, v178\n"
"v_accvgpr_write_b32 a9, v179\n"
"v_accvgpr_read_b32 v178, a10\n"
"v_accvgpr_read_b32 v179, a11\n"
"v_mul_f32 v178, v178, v172\n"
"v_mul_f32 v179, v179, v172\n"
"v_accvgpr_write_b32 a10, v178\n"
"v_accvgpr_write_b32 a11, v179\n"
"v_accvgpr_read_b32 v178, a12\n"
"v_accvgpr_read_b32 v179, a13\n"
"v_mul_f32 v178, v178, v172\n"
"v_mul_f32 v179, v179, v172\n"
"v_accvgpr_write_b32 a12, v178\n"
"v_accvgpr_write_b32 a13, v179\n"
"v_accvgpr_read_b32 v178, a14\n"
"v_accvgpr_read_b32 v179, a15\n"
"v_mul_f32 v178, v178, v172\n"
"v_mul_f32 v179, v179, v172\n"
"v_accvgpr_write_b32 a14, v178\n"
"v_accvgpr_write_b32 a15, v179\n"
"v_accvgpr_read_b32 v178, a16\n"
"v_accvgpr_read_b32 v179, a17\n"
"v_mul_f32 v178, v178, v173\n"
"v_mul_f32 v179, v179, v173\n"
"v_accvgpr_write_b32 a16, v178\n"
"v_accvgpr_write_b32 a17, v179\n"
"v_accvgpr_read_b32 v178, a18\n"
"v_accvgpr_read_b32 v179, a19\n"
"v_mul_f32 v178, v178, v173\n"
"v_mul_f32 v179, v179, v173\n"
"v_accvgpr_write_b32 a18, v178\n"
"v_accvgpr_write_b32 a19, v179\n"
"v_accvgpr_read_b32 v178, a20\n"
"v_accvgpr_read_b32 v179, a21\n"
"v_mul_f32 v178, v178, v173\n"
"v_mul_f32 v179, v179, v173\n"
"v_accvgpr_write_b32 a20, v178\n"
"v_accvgpr_write_b32 a21, v179\n"
"v_accvgpr_read_b32 v178, a22\n"
"v_accvgpr_read_b32 v179, a23\n"
"v_mul_f32 v178, v178, v173\n"
"v_mul_f32 v179, v179, v173\n"
"v_accvgpr_write_b32 a22, v178\n"
"v_accvgpr_write_b32 a23, v179\n"
"v_accvgpr_read_b32 v178, a24\n"
"v_accvgpr_read_b32 v179, a25\n"
"v_mul_f32 v178, v178, v173\n"
"v_mul_f32 v179, v179, v173\n"
"v_accvgpr_write_b32 a24, v178\n"
"v_accvgpr_write_b32 a25, v179\n"
"v_accvgpr_read_b32 v178, a26\n"
"v_accvgpr_read_b32 v179, a27\n"
"v_mul_f32 v178, v178, v173\n"
"v_mul_f32 v179, v179, v173\n"
"v_accvgpr_write_b32 a26, v178\n"
"v_accvgpr_write_b32 a27, v179\n"
"v_accvgpr_read_b32 v178, a28\n"
"v_accvgpr_read_b32 v179, a29\n"
"v_mul_f32 v178, v178, v173\n"
"v_mul_f32 v179, v179, v173\n"
"v_accvgpr_write_b32 a28, v178\n"
"v_accvgpr_write_b32 a29, v179\n"
"v_accvgpr_read_b32 v178, a30\n"
"v_accvgpr_read_b32 v179, a31\n"
"v_mul_f32 v178, v178, v173\n"
"v_mul_f32 v179, v179, v173\n"
"v_accvgpr_write_b32 a30, v178\n"
"v_accvgpr_write_b32 a31, v179\n"
"v_accvgpr_read_b32 v178, a32\n"
"v_accvgpr_read_b32 v179, a33\n"
"v_mul_f32 v178, v178, v172\n"
"v_mul_f32 v179, v179, v172\n"
"v_accvgpr_write_b32 a32, v178\n"
"v_accvgpr_write_b32 a33, v179\n"
"v_accvgpr_read_b32 v178, a34\n"
"v_accvgpr_read_b32 v179, a35\n"
"v_mul_f32 v178, v178, v172\n"
"v_mul_f32 v179, v179, v172\n"
"v_accvgpr_write_b32 a34, v178\n"
"v_accvgpr_write_b32 a35, v179\n"
"v_accvgpr_read_b32 v178, a36\n"
"v_accvgpr_read_b32 v179, a37\n"
"v_mul_f32 v178, v178, v172\n"
"v_mul_f32 v179, v179, v172\n"
"v_accvgpr_write_b32 a36, v178\n"
"v_accvgpr_write_b32 a37, v179\n"
"v_accvgpr_read_b32 v178, a38\n"
"v_accvgpr_read_b32 v179, a39\n"
"v_mul_f32 v178, v178, v172\n"
"v_mul_f32 v179, v179, v172\n"
"v_accvgpr_write_b32 a38, v178\n"
"v_accvgpr_write_b32 a39, v179\n"
"v_accvgpr_read_b32 v178, a40\n"
"v_accvgpr_read_b32 v179, a41\n"
"v_mul_f32 v178, v178, v172\n"
"v_mul_f32 v179, v179, v172\n"
"v_accvgpr_write_b32 a40, v178\n"
"v_accvgpr_write_b32 a41, v179\n"
"v_accvgpr_read_b32 v178, a42\n"
"v_accvgpr_read_b32 v179, a43\n"
"v_mul_f32 v178, v178, v172\n"
"v_mul_f32 v179, v179, v172\n"
"v_accvgpr_write_b32 a42, v178\n"
"v_accvgpr_write_b32 a43, v179\n"
"v_accvgpr_read_b32 v178, a44\n"
"v_accvgpr_read_b32 v179, a45\n"
"v_mul_f32 v178, v178, v172\n"
"v_mul_f32 v179, v179, v172\n"
"v_accvgpr_write_b32 a44, v178\n"
"v_accvgpr_write_b32 a45, v179\n"
"v_accvgpr_read_b32 v178, a46\n"
"v_accvgpr_read_b32 v179, a47\n"
"v_mul_f32 v178, v178, v172\n"
"v_mul_f32 v179, v179, v172\n"
"v_accvgpr_write_b32 a46, v178\n"
"v_accvgpr_write_b32 a47, v179\n"
"v_accvgpr_read_b32 v178, a48\n"
"v_accvgpr_read_b32 v179, a49\n"
"v_mul_f32 v178, v178, v173\n"
"v_mul_f32 v179, v179, v173\n"
"v_accvgpr_write_b32 a48, v178\n"
"v_accvgpr_write_b32 a49, v179\n"
"v_accvgpr_read_b32 v178, a50\n"
"v_accvgpr_read_b32 v179, a51\n"
"v_mul_f32 v178, v178, v173\n"
"v_mul_f32 v179, v179, v173\n"
"v_accvgpr_write_b32 a50, v178\n"
"v_accvgpr_write_b32 a51, v179\n"
"v_accvgpr_read_b32 v178, a52\n"
"v_accvgpr_read_b32 v179, a53\n"
"v_mul_f32 v178, v178, v173\n"
"v_mul_f32 v179, v179, v173\n"
"v_accvgpr_write_b32 a52, v178\n"
"v_accvgpr_write_b32 a53, v179\n"
"v_accvgpr_read_b32 v178, a54\n"
"v_accvgpr_read_b32 v179, a55\n"
"v_mul_f32 v178, v178, v173\n"
"v_mul_f32 v179, v179, v173\n"
"v_accvgpr_write_b32 a54, v178\n"
"v_accvgpr_write_b32 a55, v179\n"
"v_accvgpr_read_b32 v178, a56\n"
"v_accvgpr_read_b32 v179, a57\n"
"v_mul_f32 v178, v178, v173\n"
"v_mul_f32 v179, v179, v173\n"
"v_accvgpr_write_b32 a56, v178\n"
"v_accvgpr_write_b32 a57, v179\n"
"v_accvgpr_read_b32 v178, a58\n"
"v_accvgpr_read_b32 v179, a59\n"
"v_mul_f32 v178, v178, v173\n"
"v_mul_f32 v179, v179, v173\n"
"v_accvgpr_write_b32 a58, v178\n"
"v_accvgpr_write_b32 a59, v179\n"
"v_accvgpr_read_b32 v178, a60\n"
"v_accvgpr_read_b32 v179, a61\n"
"v_mul_f32 v178, v178, v173\n"
"v_mul_f32 v179, v179, v173\n"
"v_accvgpr_write_b32 a60, v178\n"
"v_accvgpr_write_b32 a61, v179\n"
"v_accvgpr_read_b32 v178, a62\n"
"v_accvgpr_read_b32 v179, a63\n"
"v_mul_f32 v178, v178, v173\n"
"v_mul_f32 v179, v179, v173\n"
"v_accvgpr_write_b32 a62, v178\n"
"v_accvgpr_write_b32 a63, v179\n"
"v_accvgpr_read_b32 v178, a64\n"
"v_accvgpr_read_b32 v179, a65\n"
"v_mul_f32 v178, v178, v172\n"
"v_mul_f32 v179, v179, v172\n"
"v_accvgpr_write_b32 a64, v178\n"
"v_accvgpr_write_b32 a65, v179\n"
"v_accvgpr_read_b32 v178, a66\n"
"v_accvgpr_read_b32 v179, a67\n"
"v_mul_f32 v178, v178, v172\n"
"v_mul_f32 v179, v179, v172\n"
"v_accvgpr_write_b32 a66, v178\n"
"v_accvgpr_write_b32 a67, v179\n"
"v_accvgpr_read_b32 v178, a68\n"
"v_accvgpr_read_b32 v179, a69\n"
"v_mul_f32 v178, v178, v172\n"
"v_mul_f32 v179, v179, v172\n"
"v_accvgpr_write_b32 a68, v178\n"
"v_accvgpr_write_b32 a69, v179\n"
"v_accvgpr_read_b32 v178, a70\n"
"v_accvgpr_read_b32 v179, a71\n"
"v_mul_f32 v178, v178, v172\n"
"v_mul_f32 v179, v179, v172\n"
"v_accvgpr_write_b32 a70, v178\n"
"v_accvgpr_write_b32 a71, v179\n"
"v_accvgpr_read_b32 v178, a72\n"
"v_accvgpr_read_b32 v179, a73\n"
"v_mul_f32 v178, v178, v172\n"
"v_mul_f32 v179, v179, v172\n"
"v_accvgpr_write_b32 a72, v178\n"
"v_accvgpr_write_b32 a73, v179\n"
"v_accvgpr_read_b32 v178, a74\n"
"v_accvgpr_read_b32 v179, a75\n"
"v_mul_f32 v178, v178, v172\n"
"v_mul_f32 v179, v179, v172\n"
"v_accvgpr_write_b32 a74, v178\n"
"v_accvgpr_write_b32 a75, v179\n"
"v_accvgpr_read_b32 v178, a76\n"
"v_accvgpr_read_b32 v179, a77\n"
"v_mul_f32 v178, v178, v172\n"
"v_mul_f32 v179, v179, v172\n"
"v_accvgpr_write_b32 a76, v178\n"
"v_accvgpr_write_b32 a77, v179\n"
"v_accvgpr_read_b32 v178, a78\n"
"v_accvgpr_read_b32 v179, a79\n"
"v_mul_f32 v178, v178, v172\n"
"v_mul_f32 v179, v179, v172\n"
"v_accvgpr_write_b32 a78, v178\n"
"v_accvgpr_write_b32 a79, v179\n"
"v_accvgpr_read_b32 v178, a80\n"
"v_accvgpr_read_b32 v179, a81\n"
"v_mul_f32 v178, v178, v173\n"
"v_mul_f32 v179, v179, v173\n"
"v_accvgpr_write_b32 a80, v178\n"
"v_accvgpr_write_b32 a81, v179\n"
"v_accvgpr_read_b32 v178, a82\n"
"v_accvgpr_read_b32 v179, a83\n"
"v_mul_f32 v178, v178, v173\n"
"v_mul_f32 v179, v179, v173\n"
"v_accvgpr_write_b32 a82, v178\n"
"v_accvgpr_write_b32 a83, v179\n"
"v_accvgpr_read_b32 v178, a84\n"
"v_accvgpr_read_b32 v179, a85\n"
"v_mul_f32 v178, v178, v173\n"
"v_mul_f32 v179, v179, v173\n"
"v_accvgpr_write_b32 a84, v178\n"
"v_accvgpr_write_b32 a85, v179\n"
"v_accvgpr_read_b32 v178, a86\n"
"v_accvgpr_read_b32 v179, a87\n"
"v_mul_f32 v178, v178, v173\n"
"v_mul_f32 v179, v179, v173\n"
"v_accvgpr_write_b32 a86, v178\n"
"v_accvgpr_write_b32 a87, v179\n"
"v_accvgpr_read_b32 v178, a88\n"
"v_accvgpr_read_b32 v179, a89\n"
"v_mul_f32 v178, v178, v173\n"
"v_mul_f32 v179, v179, v173\n"
"v_accvgpr_write_b32 a88, v178\n"
"v_accvgpr_write_b32 a89, v179\n"
"v_accvgpr_read_b32 v178, a90\n"
"v_accvgpr_read_b32 v179, a91\n"
"v_mul_f32 v178, v178, v173\n"
"v_mul_f32 v179, v179, v173\n"
"v_accvgpr_write_b32 a90, v178\n"
"v_accvgpr_write_b32 a91, v179\n"
"v_accvgpr_read_b32 v178, a92\n"
"v_accvgpr_read_b32 v179, a93\n"
"v_mul_f32 v178, v178, v173\n"
"v_mul_f32 v179, v179, v173\n"
"v_accvgpr_write_b32 a92, v178\n"
"v_accvgpr_write_b32 a93, v179\n"
"v_accvgpr_read_b32 v178, a94\n"
"v_accvgpr_read_b32 v179, a95\n"
"v_mul_f32 v178, v178, v173\n"
"v_mul_f32 v179, v179, v173\n"
"v_accvgpr_write_b32 a94, v178\n"
"v_accvgpr_write_b32 a95, v179\n"
"v_accvgpr_read_b32 v178, a96\n"
"v_accvgpr_read_b32 v179, a97\n"
"v_mul_f32 v178, v178, v172\n"
"v_mul_f32 v179, v179, v172\n"
"v_accvgpr_write_b32 a96, v178\n"
"v_accvgpr_write_b32 a97, v179\n"
"v_accvgpr_read_b32 v178, a98\n"
"v_accvgpr_read_b32 v179, a99\n"
"v_mul_f32 v178, v178, v172\n"
"v_mul_f32 v179, v179, v172\n"
"v_accvgpr_write_b32 a98, v178\n"
"v_accvgpr_write_b32 a99, v179\n"
"v_accvgpr_read_b32 v178, a100\n"
"v_accvgpr_read_b32 v179, a101\n"
"v_mul_f32 v178, v178, v172\n"
"v_mul_f32 v179, v179, v172\n"
"v_accvgpr_write_b32 a100, v178\n"
"v_accvgpr_write_b32 a101, v179\n"
"v_accvgpr_read_b32 v178, a102\n"
"v_accvgpr_read_b32 v179, a103\n"
"v_mul_f32 v178, v178, v172\n"
"v_mul_f32 v179, v179, v172\n"
"v_accvgpr_write_b32 a102, v178\n"
"v_accvgpr_write_b32 a103, v179\n"
"v_accvgpr_read_b32 v178, a104\n"
"v_accvgpr_read_b32 v179, a105\n"
"v_mul_f32 v178, v178, v172\n"
"v_mul_f32 v179, v179, v172\n"
"v_accvgpr_write_b32 a104, v178\n"
"v_accvgpr_write_b32 a105, v179\n"
"v_accvgpr_read_b32 v178, a106\n"
"v_accvgpr_read_b32 v179, a107\n"
"v_mul_f32 v178, v178, v172\n"
"v_mul_f32 v179, v179, v172\n"
"v_accvgpr_write_b32 a106, v178\n"
"v_accvgpr_write_b32 a107, v179\n"
"v_accvgpr_read_b32 v178, a108\n"
"v_accvgpr_read_b32 v179, a109\n"
"v_mul_f32 v178, v178, v172\n"
"v_mul_f32 v179, v179, v172\n"
"v_accvgpr_write_b32 a108, v178\n"
"v_accvgpr_write_b32 a109, v179\n"
"v_accvgpr_read_b32 v178, a110\n"
"v_accvgpr_read_b32 v179, a111\n"
"v_mul_f32 v178, v178, v172\n"
"v_mul_f32 v179, v179, v172\n"
"v_accvgpr_write_b32 a110, v178\n"
"v_accvgpr_write_b32 a111, v179\n"
"v_accvgpr_read_b32 v178, a112\n"
"v_accvgpr_read_b32 v179, a113\n"
"v_mul_f32 v178, v178, v173\n"
"v_mul_f32 v179, v179, v173\n"
"v_accvgpr_write_b32 a112, v178\n"
"v_accvgpr_write_b32 a113, v179\n"
"v_accvgpr_read_b32 v178, a114\n"
"v_accvgpr_read_b32 v179, a115\n"
"v_mul_f32 v178, v178, v173\n"
"v_mul_f32 v179, v179, v173\n"
"v_accvgpr_write_b32 a114, v178\n"
"v_accvgpr_write_b32 a115, v179\n"
"v_accvgpr_read_b32 v178, a116\n"
"v_accvgpr_read_b32 v179, a117\n"
"v_mul_f32 v178, v178, v173\n"
"v_mul_f32 v179, v179, v173\n"
"v_accvgpr_write_b32 a116, v178\n"
"v_accvgpr_write_b32 a117, v179\n"
"v_accvgpr_read_b32 v178, a118\n"
"v_accvgpr_read_b32 v179, a119\n"
"v_mul_f32 v178, v178, v173\n"
"v_mul_f32 v179, v179, v173\n"
"v_accvgpr_write_b32 a118, v178\n"
"v_accvgpr_write_b32 a119, v179\n"
"v_accvgpr_read_b32 v178, a120\n"
"v_accvgpr_read_b32 v179, a121\n"
"v_mul_f32 v178, v178, v173\n"
"v_mul_f32 v179, v179, v173\n"
"v_accvgpr_write_b32 a120, v178\n"
"v_accvgpr_write_b32 a121, v179\n"
"v_accvgpr_read_b32 v178, a122\n"
"v_accvgpr_read_b32 v179, a123\n"
"v_mul_f32 v178, v178, v173\n"
"v_mul_f32 v179, v179, v173\n"
"v_accvgpr_write_b32 a122, v178\n"
"v_accvgpr_write_b32 a123, v179\n"
"v_accvgpr_read_b32 v178, a124\n"
"v_accvgpr_read_b32 v179, a125\n"
"v_mul_f32 v178, v178, v173\n"
"v_mul_f32 v179, v179, v173\n"
"v_accvgpr_write_b32 a124, v178\n"
"v_accvgpr_write_b32 a125, v179\n"
"v_accvgpr_read_b32 v178, a126\n"
"v_accvgpr_read_b32 v179, a127\n"
"v_mul_f32 v178, v178, v173\n"
"v_mul_f32 v179, v179, v173\n"
"v_accvgpr_write_b32 a126, v178\n"
"v_accvgpr_write_b32 a127, v179\n"
"v_mul_f32 v162, v162, v172\n"
"v_mul_f32 v163, v163, v173\n"
"s_mov_b32 s42, 0\n"
"s_nop 1\n"
"s_branch L_att_rescd1\n"
"L_oe:\n"
"s_load_dwordx2 s[52:53], s[0:1], s65\n"
"s_load_dwordx2 s[54:55], s[0:1], 96\n"
"s_and_b32 s46, s67, 0xff\n"
"s_bfe_u32 s39, s67, 0x80008\n"
"s_lshr_b32 s40, s67, 16\n"
"v_mov_b32 v0, 0x0\n"
"v_bfe_i32 v4, v255, 0, 1\n"
"v_and_b32 v4, 0x10, v4\n"
"v_add_u32 v0, v0, v4\n"
"v_bfe_i32 v4, v255, 1, 1\n"
"v_and_b32 v4, 0x20, v4\n"
"v_add_u32 v0, v0, v4\n"
"v_bfe_i32 v4, v255, 2, 1\n"
"v_and_b32 v4, 0x400, v4\n"
"v_add_u32 v0, v0, v4\n"
"v_bfe_i32 v4, v255, 3, 1\n"
"v_and_b32 v4, 0x800, v4\n"
"v_add_u32 v0, v0, v4\n"
"v_bfe_i32 v4, v255, 4, 1\n"
"v_and_b32 v4, 0x1000, v4\n"
"v_add_u32 v0, v0, v4\n"
"v_bfe_i32 v4, v255, 5, 1\n"
"v_and_b32 v4, 0x2000, v4\n"
"v_add_u32 v0, v0, v4\n"
"v_mov_b32 v1, 0x0\n"
"v_bfe_i32 v4, v255, 0, 1\n"
"v_and_b32 v4, 0x8, v4\n"
"v_add_u32 v1, v1, v4\n"
"v_bfe_i32 v4, v255, 1, 1\n"
"v_and_b32 v4, 0x10, v4\n"
"v_add_u32 v1, v1, v4\n"
"v_bfe_i32 v4, v255, 2, 1\n"
"v_and_b32 v4, 0x40, v4\n"
"v_add_u32 v1, v1, v4\n"
"v_bfe_i32 v4, v255, 3, 1\n"
"v_and_b32 v4, 0x80, v4\n"
"v_add_u32 v1, v1, v4\n"
"v_bfe_i32 v4, v255, 4, 1\n"
"v_and_b32 v4, 0x20, v4\n"
"v_add_u32 v1, v1, v4\n"
"v_bfe_i32 v4, v255, 5, 1\n"
"v_and_b32 v4, 0x100, v4\n"
"v_add_u32 v1, v1, v4\n"
"s_mul_i32 s3, s5, 0xa000\n"
"v_add_u32 v1, s3, v1\n"
"s_mul_i32 s56, s5, 40960\n"
"s_lshl_b32 s57, s46, 8\n"
"s_lshl_b32 s3, s5, 6\n"
"s_add_u32 s57, s57, s3\n"
"s_lshl_b32 s58, s39, 20\n"
"s_add_u32 s58, s58, s57\n"
"s_waitcnt lgkmcnt(0)\n"
"s_add_u32 s16, s52, 0x0\n"
"s_addc_u32 s17, s53, 0\n"
"s_add_u32 s16, s16, s58\n"
"s_addc_u32 s17, s17, 0\n"
"s_and_b32 s17, s17, 0xffff\n"
"s_mov_b32 s18, 0x100000\n"
"s_mov_b32 s19, 0x20000\n"
"s_sub_u32 s18, s18, s57\n"
"s_barrier\n"
"s_add_u32 m0, s56, 0\n"
"s_mov_b32 s3, 0\n"
"buffer_load_dwordx4 v0, s[16:19], s3 offen lds\n"
"s_add_u32 m0, s56, 1024\n"
"s_mov_b32 s3, 16384\n"
"buffer_load_dwordx4 v0, s[16:19], s3 offen lds\n"
"s_add_u32 m0, s56, 2048\n"
"s_mov_b32 s3, 32768\n"
"buffer_load_dwordx4 v0, s[16:19], s3 offen lds\n"
"s_add_u32 m0, s56, 3072\n"
"s_mov_b32 s3, 49152\n"
"buffer_load_dwordx4 v0, s[16:19], s3 offen lds\n"
"s_add_u32 m0, s56, 4096\n"
"s_mov_b32 s3, 65536\n"
"buffer_load_dwordx4 v0, s[16:19], s3 offen lds\n"
"s_add_u32 m0, s56, 5120\n"
"s_mov_b32 s3, 81920\n"
"buffer_load_dwordx4 v0, s[16:19], s3 offen lds\n"
"s_add_u32 m0, s56, 6144\n"
"s_mov_b32 s3, 98304\n"
"buffer_load_dwordx4 v0, s[16:19], s3 offen lds\n"
"s_add_u32 m0, s56, 7168\n"
"s_mov_b32 s3, 114688\n"
"buffer_load_dwordx4 v0, s[16:19], s3 offen lds\n"
"s_add_u32 m0, s56, 8192\n"
"s_mov_b32 s3, 131072\n"
"buffer_load_dwordx4 v0, s[16:19], s3 offen lds\n"
"s_add_u32 m0, s56, 9216\n"
"s_mov_b32 s3, 147456\n"
"buffer_load_dwordx4 v0, s[16:19], s3 offen lds\n"
"s_add_u32 m0, s56, 10240\n"
"s_mov_b32 s3, 163840\n"
"buffer_load_dwordx4 v0, s[16:19], s3 offen lds\n"
"s_add_u32 m0, s56, 11264\n"
"s_mov_b32 s3, 180224\n"
"buffer_load_dwordx4 v0, s[16:19], s3 offen lds\n"
"s_add_u32 m0, s56, 12288\n"
"s_mov_b32 s3, 196608\n"
"buffer_load_dwordx4 v0, s[16:19], s3 offen lds\n"
"s_add_u32 m0, s56, 13312\n"
"s_mov_b32 s3, 212992\n"
"buffer_load_dwordx4 v0, s[16:19], s3 offen lds\n"
"s_add_u32 m0, s56, 14336\n"
"s_mov_b32 s3, 229376\n"
"buffer_load_dwordx4 v0, s[16:19], s3 offen lds\n"
"s_add_u32 m0, s56, 15360\n"
"s_mov_b32 s3, 245760\n"
"buffer_load_dwordx4 v0, s[16:19], s3 offen lds\n"
"s_add_u32 m0, s56, 16384\n"
"s_mov_b32 s3, 262144\n"
"buffer_load_dwordx4 v0, s[16:19], s3 offen lds\n"
"s_add_u32 m0, s56, 17408\n"
"s_mov_b32 s3, 278528\n"
"buffer_load_dwordx4 v0, s[16:19], s3 offen lds\n"
"s_add_u32 m0, s56, 18432\n"
"s_mov_b32 s3, 294912\n"
"buffer_load_dwordx4 v0, s[16:19], s3 offen lds\n"
"s_add_u32 m0, s56, 19456\n"
"s_mov_b32 s3, 311296\n"
"buffer_load_dwordx4 v0, s[16:19], s3 offen lds\n"
"s_add_u32 m0, s56, 20480\n"
"s_mov_b32 s3, 327680\n"
"buffer_load_dwordx4 v0, s[16:19], s3 offen lds\n"
"s_add_u32 m0, s56, 21504\n"
"s_mov_b32 s3, 344064\n"
"buffer_load_dwordx4 v0, s[16:19], s3 offen lds\n"
"s_add_u32 m0, s56, 22528\n"
"s_mov_b32 s3, 360448\n"
"buffer_load_dwordx4 v0, s[16:19], s3 offen lds\n"
"s_add_u32 m0, s56, 23552\n"
"s_mov_b32 s3, 376832\n"
"buffer_load_dwordx4 v0, s[16:19], s3 offen lds\n"
"s_add_u32 m0, s56, 24576\n"
"s_mov_b32 s3, 393216\n"
"buffer_load_dwordx4 v0, s[16:19], s3 offen lds\n"
"s_add_u32 m0, s56, 25600\n"
"s_mov_b32 s3, 409600\n"
"buffer_load_dwordx4 v0, s[16:19], s3 offen lds\n"
"s_add_u32 m0, s56, 26624\n"
"s_mov_b32 s3, 425984\n"
"buffer_load_dwordx4 v0, s[16:19], s3 offen lds\n"
"s_add_u32 m0, s56, 27648\n"
"s_mov_b32 s3, 442368\n"
"buffer_load_dwordx4 v0, s[16:19], s3 offen lds\n"
"s_add_u32 m0, s56, 28672\n"
"s_mov_b32 s3, 458752\n"
"buffer_load_dwordx4 v0, s[16:19], s3 offen lds\n"
"s_add_u32 m0, s56, 29696\n"
"s_mov_b32 s3, 475136\n"
"buffer_load_dwordx4 v0, s[16:19], s3 offen lds\n"
"s_add_u32 m0, s56, 30720\n"
"s_mov_b32 s3, 491520\n"
"buffer_load_dwordx4 v0, s[16:19], s3 offen lds\n"
"s_add_u32 m0, s56, 31744\n"
"s_mov_b32 s3, 507904\n"
"buffer_load_dwordx4 v0, s[16:19], s3 offen lds\n"
"s_add_u32 m0, s56, 32768\n"
"s_mov_b32 s3, 524288\n"
"buffer_load_dwordx4 v0, s[16:19], s3 offen lds\n"
"s_add_u32 m0, s56, 33792\n"
"s_mov_b32 s3, 540672\n"
"buffer_load_dwordx4 v0, s[16:19], s3 offen lds\n"
"s_add_u32 m0, s56, 34816\n"
"s_mov_b32 s3, 557056\n"
"buffer_load_dwordx4 v0, s[16:19], s3 offen lds\n"
"s_add_u32 m0, s56, 35840\n"
"s_mov_b32 s3, 573440\n"
"buffer_load_dwordx4 v0, s[16:19], s3 offen lds\n"
"s_add_u32 m0, s56, 36864\n"
"s_mov_b32 s3, 589824\n"
"buffer_load_dwordx4 v0, s[16:19], s3 offen lds\n"
"s_add_u32 m0, s56, 37888\n"
"s_mov_b32 s3, 606208\n"
"buffer_load_dwordx4 v0, s[16:19], s3 offen lds\n"
"s_add_u32 m0, s56, 38912\n"
"s_mov_b32 s3, 622592\n"
"buffer_load_dwordx4 v0, s[16:19], s3 offen lds\n"
"s_add_u32 m0, s56, 39936\n"
"s_mov_b32 s3, 638976\n"
"buffer_load_dwordx4 v0, s[16:19], s3 offen lds\n"
"v_lshlrev_b32 v240, 4, v255\n"
"s_lshl_b32 s3, s5, 13\n"
"s_add_u32 s58, s3, s66\n"
"s_add_u32 s20, s10, 0x0\n"
"s_addc_u32 s21, s11, 0\n"
"s_add_u32 s20, s20, s58\n"
"s_addc_u32 s21, s21, 0\n"
"s_and_b32 s21, s21, 0xffff\n"
"s_mov_b32 s22, 0xfd0000\n"
"s_mov_b32 s23, 0x20000\n"
"s_lshl_b32 s3, s5, 13\n"
"s_sub_u32 s22, s22, s3\n"
"s_lshl_b32 s58, s40, 24\n"
"s_lshl_b32 s3, s46, 16\n"
"s_add_u32 s59, s58, s3\n"
"s_lshl_b32 s3, s5, 14\n"
"s_add_u32 s58, s59, s3\n"
"s_lshl_b32 s3, s39, 12\n"
"s_add_u32 s58, s58, s3\n"
"s_add_u32 s28, s10, 0x8800000\n"
"s_addc_u32 s29, s11, 0\n"
"s_add_u32 s28, s28, s58\n"
"s_addc_u32 s29, s29, 0\n"
"s_and_b32 s29, s29, 0xffff\n"
"s_mov_b32 s30, 0xfc1000\n"
"s_mov_b32 s31, 0x20000\n"
"s_lshl_b32 s3, s39, 14\n"
"s_add_u32 s58, s59, s3\n"
"s_add_u32 s60, s10, 0x8800000\n"
"s_addc_u32 s61, s11, 0\n"
"s_add_u32 s60, s60, s58\n"
"s_addc_u32 s61, s61, 0\n"
"s_and_b32 s61, s61, 0xffff\n"
"s_mov_b32 s62, 0xfc4000\n"
"s_mov_b32 s63, 0x20000\n"
"s_lshl_b32 s58, s40, 10\n"
"s_lshl_b32 s3, s46, 8\n"
"s_add_u32 s58, s58, s3\n"
"s_lshl_b32 s3, s39, 6\n"
"s_add_u32 s58, s58, s3\n"
"s_add_u32 s24, s54, 0x0\n"
"s_addc_u32 s25, s55, 0\n"
"s_add_u32 s24, s24, s58\n"
"s_addc_u32 s25, s25, 0\n"
"s_and_b32 s25, s25, 0xffff\n"
"s_mov_b32 s26, 0x400000\n"
"s_mov_b32 s27, 0x20000\n"
"s_sub_u32 s26, s26, s58\n"
"v_mov_b32 v246, 0x0\n"
"v_bfe_i32 v4, v255, 0, 1\n"
"v_and_b32 v4, 0x10, v4\n"
"v_add_u32 v246, v246, v4\n"
"v_bfe_i32 v4, v255, 1, 1\n"
"v_and_b32 v4, 0x20, v4\n"
"v_add_u32 v246, v246, v4\n"
"v_bfe_i32 v4, v255, 2, 1\n"
"v_and_b32 v4, 0x40, v4\n"
"v_add_u32 v246, v246, v4\n"
"v_bfe_i32 v4, v255, 3, 1\n"
"v_and_b32 v4, 0x80, v4\n"
"v_add_u32 v246, v246, v4\n"
"v_bfe_i32 v4, v255, 4, 1\n"
"v_and_b32 v4, 0x400, v4\n"
"v_add_u32 v246, v246, v4\n"
"v_bfe_i32 v4, v255, 5, 1\n"
"v_and_b32 v4, 0x800, v4\n"
"v_add_u32 v246, v246, v4\n"
"v_mov_b32 v247, 0x0\n"
"v_bfe_i32 v4, v255, 0, 1\n"
"v_and_b32 v4, 0x800, v4\n"
"v_add_u32 v247, v247, v4\n"
"v_bfe_i32 v4, v255, 1, 1\n"
"v_and_b32 v4, 0x1000, v4\n"
"v_add_u32 v247, v247, v4\n"
"v_bfe_i32 v4, v255, 2, 1\n"
"v_and_b32 v4, 0x2000, v4\n"
"v_add_u32 v247, v247, v4\n"
"v_bfe_i32 v4, v255, 3, 1\n"
"v_and_b32 v4, 0x4000, v4\n"
"v_add_u32 v247, v247, v4\n"
"v_bfe_i32 v4, v255, 4, 1\n"
"v_and_b32 v4, 0x10, v4\n"
"v_add_u32 v247, v247, v4\n"
"v_bfe_i32 v4, v255, 5, 1\n"
"v_and_b32 v4, 0x20, v4\n"
"v_add_u32 v247, v247, v4\n"
"s_and_b32 s3, s5, 1\n"
"s_lshl_b32 s58, s3, 8\n"
"v_add_u32 v246, s58, v246\n"
"s_lshl_b32 s58, s3, 15\n"
"v_add_u32 v247, s58, v247\n"
"s_and_b32 s47, s69, 0xff\n"
"s_bfe_u32 s50, s69, 0x80008\n"
"s_bfe_u32 s35, s69, 0x80010\n"
"s_lshr_b32 s36, s69, 24\n"
"s_lshl_b32 s34, s50, 18\n"
"s_sub_u32 s34, s34, 0x8000\n"
"s_mov_b32 s45, 0\n"
"v_and_b32 v242, 7, v255\n"
"s_lshl_b32 s3, s39, 3\n"
"v_add_u32 v242, s3, v242\n"
"v_lshlrev_b32 v242, 6, v242\n"
"s_cmp_eq_u32 s40, 0\n"
"s_cselect_b32 s3, -1, 0\n"
"v_and_b32 v242, s3, v242\n"
"v_and_b32 v245, 3, v255\n"
"v_lshlrev_b32 v245, 7, v245\n"
"s_lshl_b32 s51, s39, 7\n"
"s_add_u32 s51, s51, s68\n"
"s_cmp_eq_u32 s5, 0\n"
"s_cselect_b32 s51, s51, 0x7fff0000\n"
"s_waitcnt vmcnt(32)\n"
"ds_read_b64_tr_b16 a[0:1], v1 offset:0\n"
"ds_read_b64_tr_b16 a[2:3], v1 offset:512\n"
"ds_read_b64_tr_b16 a[4:5], v1 offset:1024\n"
"ds_read_b64_tr_b16 a[6:7], v1 offset:1536\n"
"ds_read_b64_tr_b16 a[8:9], v1 offset:2048\n"
"ds_read_b64_tr_b16 a[10:11], v1 offset:2560\n"
"ds_read_b64_tr_b16 a[12:13], v1 offset:3072\n"
"ds_read_b64_tr_b16 a[14:15], v1 offset:3584\n"
"ds_read_b64_tr_b16 a[16:17], v1 offset:4096\n"
"ds_read_b64_tr_b16 a[18:19], v1 offset:4608\n"
"ds_read_b64_tr_b16 a[20:21], v1 offset:5120\n"
"ds_read_b64_tr_b16 a[22:23], v1 offset:5632\n"
"ds_read_b64_tr_b16 a[24:25], v1 offset:6144\n"
"ds_read_b64_tr_b16 a[26:27], v1 offset:6656\n"
"ds_read_b64_tr_b16 a[28:29], v1 offset:7168\n"
"ds_read_b64_tr_b16 a[30:31], v1 offset:7680\n"
"s_waitcnt lgkmcnt(0)\n"
"s_add_u32 m0, s56, 0\n"
"s_mov_b32 s3, 655360\n"
"buffer_load_dwordx4 v0, s[16:19], s3 offen lds\n"
"s_add_u32 m0, s56, 1024\n"
"s_mov_b32 s3, 671744\n"
"buffer_load_dwordx4 v0, s[16:19], s3 offen lds\n"
"s_add_u32 m0, s56, 2048\n"
"s_mov_b32 s3, 688128\n"
"buffer_load_dwordx4 v0, s[16:19], s3 offen lds\n"
"s_add_u32 m0, s56, 3072\n"
"s_mov_b32 s3, 704512\n"
"buffer_load_dwordx4 v0, s[16:19], s3 offen lds\n"
"s_add_u32 m0, s56, 4096\n"
"s_mov_b32 s3, 720896\n"
"buffer_load_dwordx4 v0, s[16:19], s3 offen lds\n"
"s_add_u32 m0, s56, 5120\n"
"s_mov_b32 s3, 737280\n"
"buffer_load_dwordx4 v0, s[16:19], s3 offen lds\n"
"s_add_u32 m0, s56, 6144\n"
"s_mov_b32 s3, 753664\n"
"buffer_load_dwordx4 v0, s[16:19], s3 offen lds\n"
"s_add_u32 m0, s56, 7168\n"
"s_mov_b32 s3, 770048\n"
"buffer_load_dwordx4 v0, s[16:19], s3 offen lds\n"
"s_waitcnt vmcnt(32)\n"
"ds_read_b64_tr_b16 a[32:33], v1 offset:8192\n"
"ds_read_b64_tr_b16 a[34:35], v1 offset:8704\n"
"ds_read_b64_tr_b16 a[36:37], v1 offset:9216\n"
"ds_read_b64_tr_b16 a[38:39], v1 offset:9728\n"
"ds_read_b64_tr_b16 a[40:41], v1 offset:10240\n"
"ds_read_b64_tr_b16 a[42:43], v1 offset:10752\n"
"ds_read_b64_tr_b16 a[44:45], v1 offset:11264\n"
"ds_read_b64_tr_b16 a[46:47], v1 offset:11776\n"
"ds_read_b64_tr_b16 a[48:49], v1 offset:12288\n"
"ds_read_b64_tr_b16 a[50:51], v1 offset:12800\n"
"ds_read_b64_tr_b16 a[52:53], v1 offset:13312\n"
"ds_read_b64_tr_b16 a[54:55], v1 offset:13824\n"
"ds_read_b64_tr_b16 a[56:57], v1 offset:14336\n"
"ds_read_b64_tr_b16 a[58:59], v1 offset:14848\n"
"ds_read_b64_tr_b16 a[60:61], v1 offset:15360\n"
"ds_read_b64_tr_b16 a[62:63], v1 offset:15872\n"
"s_waitcnt lgkmcnt(0)\n"
"s_add_u32 m0, s56, 8192\n"
"s_mov_b32 s3, 786432\n"
"buffer_load_dwordx4 v0, s[16:19], s3 offen lds\n"
"s_add_u32 m0, s56, 9216\n"
"s_mov_b32 s3, 802816\n"
"buffer_load_dwordx4 v0, s[16:19], s3 offen lds\n"
"s_add_u32 m0, s56, 10240\n"
"s_mov_b32 s3, 819200\n"
"buffer_load_dwordx4 v0, s[16:19], s3 offen lds\n"
"s_add_u32 m0, s56, 11264\n"
"s_mov_b32 s3, 835584\n"
"buffer_load_dwordx4 v0, s[16:19], s3 offen lds\n"
"s_add_u32 m0, s56, 12288\n"
"s_mov_b32 s3, 851968\n"
"buffer_load_dwordx4 v0, s[16:19], s3 offen lds\n"
"s_add_u32 m0, s56, 13312\n"
"s_mov_b32 s3, 868352\n"
"buffer_load_dwordx4 v0, s[16:19], s3 offen lds\n"
"s_add_u32 m0, s56, 14336\n"
"s_mov_b32 s3, 884736\n"
"buffer_load_dwordx4 v0, s[16:19], s3 offen lds\n"
"s_add_u32 m0, s56, 15360\n"
"s_mov_b32 s3, 901120\n"
"buffer_load_dwordx4 v0, s[16:19], s3 offen lds\n"
"s_waitcnt vmcnt(32)\n"
"ds_read_b64_tr_b16 a[64:65], v1 offset:16384\n"
"ds_read_b64_tr_b16 a[66:67], v1 offset:16896\n"
"ds_read_b64_tr_b16 a[68:69], v1 offset:17408\n"
"ds_read_b64_tr_b16 a[70:71], v1 offset:17920\n"
"ds_read_b64_tr_b16 a[72:73], v1 offset:18432\n"
"ds_read_b64_tr_b16 a[74:75], v1 offset:18944\n"
"ds_read_b64_tr_b16 a[76:77], v1 offset:19456\n"
"ds_read_b64_tr_b16 a[78:79], v1 offset:19968\n"
"ds_read_b64_tr_b16 a[80:81], v1 offset:20480\n"
"ds_read_b64_tr_b16 a[82:83], v1 offset:20992\n"
"ds_read_b64_tr_b16 a[84:85], v1 offset:21504\n"
"ds_read_b64_tr_b16 a[86:87], v1 offset:22016\n"
"ds_read_b64_tr_b16 a[88:89], v1 offset:22528\n"
"ds_read_b64_tr_b16 a[90:91], v1 offset:23040\n"
"ds_read_b64_tr_b16 a[92:93], v1 offset:23552\n"
"ds_read_b64_tr_b16 a[94:95], v1 offset:24064\n"
"s_waitcnt lgkmcnt(0)\n"
"s_add_u32 m0, s56, 16384\n"
"s_mov_b32 s3, 917504\n"
"buffer_load_dwordx4 v0, s[16:19], s3 offen lds\n"
"s_add_u32 m0, s56, 17408\n"
"s_mov_b32 s3, 933888\n"
"buffer_load_dwordx4 v0, s[16:19], s3 offen lds\n"
"s_add_u32 m0, s56, 18432\n"
"s_mov_b32 s3, 950272\n"
"buffer_load_dwordx4 v0, s[16:19], s3 offen lds\n"
"s_add_u32 m0, s56, 19456\n"
"s_mov_b32 s3, 966656\n"
"buffer_load_dwordx4 v0, s[16:19], s3 offen lds\n"
"s_add_u32 m0, s56, 20480\n"
"s_mov_b32 s3, 983040\n"
"buffer_load_dwordx4 v0, s[16:19], s3 offen lds\n"
"s_add_u32 m0, s56, 21504\n"
"s_mov_b32 s3, 999424\n"
"buffer_load_dwordx4 v0, s[16:19], s3 offen lds\n"
"s_add_u32 m0, s56, 22528\n"
"s_mov_b32 s3, 1015808\n"
"buffer_load_dwordx4 v0, s[16:19], s3 offen lds\n"
"s_add_u32 m0, s56, 23552\n"
"s_mov_b32 s3, 1032192\n"
"buffer_load_dwordx4 v0, s[16:19], s3 offen lds\n"
"s_waitcnt vmcnt(32)\n"
"ds_read_b64_tr_b16 a[96:97], v1 offset:24576\n"
"ds_read_b64_tr_b16 a[98:99], v1 offset:25088\n"
"ds_read_b64_tr_b16 a[100:101], v1 offset:25600\n"
"ds_read_b64_tr_b16 a[102:103], v1 offset:26112\n"
"ds_read_b64_tr_b16 a[104:105], v1 offset:26624\n"
"ds_read_b64_tr_b16 a[106:107], v1 offset:27136\n"
"ds_read_b64_tr_b16 a[108:109], v1 offset:27648\n"
"ds_read_b64_tr_b16 a[110:111], v1 offset:28160\n"
"ds_read_b64_tr_b16 a[112:113], v1 offset:28672\n"
"ds_read_b64_tr_b16 a[114:115], v1 offset:29184\n"
"ds_read_b64_tr_b16 a[116:117], v1 offset:29696\n"
"ds_read_b64_tr_b16 a[118:119], v1 offset:30208\n"
"ds_read_b64_tr_b16 a[120:121], v1 offset:30720\n"
"ds_read_b64_tr_b16 a[122:123], v1 offset:31232\n"
"ds_read_b64_tr_b16 a[124:125], v1 offset:31744\n"
"ds_read_b64_tr_b16 a[126:127], v1 offset:32256\n"
"s_waitcnt vmcnt(24)\n"
"ds_read_b64_tr_b16 a[128:129], v1 offset:32768\n"
"ds_read_b64_tr_b16 a[130:131], v1 offset:33280\n"
"ds_read_b64_tr_b16 a[132:133], v1 offset:33792\n"
"ds_read_b64_tr_b16 a[134:135], v1 offset:34304\n"
"ds_read_b64_tr_b16 a[136:137], v1 offset:34816\n"
"ds_read_b64_tr_b16 a[138:139], v1 offset:35328\n"
"ds_read_b64_tr_b16 a[140:141], v1 offset:35840\n"
"ds_read_b64_tr_b16 a[142:143], v1 offset:36352\n"
"ds_read_b64_tr_b16 a[144:145], v1 offset:36864\n"
"ds_read_b64_tr_b16 a[146:147], v1 offset:37376\n"
"ds_read_b64_tr_b16 a[148:149], v1 offset:37888\n"
"ds_read_b64_tr_b16 a[150:151], v1 offset:38400\n"
"ds_read_b64_tr_b16 a[152:153], v1 offset:38912\n"
"ds_read_b64_tr_b16 a[154:155], v1 offset:39424\n"
"ds_read_b64_tr_b16 a[156:157], v1 offset:39936\n"
"ds_read_b64_tr_b16 a[158:159], v1 offset:40448\n"
"s_waitcnt vmcnt(16)\n"
"ds_read_b64_tr_b16 a[160:161], v1 offset:0\n"
"ds_read_b64_tr_b16 a[162:163], v1 offset:512\n"
"ds_read_b64_tr_b16 a[164:165], v1 offset:1024\n"
"ds_read_b64_tr_b16 a[166:167], v1 offset:1536\n"
"ds_read_b64_tr_b16 a[168:169], v1 offset:2048\n"
"ds_read_b64_tr_b16 a[170:171], v1 offset:2560\n"
"ds_read_b64_tr_b16 a[172:173], v1 offset:3072\n"
"ds_read_b64_tr_b16 a[174:175], v1 offset:3584\n"
"ds_read_b64_tr_b16 a[176:177], v1 offset:4096\n"
"ds_read_b64_tr_b16 a[178:179], v1 offset:4608\n"
"ds_read_b64_tr_b16 a[180:181], v1 offset:5120\n"
"ds_read_b64_tr_b16 a[182:183], v1 offset:5632\n"
"ds_read_b64_tr_b16 a[184:185], v1 offset:6144\n"
"ds_read_b64_tr_b16 a[186:187], v1 offset:6656\n"
"ds_read_b64_tr_b16 a[188:189], v1 offset:7168\n"
"ds_read_b64_tr_b16 a[190:191], v1 offset:7680\n"
"s_waitcnt vmcnt(8)\n"
"ds_read_b64_tr_b16 a[192:193], v1 offset:8192\n"
"ds_read_b64_tr_b16 a[194:195], v1 offset:8704\n"
"ds_read_b64_tr_b16 a[196:197], v1 offset:9216\n"
"ds_read_b64_tr_b16 a[198:199], v1 offset:9728\n"
"ds_read_b64_tr_b16 a[200:201], v1 offset:10240\n"
"ds_read_b64_tr_b16 a[202:203], v1 offset:10752\n"
"ds_read_b64_tr_b16 a[204:205], v1 offset:11264\n"
"ds_read_b64_tr_b16 a[206:207], v1 offset:11776\n"
"ds_read_b64_tr_b16 a[208:209], v1 offset:12288\n"
"ds_read_b64_tr_b16 a[210:211], v1 offset:12800\n"
"ds_read_b64_tr_b16 a[212:213], v1 offset:13312\n"
"ds_read_b64_tr_b16 a[214:215], v1 offset:13824\n"
"ds_read_b64_tr_b16 a[216:217], v1 offset:14336\n"
"ds_read_b64_tr_b16 a[218:219], v1 offset:14848\n"
"ds_read_b64_tr_b16 a[220:221], v1 offset:15360\n"
"ds_read_b64_tr_b16 a[222:223], v1 offset:15872\n"
"s_waitcnt vmcnt(0)\n"
"ds_read_b64_tr_b16 a[224:225], v1 offset:16384\n"
"ds_read_b64_tr_b16 a[226:227], v1 offset:16896\n"
"ds_read_b64_tr_b16 a[228:229], v1 offset:17408\n"
"ds_read_b64_tr_b16 a[230:231], v1 offset:17920\n"
"ds_read_b64_tr_b16 a[232:233], v1 offset:18432\n"
"ds_read_b64_tr_b16 a[234:235], v1 offset:18944\n"
"ds_read_b64_tr_b16 a[236:237], v1 offset:19456\n"
"ds_read_b64_tr_b16 a[238:239], v1 offset:19968\n"
"ds_read_b64_tr_b16 a[240:241], v1 offset:20480\n"
"ds_read_b64_tr_b16 a[242:243], v1 offset:20992\n"
"ds_read_b64_tr_b16 a[244:245], v1 offset:21504\n"
"ds_read_b64_tr_b16 a[246:247], v1 offset:22016\n"
"ds_read_b64_tr_b16 a[248:249], v1 offset:22528\n"
"ds_read_b64_tr_b16 a[250:251], v1 offset:23040\n"
"ds_read_b64_tr_b16 a[252:253], v1 offset:23552\n"
"ds_read_b64_tr_b16 a[254:255], v1 offset:24064\n"
"L_oe_batch:\n"
"s_mov_b32 s33, s47\n"
"s_mov_b32 s43, s35\n"
"L_oe_chk:\n"
"s_cmp_eq_u32 s40, 0\n"
"s_cbranch_scc0 L_oe_b\n"
"s_and_b32 s3, s33, 3\n"
"s_lshl_b32 s3, s3, 11\n"
"s_add_u32 s38, s3, s70\n"
"s_add_u32 s3, s33, 1\n"
"s_branch L_oe_w\n"
"L_oe_b:\n"
"s_lshr_b32 s3, s33, 3\n"
"s_lshl_b32 s3, s3, 7\n"
"s_add_u32 s38, s3, s70\n"
"s_mov_b32 s3, 127\n"
"L_oe_w:\n"
"v_mov_b32 v243, s3\n"
"Lw_11:\n"
"buffer_load_dword v244, v242, s[12:15], s38 offen sc1\n"
"s_waitcnt vmcnt(0)\n"
"v_cmp_lt_i32 vcc, v244, v243\n"
"s_cbranch_vccz Lwok_12\n"
"s_sub_u32 s8, s8, 1\n"
"s_cbranch_scc1 L_bulk_fail\n"
"s_sleep 4\n"
"s_branch Lw_11\n"
"Lwok_12:\n"
"s_add_u32 s33, s33, s50\n"
"s_sub_u32 s43, s43, 1\n"
"s_cmp_lg_u32 s43, 0\n"
"s_cbranch_scc1 L_oe_chk\n"
"s_waitcnt lgkmcnt(0)\n"
"s_barrier\n"
"s_mov_b32 s33, s47\n"
"s_mov_b32 s43, s35\n"
"s_lshl_b32 s41, s47, 18\n"
"s_lshl_b32 s42, s5, 13\n"
"s_mov_b32 s49, 0\n"
"s_mov_b32 s44, 0x7fff0000\n"
"s_add_u32 m0, s42, 0\n"
"s_add_u32 s37, s41, 0\n"
"buffer_load_dwordx4 v240, s[20:23], s37 offen offset:0 sc1 lds\n"
"buffer_load_dwordx4 v240, s[20:23], s37 offen offset:1024 sc1 lds\n"
"buffer_load_dwordx4 v240, s[20:23], s37 offen offset:2048 sc1 lds\n"
"buffer_load_dwordx4 v240, s[20:23], s37 offen offset:3072 sc1 lds\n"
"s_add_u32 m0, s42, 4096\n"
"s_add_u32 s37, s41, 4096\n"
"buffer_load_dwordx4 v240, s[20:23], s37 offen offset:0 sc1 lds\n"
"buffer_load_dwordx4 v240, s[20:23], s37 offen offset:1024 sc1 lds\n"
"buffer_load_dwordx4 v240, s[20:23], s37 offen offset:2048 sc1 lds\n"
"buffer_load_dwordx4 v240, s[20:23], s37 offen offset:3072 sc1 lds\n"
"s_add_u32 s42, s42, 0x8000\n"
"s_cmp_ge_u32 s42, 163840\n"
"s_cselect_b32 s3, 163840, 0\n"
"s_sub_u32 s42, s42, s3\n"
"s_add_u32 s41, s41, 0x8000\n"
"s_add_u32 m0, s42, 0\n"
"s_add_u32 s37, s41, 0\n"
"buffer_load_dwordx4 v240, s[20:23], s37 offen offset:0 sc1 lds\n"
"buffer_load_dwordx4 v240, s[20:23], s37 offen offset:1024 sc1 lds\n"
"buffer_load_dwordx4 v240, s[20:23], s37 offen offset:2048 sc1 lds\n"
"buffer_load_dwordx4 v240, s[20:23], s37 offen offset:3072 sc1 lds\n"
"s_add_u32 m0, s42, 4096\n"
"s_add_u32 s37, s41, 4096\n"
"buffer_load_dwordx4 v240, s[20:23], s37 offen offset:0 sc1 lds\n"
"buffer_load_dwordx4 v240, s[20:23], s37 offen offset:1024 sc1 lds\n"
"buffer_load_dwordx4 v240, s[20:23], s37 offen offset:2048 sc1 lds\n"
"buffer_load_dwordx4 v240, s[20:23], s37 offen offset:3072 sc1 lds\n"
"s_add_u32 s42, s42, 0x8000\n"
"s_cmp_ge_u32 s42, 163840\n"
"s_cselect_b32 s3, 163840, 0\n"
"s_sub_u32 s42, s42, s3\n"
"s_add_u32 s41, s41, s34\n"
"s_sub_u32 s43, s43, 1\n"
"s_cmp_gt_i32 s43, 0\n"
"s_cselect_b32 s41, s41, 0x7fff0000\n"
"s_add_u32 m0, s42, 0\n"
"s_add_u32 s37, s41, 0\n"
"buffer_load_dwordx4 v240, s[20:23], s37 offen offset:0 sc1 lds\n"
"buffer_load_dwordx4 v240, s[20:23], s37 offen offset:1024 sc1 lds\n"
"buffer_load_dwordx4 v240, s[20:23], s37 offen offset:2048 sc1 lds\n"
"buffer_load_dwordx4 v240, s[20:23], s37 offen offset:3072 sc1 lds\n"
"s_add_u32 m0, s42, 4096\n"
"s_add_u32 s37, s41, 4096\n"
"buffer_load_dwordx4 v240, s[20:23], s37 offen offset:0 sc1 lds\n"
"buffer_load_dwordx4 v240, s[20:23], s37 offen offset:1024 sc1 lds\n"
"buffer_load_dwordx4 v240, s[20:23], s37 offen offset:2048 sc1 lds\n"
"buffer_load_dwordx4 v240, s[20:23], s37 offen offset:3072 sc1 lds\n"
"s_add_u32 s42, s42, 0x8000\n"
"s_cmp_ge_u32 s42, 163840\n"
"s_cselect_b32 s3, 163840, 0\n"
"s_sub_u32 s42, s42, s3\n"
"s_add_u32 s41, s41, 0x8000\n"
"s_mov_b32 s48, s35\n"
"v_mov_b32 v241, v240\n"
"s_waitcnt vmcnt(16)\n"
"s_barrier\n"
"ds_read_b128 v[32:35], v241 offset:0\n"
"ds_read_b128 v[36:39], v241 offset:1024\n"
"ds_read_b128 v[40:43], v241 offset:2048\n"
"ds_read_b128 v[44:47], v241 offset:3072\n"
"ds_read_b128 v[48:51], v241 offset:4096\n"
"ds_read_b128 v[52:55], v241 offset:5120\n"
".p2align 6\n"
"L_oe_loop:\n"
"s_waitcnt lgkmcnt(5)\n"
"v_mfma_f32_32x32x16_bf16 v[0:15], a[0:3], v[32:35], 0\n"
"ds_read_b128 v[56:59], v241 offset:6144\n"
"s_waitcnt lgkmcnt(5)\n"
"v_mfma_f32_32x32x16_bf16 v[0:15], a[4:7], v[36:39], v[0:15]\n"
"ds_read_b128 v[60:63], v241 offset:7168\n"
"s_waitcnt lgkmcnt(5)\n"
"v_mfma_f32_32x32x16_bf16 v[0:15], a[8:11], v[40:43], v[0:15]\n"
"ds_read_b128 v[32:35], v241 offset:8192\n"
"s_waitcnt lgkmcnt(5)\n"
"v_mfma_f32_32x32x16_bf16 v[0:15], a[12:15], v[44:47], v[0:15]\n"
"ds_read_b128 v[36:39], v241 offset:9216\n"
"s_waitcnt lgkmcnt(5)\n"
"v_mfma_f32_32x32x16_bf16 v[0:15], a[16:19], v[48:51], v[0:15]\n"
"ds_read_b128 v[40:43], v241 offset:10240\n"
"s_waitcnt lgkmcnt(5)\n"
"v_mfma_f32_32x32x16_bf16 v[0:15], a[20:23], v[52:55], v[0:15]\n"
"ds_read_b128 v[44:47], v241 offset:11264\n"
"s_waitcnt lgkmcnt(5)\n"
"v_mfma_f32_32x32x16_bf16 v[0:15], a[24:27], v[56:59], v[0:15]\n"
"ds_read_b128 v[48:51], v241 offset:12288\n"
"s_waitcnt lgkmcnt(5)\n"
"v_mfma_f32_32x32x16_bf16 v[0:15], a[28:31], v[60:63], v[0:15]\n"
"ds_read_b128 v[52:55], v241 offset:13312\n"
"s_waitcnt lgkmcnt(5)\n"
"v_mfma_f32_32x32x16_bf16 v[0:15], a[32:35], v[32:35], v[0:15]\n"
"ds_read_b128 v[56:59], v241 offset:14336\n"
"buffer_store_dwordx4 v[16:19], v240, s[28:31], s44 offen offset:0 sc1\n"
"s_waitcnt lgkmcnt(5)\n"
"v_mfma_f32_32x32x16_bf16 v[0:15], a[36:39], v[36:39], v[0:15]\n"
"ds_read_b128 v[60:63], v241 offset:15360\n"
"buffer_store_dwordx4 v[20:23], v240, s[28:31], s44 offen offset:1024 sc1\n"
"s_waitcnt lgkmcnt(5)\n"
"v_mfma_f32_32x32x16_bf16 v[0:15], a[40:43], v[40:43], v[0:15]\n"
"ds_read_b128 v[32:35], v241 offset:16384\n"
"buffer_store_dwordx4 v[24:27], v240, s[28:31], s44 offen offset:2048 sc1\n"
"s_waitcnt lgkmcnt(5)\n"
"v_mfma_f32_32x32x16_bf16 v[0:15], a[44:47], v[44:47], v[0:15]\n"
"ds_read_b128 v[36:39], v241 offset:17408\n"
"buffer_store_dwordx4 v[28:31], v240, s[28:31], s44 offen offset:3072 sc1\n"
"s_waitcnt lgkmcnt(5)\n"
"v_mfma_f32_32x32x16_bf16 v[0:15], a[48:51], v[48:51], v[0:15]\n"
"ds_read_b128 v[40:43], v241 offset:18432\n"
"s_lshl_b32 s44, s33, 18\n"
"s_add_u32 s33, s33, s50\n"
"s_waitcnt lgkmcnt(5)\n"
"v_mfma_f32_32x32x16_bf16 v[0:15], a[52:55], v[52:55], v[0:15]\n"
"ds_read_b128 v[44:47], v241 offset:19456\n"
"s_add_u32 m0, s42, 0\n"
"s_add_u32 s37, s41, 0\n"
"buffer_load_dwordx4 v240, s[20:23], s37 offen offset:0 sc1 lds\n"
"s_waitcnt lgkmcnt(5)\n"
"v_mfma_f32_32x32x16_bf16 v[0:15], a[56:59], v[56:59], v[0:15]\n"
"ds_read_b128 v[48:51], v241 offset:20480\n"
"buffer_load_dwordx4 v240, s[20:23], s37 offen offset:1024 sc1 lds\n"
"s_waitcnt lgkmcnt(5)\n"
"v_mfma_f32_32x32x16_bf16 v[0:15], a[60:63], v[60:63], v[0:15]\n"
"ds_read_b128 v[52:55], v241 offset:21504\n"
"buffer_load_dwordx4 v240, s[20:23], s37 offen offset:2048 sc1 lds\n"
"s_waitcnt lgkmcnt(5)\n"
"v_mfma_f32_32x32x16_bf16 v[0:15], a[64:67], v[32:35], v[0:15]\n"
"ds_read_b128 v[56:59], v241 offset:22528\n"
"buffer_load_dwordx4 v240, s[20:23], s37 offen offset:3072 sc1 lds\n"
"s_waitcnt lgkmcnt(5)\n"
"v_mfma_f32_32x32x16_bf16 v[0:15], a[68:71], v[36:39], v[0:15]\n"
"ds_read_b128 v[60:63], v241 offset:23552\n"
"s_add_u32 m0, s42, 4096\n"
"s_add_u32 s37, s41, 4096\n"
"buffer_load_dwordx4 v240, s[20:23], s37 offen offset:0 sc1 lds\n"
"s_waitcnt lgkmcnt(5)\n"
"v_mfma_f32_32x32x16_bf16 v[0:15], a[72:75], v[40:43], v[0:15]\n"
"ds_read_b128 v[32:35], v241 offset:24576\n"
"buffer_load_dwordx4 v240, s[20:23], s37 offen offset:1024 sc1 lds\n"
"s_waitcnt lgkmcnt(5)\n"
"v_mfma_f32_32x32x16_bf16 v[0:15], a[76:79], v[44:47], v[0:15]\n"
"ds_read_b128 v[36:39], v241 offset:25600\n"
"buffer_load_dwordx4 v240, s[20:23], s37 offen offset:2048 sc1 lds\n"
"s_waitcnt lgkmcnt(5)\n"
"v_mfma_f32_32x32x16_bf16 v[0:15], a[80:83], v[48:51], v[0:15]\n"
"ds_read_b128 v[40:43], v241 offset:26624\n"
"buffer_load_dwordx4 v240, s[20:23], s37 offen offset:3072 sc1 lds\n"
"s_waitcnt lgkmcnt(5)\n"
"v_mfma_f32_32x32x16_bf16 v[0:15], a[84:87], v[52:55], v[0:15]\n"
"ds_read_b128 v[44:47], v241 offset:27648\n"
"s_add_u32 s42, s42, 0x8000\n"
"s_cmp_ge_u32 s42, 163840\n"
"s_cselect_b32 s3, 163840, 0\n"
"s_sub_u32 s42, s42, s3\n"
"s_waitcnt lgkmcnt(5)\n"
"v_mfma_f32_32x32x16_bf16 v[0:15], a[88:91], v[56:59], v[0:15]\n"
"ds_read_b128 v[48:51], v241 offset:28672\n"
"s_add_u32 s41, s41, s34\n"
"s_sub_u32 s43, s43, 1\n"
"s_cmp_gt_i32 s43, 0\n"
"s_cselect_b32 s41, s41, 0x7fff0000\n"
"s_waitcnt lgkmcnt(5)\n"
"v_mfma_f32_32x32x16_bf16 v[0:15], a[92:95], v[60:63], v[0:15]\n"
"ds_read_b128 v[52:55], v241 offset:29696\n"
"s_waitcnt lgkmcnt(5)\n"
"v_mfma_f32_32x32x16_bf16 v[0:15], a[96:99], v[32:35], v[0:15]\n"
"ds_read_b128 v[56:59], v241 offset:30720\n"
"s_add_u32 s49, s49, 0x8000\n"
"s_cmp_ge_u32 s49, 163840\n"
"s_cselect_b32 s3, 163840, 0\n"
"s_sub_u32 s49, s49, s3\n"
"s_waitcnt lgkmcnt(5)\n"
"v_mfma_f32_32x32x16_bf16 v[0:15], a[100:103], v[36:39], v[0:15]\n"
"ds_read_b128 v[60:63], v241 offset:31744\n"
"s_waitcnt vmcnt(20)\n"
"s_barrier\n"
"v_add_u32 v241, s49, v240\n"
"s_waitcnt lgkmcnt(5)\n"
"v_mfma_f32_32x32x16_bf16 v[0:15], a[104:107], v[40:43], v[0:15]\n"
"ds_read_b128 v[32:35], v241 offset:0\n"
"s_waitcnt lgkmcnt(5)\n"
"v_mfma_f32_32x32x16_bf16 v[0:15], a[108:111], v[44:47], v[0:15]\n"
"ds_read_b128 v[36:39], v241 offset:1024\n"
"s_waitcnt lgkmcnt(5)\n"
"v_mfma_f32_32x32x16_bf16 v[0:15], a[112:115], v[48:51], v[0:15]\n"
"ds_read_b128 v[40:43], v241 offset:2048\n"
"s_waitcnt lgkmcnt(5)\n"
"v_mfma_f32_32x32x16_bf16 v[0:15], a[116:119], v[52:55], v[0:15]\n"
"ds_read_b128 v[44:47], v241 offset:3072\n"
"s_waitcnt lgkmcnt(5)\n"
"v_mfma_f32_32x32x16_bf16 v[0:15], a[120:123], v[56:59], v[0:15]\n"
"ds_read_b128 v[48:51], v241 offset:4096\n"
"s_waitcnt lgkmcnt(5)\n"
"v_mfma_f32_32x32x16_bf16 v[0:15], a[124:127], v[60:63], v[0:15]\n"
"ds_read_b128 v[52:55], v241 offset:5120\n"
"s_waitcnt lgkmcnt(5)\n"
"v_mfma_f32_32x32x16_bf16 v[0:15], a[128:131], v[32:35], v[0:15]\n"
"ds_read_b128 v[56:59], v241 offset:6144\n"
"s_waitcnt lgkmcnt(5)\n"
"v_mfma_f32_32x32x16_bf16 v[0:15], a[132:135], v[36:39], v[0:15]\n"
"ds_read_b128 v[60:63], v241 offset:7168\n"
"s_waitcnt lgkmcnt(5)\n"
"v_mfma_f32_32x32x16_bf16 v[0:15], a[136:139], v[40:43], v[0:15]\n"
"ds_read_b128 v[32:35], v241 offset:8192\n"
"s_waitcnt lgkmcnt(5)\n"
"v_mfma_f32_32x32x16_bf16 v[0:15], a[140:143], v[44:47], v[0:15]\n"
"ds_read_b128 v[36:39], v241 offset:9216\n"
"s_waitcnt lgkmcnt(5)\n"
"v_mfma_f32_32x32x16_bf16 v[0:15], a[144:147], v[48:51], v[0:15]\n"
"ds_read_b128 v[40:43], v241 offset:10240\n"
"s_waitcnt lgkmcnt(5)\n"
"v_mfma_f32_32x32x16_bf16 v[0:15], a[148:151], v[52:55], v[0:15]\n"
"ds_read_b128 v[44:47], v241 offset:11264\n"
"s_waitcnt lgkmcnt(5)\n"
"v_mfma_f32_32x32x16_bf16 v[0:15], a[152:155], v[56:59], v[0:15]\n"
"ds_read_b128 v[48:51], v241 offset:12288\n"
"s_waitcnt lgkmcnt(5)\n"
"v_mfma_f32_32x32x16_bf16 v[0:15], a[156:159], v[60:63], v[0:15]\n"
"ds_read_b128 v[52:55], v241 offset:13312\n"
"s_waitcnt lgkmcnt(5)\n"
"v_mfma_f32_32x32x16_bf16 v[0:15], a[160:163], v[32:35], v[0:15]\n"
"ds_read_b128 v[56:59], v241 offset:14336\n"
"s_waitcnt lgkmcnt(5)\n"
"v_mfma_f32_32x32x16_bf16 v[0:15], a[164:167], v[36:39], v[0:15]\n"
"ds_read_b128 v[60:63], v241 offset:15360\n"
"s_waitcnt lgkmcnt(5)\n"
"v_mfma_f32_32x32x16_bf16 v[0:15], a[168:171], v[40:43], v[0:15]\n"
"ds_read_b128 v[32:35], v241 offset:16384\n"
"s_waitcnt lgkmcnt(5)\n"
"v_mfma_f32_32x32x16_bf16 v[0:15], a[172:175], v[44:47], v[0:15]\n"
"ds_read_b128 v[36:39], v241 offset:17408\n"
"s_waitcnt lgkmcnt(5)\n"
"v_mfma_f32_32x32x16_bf16 v[0:15], a[176:179], v[48:51], v[0:15]\n"
"ds_read_b128 v[40:43], v241 offset:18432\n"
"s_waitcnt lgkmcnt(5)\n"
"v_mfma_f32_32x32x16_bf16 v[0:15], a[180:183], v[52:55], v[0:15]\n"
"ds_read_b128 v[44:47], v241 offset:19456\n"
"s_add_u32 m0, s42, 0\n"
"s_add_u32 s37, s41, 0\n"
"buffer_load_dwordx4 v240, s[20:23], s37 offen offset:0 sc1 lds\n"
"s_waitcnt lgkmcnt(5)\n"
"v_mfma_f32_32x32x16_bf16 v[0:15], a[184:187], v[56:59], v[0:15]\n"
"ds_read_b128 v[48:51], v241 offset:20480\n"
"buffer_load_dwordx4 v240, s[20:23], s37 offen offset:1024 sc1 lds\n"
"s_waitcnt lgkmcnt(5)\n"
"v_mfma_f32_32x32x16_bf16 v[0:15], a[188:191], v[60:63], v[0:15]\n"
"ds_read_b128 v[52:55], v241 offset:21504\n"
"buffer_load_dwordx4 v240, s[20:23], s37 offen offset:2048 sc1 lds\n"
"s_waitcnt lgkmcnt(5)\n"
"v_mfma_f32_32x32x16_bf16 v[0:15], a[192:195], v[32:35], v[0:15]\n"
"ds_read_b128 v[56:59], v241 offset:22528\n"
"buffer_load_dwordx4 v240, s[20:23], s37 offen offset:3072 sc1 lds\n"
"s_waitcnt lgkmcnt(5)\n"
"v_mfma_f32_32x32x16_bf16 v[0:15], a[196:199], v[36:39], v[0:15]\n"
"ds_read_b128 v[60:63], v241 offset:23552\n"
"s_add_u32 m0, s42, 4096\n"
"s_add_u32 s37, s41, 4096\n"
"buffer_load_dwordx4 v240, s[20:23], s37 offen offset:0 sc1 lds\n"
"s_waitcnt lgkmcnt(5)\n"
"v_mfma_f32_32x32x16_bf16 v[0:15], a[200:203], v[40:43], v[0:15]\n"
"ds_read_b128 v[32:35], v241 offset:24576\n"
"buffer_load_dwordx4 v240, s[20:23], s37 offen offset:1024 sc1 lds\n"
"s_waitcnt lgkmcnt(5)\n"
"v_mfma_f32_32x32x16_bf16 v[0:15], a[204:207], v[44:47], v[0:15]\n"
"ds_read_b128 v[36:39], v241 offset:25600\n"
"buffer_load_dwordx4 v240, s[20:23], s37 offen offset:2048 sc1 lds\n"
"s_waitcnt lgkmcnt(5)\n"
"v_mfma_f32_32x32x16_bf16 v[0:15], a[208:211], v[48:51], v[0:15]\n"
"ds_read_b128 v[40:43], v241 offset:26624\n"
"buffer_load_dwordx4 v240, s[20:23], s37 offen offset:3072 sc1 lds\n"
"s_waitcnt lgkmcnt(5)\n"
"v_mfma_f32_32x32x16_bf16 v[0:15], a[212:215], v[52:55], v[0:15]\n"
"ds_read_b128 v[44:47], v241 offset:27648\n"
"s_add_u32 s42, s42, 0x8000\n"
"s_cmp_ge_u32 s42, 163840\n"
"s_cselect_b32 s3, 163840, 0\n"
"s_sub_u32 s42, s42, s3\n"
"s_waitcnt lgkmcnt(5)\n"
"v_mfma_f32_32x32x16_bf16 v[0:15], a[216:219], v[56:59], v[0:15]\n"
"ds_read_b128 v[48:51], v241 offset:28672\n"
"s_add_u32 s41, s41, 0x8000\n"
"s_waitcnt lgkmcnt(5)\n"
"v_mfma_f32_32x32x16_bf16 v[0:15], a[220:223], v[60:63], v[0:15]\n"
"ds_read_b128 v[52:55], v241 offset:29696\n"
"s_waitcnt lgkmcnt(5)\n"
"v_mfma_f32_32x32x16_bf16 v[0:15], a[224:227], v[32:35], v[0:15]\n"
"ds_read_b128 v[56:59], v241 offset:30720\n"
"s_add_u32 s49, s49, 0x8000\n"
"s_cmp_ge_u32 s49, 163840\n"
"s_cselect_b32 s3, 163840, 0\n"
"s_sub_u32 s49, s49, s3\n"
"s_waitcnt lgkmcnt(5)\n"
"v_mfma_f32_32x32x16_bf16 v[0:15], a[228:231], v[36:39], v[0:15]\n"
"ds_read_b128 v[60:63], v241 offset:31744\n"
"s_waitcnt vmcnt(20)\n"
"s_barrier\n"
"v_add_u32 v241, s49, v240\n"
"s_waitcnt lgkmcnt(5)\n"
"v_mfma_f32_32x32x16_bf16 v[0:15], a[232:235], v[40:43], v[0:15]\n"
"ds_read_b128 v[32:35], v241 offset:0\n"
"s_waitcnt lgkmcnt(5)\n"
"v_mfma_f32_32x32x16_bf16 v[0:15], a[236:239], v[44:47], v[0:15]\n"
"ds_read_b128 v[36:39], v241 offset:1024\n"
"s_waitcnt lgkmcnt(5)\n"
"v_mfma_f32_32x32x16_bf16 v[0:15], a[240:243], v[48:51], v[0:15]\n"
"ds_read_b128 v[40:43], v241 offset:2048\n"
"s_waitcnt lgkmcnt(5)\n"
"v_mfma_f32_32x32x16_bf16 v[0:15], a[244:247], v[52:55], v[0:15]\n"
"ds_read_b128 v[44:47], v241 offset:3072\n"
"s_waitcnt lgkmcnt(5)\n"
"v_mfma_f32_32x32x16_bf16 v[0:15], a[248:251], v[56:59], v[0:15]\n"
"ds_read_b128 v[48:51], v241 offset:4096\n"
"s_waitcnt lgkmcnt(5)\n"
"v_mfma_f32_32x32x16_bf16 v[0:15], a[252:255], v[60:63], v[0:15]\n"
"ds_read_b128 v[52:55], v241 offset:5120\n"
"s_sub_u32 s48, s48, 1\n"
"s_cmp_eq_u32 s48, 0\n"
"s_cbranch_scc1 L_oe_fin0\n"
"s_waitcnt lgkmcnt(5)\n"
"v_mfma_f32_32x32x16_bf16 v[16:31], a[0:3], v[32:35], 0\n"
"ds_read_b128 v[56:59], v241 offset:6144\n"
"s_waitcnt lgkmcnt(5)\n"
"v_mfma_f32_32x32x16_bf16 v[16:31], a[4:7], v[36:39], v[16:31]\n"
"ds_read_b128 v[60:63], v241 offset:7168\n"
"s_waitcnt lgkmcnt(5)\n"
"v_mfma_f32_32x32x16_bf16 v[16:31], a[8:11], v[40:43], v[16:31]\n"
"ds_read_b128 v[32:35], v241 offset:8192\n"
"s_waitcnt lgkmcnt(5)\n"
"v_mfma_f32_32x32x16_bf16 v[16:31], a[12:15], v[44:47], v[16:31]\n"
"ds_read_b128 v[36:39], v241 offset:9216\n"
"s_waitcnt lgkmcnt(5)\n"
"v_mfma_f32_32x32x16_bf16 v[16:31], a[16:19], v[48:51], v[16:31]\n"
"ds_read_b128 v[40:43], v241 offset:10240\n"
"s_waitcnt lgkmcnt(5)\n"
"v_mfma_f32_32x32x16_bf16 v[16:31], a[20:23], v[52:55], v[16:31]\n"
"ds_read_b128 v[44:47], v241 offset:11264\n"
"s_waitcnt lgkmcnt(5)\n"
"v_mfma_f32_32x32x16_bf16 v[16:31], a[24:27], v[56:59], v[16:31]\n"
"ds_read_b128 v[48:51], v241 offset:12288\n"
"s_waitcnt lgkmcnt(5)\n"
"v_mfma_f32_32x32x16_bf16 v[16:31], a[28:31], v[60:63], v[16:31]\n"
"ds_read_b128 v[52:55], v241 offset:13312\n"
"s_waitcnt lgkmcnt(5)\n"
"v_mfma_f32_32x32x16_bf16 v[16:31], a[32:35], v[32:35], v[16:31]\n"
"ds_read_b128 v[56:59], v241 offset:14336\n"
"buffer_store_dwordx4 v[0:3], v240, s[28:31], s44 offen offset:0 sc1\n"
"s_waitcnt lgkmcnt(5)\n"
"v_mfma_f32_32x32x16_bf16 v[16:31], a[36:39], v[36:39], v[16:31]\n"
"ds_read_b128 v[60:63], v241 offset:15360\n"
"buffer_store_dwordx4 v[4:7], v240, s[28:31], s44 offen offset:1024 sc1\n"
"s_waitcnt lgkmcnt(5)\n"
"v_mfma_f32_32x32x16_bf16 v[16:31], a[40:43], v[40:43], v[16:31]\n"
"ds_read_b128 v[32:35], v241 offset:16384\n"
"buffer_store_dwordx4 v[8:11], v240, s[28:31], s44 offen offset:2048 sc1\n"
"s_waitcnt lgkmcnt(5)\n"
"v_mfma_f32_32x32x16_bf16 v[16:31], a[44:47], v[44:47], v[16:31]\n"
"ds_read_b128 v[36:39], v241 offset:17408\n"
"buffer_store_dwordx4 v[12:15], v240, s[28:31], s44 offen offset:3072 sc1\n"
"s_waitcnt lgkmcnt(5)\n"
"v_mfma_f32_32x32x16_bf16 v[16:31], a[48:51], v[48:51], v[16:31]\n"
"ds_read_b128 v[40:43], v241 offset:18432\n"
"s_lshl_b32 s44, s33, 18\n"
"s_add_u32 s33, s33, s50\n"
"s_waitcnt lgkmcnt(5)\n"
"v_mfma_f32_32x32x16_bf16 v[16:31], a[52:55], v[52:55], v[16:31]\n"
"ds_read_b128 v[44:47], v241 offset:19456\n"
"s_add_u32 m0, s42, 0\n"
"s_add_u32 s37, s41, 0\n"
"buffer_load_dwordx4 v240, s[20:23], s37 offen offset:0 sc1 lds\n"
"s_waitcnt lgkmcnt(5)\n"
"v_mfma_f32_32x32x16_bf16 v[16:31], a[56:59], v[56:59], v[16:31]\n"
"ds_read_b128 v[48:51], v241 offset:20480\n"
"buffer_load_dwordx4 v240, s[20:23], s37 offen offset:1024 sc1 lds\n"
"s_waitcnt lgkmcnt(5)\n"
"v_mfma_f32_32x32x16_bf16 v[16:31], a[60:63], v[60:63], v[16:31]\n"
"ds_read_b128 v[52:55], v241 offset:21504\n"
"buffer_load_dwordx4 v240, s[20:23], s37 offen offset:2048 sc1 lds\n"
"s_waitcnt lgkmcnt(5)\n"
"v_mfma_f32_32x32x16_bf16 v[16:31], a[64:67], v[32:35], v[16:31]\n"
"ds_read_b128 v[56:59], v241 offset:22528\n"
"buffer_load_dwordx4 v240, s[20:23], s37 offen offset:3072 sc1 lds\n"
"s_waitcnt lgkmcnt(5)\n"
"v_mfma_f32_32x32x16_bf16 v[16:31], a[68:71], v[36:39], v[16:31]\n"
"ds_read_b128 v[60:63], v241 offset:23552\n"
"s_add_u32 m0, s42, 4096\n"
"s_add_u32 s37, s41, 4096\n"
"buffer_load_dwordx4 v240, s[20:23], s37 offen offset:0 sc1 lds\n"
"s_waitcnt lgkmcnt(5)\n"
"v_mfma_f32_32x32x16_bf16 v[16:31], a[72:75], v[40:43], v[16:31]\n"
"ds_read_b128 v[32:35], v241 offset:24576\n"
"buffer_load_dwordx4 v240, s[20:23], s37 offen offset:1024 sc1 lds\n"
"s_waitcnt lgkmcnt(5)\n"
"v_mfma_f32_32x32x16_bf16 v[16:31], a[76:79], v[44:47], v[16:31]\n"
"ds_read_b128 v[36:39], v241 offset:25600\n"
"buffer_load_dwordx4 v240, s[20:23], s37 offen offset:2048 sc1 lds\n"
"s_waitcnt lgkmcnt(5)\n"
"v_mfma_f32_32x32x16_bf16 v[16:31], a[80:83], v[48:51], v[16:31]\n"
"ds_read_b128 v[40:43], v241 offset:26624\n"
"buffer_load_dwordx4 v240, s[20:23], s37 offen offset:3072 sc1 lds\n"
"s_waitcnt lgkmcnt(5)\n"
"v_mfma_f32_32x32x16_bf16 v[16:31], a[84:87], v[52:55], v[16:31]\n"
"ds_read_b128 v[44:47], v241 offset:27648\n"
"s_add_u32 s42, s42, 0x8000\n"
"s_cmp_ge_u32 s42, 163840\n"
"s_cselect_b32 s3, 163840, 0\n"
"s_sub_u32 s42, s42, s3\n"
"s_waitcnt lgkmcnt(5)\n"
"v_mfma_f32_32x32x16_bf16 v[16:31], a[88:91], v[56:59], v[16:31]\n"
"ds_read_b128 v[48:51], v241 offset:28672\n"
"s_add_u32 s41, s41, s34\n"
"s_sub_u32 s43, s43, 1\n"
"s_cmp_gt_i32 s43, 0\n"
"s_cselect_b32 s41, s41, 0x7fff0000\n"
"s_waitcnt lgkmcnt(5)\n"
"v_mfma_f32_32x32x16_bf16 v[16:31], a[92:95], v[60:63], v[16:31]\n"
"ds_read_b128 v[52:55], v241 offset:29696\n"
"s_waitcnt lgkmcnt(5)\n"
"v_mfma_f32_32x32x16_bf16 v[16:31], a[96:99], v[32:35], v[16:31]\n"
"ds_read_b128 v[56:59], v241 offset:30720\n"
"s_add_u32 s49, s49, 0x8000\n"
"s_cmp_ge_u32 s49, 163840\n"
"s_cselect_b32 s3, 163840, 0\n"
"s_sub_u32 s49, s49, s3\n"
"s_waitcnt lgkmcnt(5)\n"
"v_mfma_f32_32x32x16_bf16 v[16:31], a[100:103], v[36:39], v[16:31]\n"
"ds_read_b128 v[60:63], v241 offset:31744\n"
"s_waitcnt vmcnt(20)\n"
"s_barrier\n"
"v_add_u32 v241, s49, v240\n"
"s_waitcnt lgkmcnt(5)\n"
"v_mfma_f32_32x32x16_bf16 v[16:31], a[104:107], v[40:43], v[16:31]\n"
"ds_read_b128 v[32:35], v241 offset:0\n"
"s_waitcnt lgkmcnt(5)\n"
"v_mfma_f32_32x32x16_bf16 v[16:31], a[108:111], v[44:47], v[16:31]\n"
"ds_read_b128 v[36:39], v241 offset:1024\n"
"s_waitcnt lgkmcnt(5)\n"
"v_mfma_f32_32x32x16_bf16 v[16:31], a[112:115], v[48:51], v[16:31]\n"
"ds_read_b128 v[40:43], v241 offset:2048\n"
"s_waitcnt lgkmcnt(5)\n"
"v_mfma_f32_32x32x16_bf16 v[16:31], a[116:119], v[52:55], v[16:31]\n"
"ds_read_b128 v[44:47], v241 offset:3072\n"
"s_waitcnt lgkmcnt(5)\n"
"v_mfma_f32_32x32x16_bf16 v[16:31], a[120:123], v[56:59], v[16:31]\n"
"ds_read_b128 v[48:51], v241 offset:4096\n"
"s_waitcnt lgkmcnt(5)\n"
"v_mfma_f32_32x32x16_bf16 v[16:31], a[124:127], v[60:63], v[16:31]\n"
"ds_read_b128 v[52:55], v241 offset:5120\n"
"s_waitcnt lgkmcnt(5)\n"
"v_mfma_f32_32x32x16_bf16 v[16:31], a[128:131], v[32:35], v[16:31]\n"
"ds_read_b128 v[56:59], v241 offset:6144\n"
"s_waitcnt lgkmcnt(5)\n"
"v_mfma_f32_32x32x16_bf16 v[16:31], a[132:135], v[36:39], v[16:31]\n"
"ds_read_b128 v[60:63], v241 offset:7168\n"
"s_waitcnt lgkmcnt(5)\n"
"v_mfma_f32_32x32x16_bf16 v[16:31], a[136:139], v[40:43], v[16:31]\n"
"ds_read_b128 v[32:35], v241 offset:8192\n"
"s_waitcnt lgkmcnt(5)\n"
"v_mfma_f32_32x32x16_bf16 v[16:31], a[140:143], v[44:47], v[16:31]\n"
"ds_read_b128 v[36:39], v241 offset:9216\n"
"s_waitcnt lgkmcnt(5)\n"
"v_mfma_f32_32x32x16_bf16 v[16:31], a[144:147], v[48:51], v[16:31]\n"
"ds_read_b128 v[40:43], v241 offset:10240\n"
"s_waitcnt lgkmcnt(5)\n"
"v_mfma_f32_32x32x16_bf16 v[16:31], a[148:151], v[52:55], v[16:31]\n"
"ds_read_b128 v[44:47], v241 offset:11264\n"
"s_waitcnt lgkmcnt(5)\n"
"v_mfma_f32_32x32x16_bf16 v[16:31], a[152:155], v[56:59], v[16:31]\n"
"ds_read_b128 v[48:51], v241 offset:12288\n"
"s_waitcnt lgkmcnt(5)\n"
"v_mfma_f32_32x32x16_bf16 v[16:31], a[156:159], v[60:63], v[16:31]\n"
"ds_read_b128 v[52:55], v241 offset:13312\n"
"s_waitcnt lgkmcnt(5)\n"
"v_mfma_f32_32x32x16_bf16 v[16:31], a[160:163], v[32:35], v[16:31]\n"
"ds_read_b128 v[56:59], v241 offset:14336\n"
"s_waitcnt lgkmcnt(5)\n"
"v_mfma_f32_32x32x16_bf16 v[16:31], a[164:167], v[36:39], v[16:31]\n"
"ds_read_b128 v[60:63], v241 offset:15360\n"
"s_waitcnt lgkmcnt(5)\n"
"v_mfma_f32_32x32x16_bf16 v[16:31], a[168:171], v[40:43], v[16:31]\n"
"ds_read_b128 v[32:35], v241 offset:16384\n"
"s_waitcnt lgkmcnt(5)\n"
"v_mfma_f32_32x32x16_bf16 v[16:31], a[172:175], v[44:47], v[16:31]\n"
"ds_read_b128 v[36:39], v241 offset:17408\n"
"s_waitcnt lgkmcnt(5)\n"
"v_mfma_f32_32x32x16_bf16 v[16:31], a[176:179], v[48:51], v[16:31]\n"
"ds_read_b128 v[40:43], v241 offset:18432\n"
"s_waitcnt lgkmcnt(5)\n"
"v_mfma_f32_32x32x16_bf16 v[16:31], a[180:183], v[52:55], v[16:31]\n"
"ds_read_b128 v[44:47], v241 offset:19456\n"
"s_add_u32 m0, s42, 0\n"
"s_add_u32 s37, s41, 0\n"
"buffer_load_dwordx4 v240, s[20:23], s37 offen offset:0 sc1 lds\n"
"s_waitcnt lgkmcnt(5)\n"
"v_mfma_f32_32x32x16_bf16 v[16:31], a[184:187], v[56:59], v[16:31]\n"
"ds_read_b128 v[48:51], v241 offset:20480\n"
"buffer_load_dwordx4 v240, s[20:23], s37 offen offset:1024 sc1 lds\n"
"s_waitcnt lgkmcnt(5)\n"
"v_mfma_f32_32x32x16_bf16 v[16:31], a[188:191], v[60:63], v[16:31]\n"
"ds_read_b128 v[52:55], v241 offset:21504\n"
"buffer_load_dwordx4 v240, s[20:23], s37 offen offset:2048 sc1 lds\n"
"s_waitcnt lgkmcnt(5)\n"
"v_mfma_f32_32x32x16_bf16 v[16:31], a[192:195], v[32:35], v[16:31]\n"
"ds_read_b128 v[56:59], v241 offset:22528\n"
"buffer_load_dwordx4 v240, s[20:23], s37 offen offset:3072 sc1 lds\n"
"s_waitcnt lgkmcnt(5)\n"
"v_mfma_f32_32x32x16_bf16 v[16:31], a[196:199], v[36:39], v[16:31]\n"
"ds_read_b128 v[60:63], v241 offset:23552\n"
"s_add_u32 m0, s42, 4096\n"
"s_add_u32 s37, s41, 4096\n"
"buffer_load_dwordx4 v240, s[20:23], s37 offen offset:0 sc1 lds\n"
"s_waitcnt lgkmcnt(5)\n"
"v_mfma_f32_32x32x16_bf16 v[16:31], a[200:203], v[40:43], v[16:31]\n"
"ds_read_b128 v[32:35], v241 offset:24576\n"
"buffer_load_dwordx4 v240, s[20:23], s37 offen offset:1024 sc1 lds\n"
"s_waitcnt lgkmcnt(5)\n"
"v_mfma_f32_32x32x16_bf16 v[16:31], a[204:207], v[44:47], v[16:31]\n"
"ds_read_b128 v[36:39], v241 offset:25600\n"
"buffer_load_dwordx4 v240, s[20:23], s37 offen offset:2048 sc1 lds\n"
"s_waitcnt lgkmcnt(5)\n"
"v_mfma_f32_32x32x16_bf16 v[16:31], a[208:211], v[48:51], v[16:31]\n"
"ds_read_b128 v[40:43], v241 offset:26624\n"
"buffer_load_dwordx4 v240, s[20:23], s37 offen offset:3072 sc1 lds\n"
"s_waitcnt lgkmcnt(5)\n"
"v_mfma_f32_32x32x16_bf16 v[16:31], a[212:215], v[52:55], v[16:31]\n"
"ds_read_b128 v[44:47], v241 offset:27648\n"
"s_add_u32 s42, s42, 0x8000\n"
"s_cmp_ge_u32 s42, 163840\n"
"s_cselect_b32 s3, 163840, 0\n"
"s_sub_u32 s42, s42, s3\n"
"s_waitcnt lgkmcnt(5)\n"
"v_mfma_f32_32x32x16_bf16 v[16:31], a[216:219], v[56:59], v[16:31]\n"
"ds_read_b128 v[48:51], v241 offset:28672\n"
"s_add_u32 s41, s41, 0x8000\n"
"s_waitcnt lgkmcnt(5)\n"
"v_mfma_f32_32x32x16_bf16 v[16:31], a[220:223], v[60:63], v[16:31]\n"
"ds_read_b128 v[52:55], v241 offset:29696\n"
"s_waitcnt lgkmcnt(5)\n"
"v_mfma_f32_32x32x16_bf16 v[16:31], a[224:227], v[32:35], v[16:31]\n"
"ds_read_b128 v[56:59], v241 offset:30720\n"
"s_add_u32 s49, s49, 0x8000\n"
"s_cmp_ge_u32 s49, 163840\n"
"s_cselect_b32 s3, 163840, 0\n"
"s_sub_u32 s49, s49, s3\n"
"s_waitcnt lgkmcnt(5)\n"
"v_mfma_f32_32x32x16_bf16 v[16:31], a[228:231], v[36:39], v[16:31]\n"
"ds_read_b128 v[60:63], v241 offset:31744\n"
"s_waitcnt vmcnt(20)\n"
"s_barrier\n"
"v_add_u32 v241, s49, v240\n"
"s_waitcnt lgkmcnt(5)\n"
"v_mfma_f32_32x32x16_bf16 v[16:31], a[232:235], v[40:43], v[16:31]\n"
"ds_read_b128 v[32:35], v241 offset:0\n"
"s_waitcnt lgkmcnt(5)\n"
"v_mfma_f32_32x32x16_bf16 v[16:31], a[236:239], v[44:47], v[16:31]\n"
"ds_read_b128 v[36:39], v241 offset:1024\n"
"s_waitcnt lgkmcnt(5)\n"
"v_mfma_f32_32x32x16_bf16 v[16:31], a[240:243], v[48:51], v[16:31]\n"
"ds_read_b128 v[40:43], v241 offset:2048\n"
"s_waitcnt lgkmcnt(5)\n"
"v_mfma_f32_32x32x16_bf16 v[16:31], a[244:247], v[52:55], v[16:31]\n"
"ds_read_b128 v[44:47], v241 offset:3072\n"
"s_waitcnt lgkmcnt(5)\n"
"v_mfma_f32_32x32x16_bf16 v[16:31], a[248:251], v[56:59], v[16:31]\n"
"ds_read_b128 v[48:51], v241 offset:4096\n"
"s_waitcnt lgkmcnt(5)\n"
"v_mfma_f32_32x32x16_bf16 v[16:31], a[252:255], v[60:63], v[16:31]\n"
"ds_read_b128 v[52:55], v241 offset:5120\n"
"s_sub_u32 s48, s48, 1\n"
"s_cmp_eq_u32 s48, 0\n"
"s_cbranch_scc1 L_oe_fin1\n"
"s_branch L_oe_loop\n"
"L_oe_fin0:\n"
"s_nop 7\n"
"s_nop 3\n"
"v_mov_b64 v[16:17], v[0:1]\n"
"v_mov_b64 v[18:19], v[2:3]\n"
"v_mov_b64 v[20:21], v[4:5]\n"
"v_mov_b64 v[22:23], v[6:7]\n"
"v_mov_b64 v[24:25], v[8:9]\n"
"v_mov_b64 v[26:27], v[10:11]\n"
"v_mov_b64 v[28:29], v[12:13]\n"
"v_mov_b64 v[30:31], v[14:15]\n"
"L_oe_fin1:\n"
"s_nop 7\n"
"s_nop 3\n"
"buffer_store_dwordx4 v[16:19], v240, s[28:31], s44 offen offset:0 sc1\n"
"buffer_store_dwordx4 v[20:23], v240, s[28:31], s44 offen offset:1024 sc1\n"
"buffer_store_dwordx4 v[24:27], v240, s[28:31], s44 offen offset:2048 sc1\n"
"buffer_store_dwordx4 v[28:31], v240, s[28:31], s44 offen offset:3072 sc1\n"
"s_add_u32 s45, s45, 1\n"
"v_mov_b32 v243, s45\n"
"s_waitcnt vmcnt(0) lgkmcnt(0)\n"
"s_barrier\n"
"s_mov_b64 exec, 1\n"
"buffer_store_dword v243, off, s[12:15], s51 sc1\n"
"s_mov_b64 exec, -1\n"
"Lw_13:\n"
"buffer_load_dword v244, v245, s[12:15], s68 offen sc1\n"
"s_waitcnt vmcnt(0)\n"
"v_cmp_lt_i32 vcc, v244, v243\n"
"s_cbranch_vccz Lwok_14\n"
"s_sub_u32 s8, s8, 1\n"
"s_cbranch_scc1 L_bulk_fail\n"
"s_sleep 4\n"
"s_branch Lw_13\n"
"Lwok_14:\n"
"s_lshr_b32 s43, s5, 1\n"
"s_mul_i32 s3, s43, s50\n"
"s_add_u32 s33, s47, s3\n"
"L_oe_sum:\n"
"s_cmp_ge_u32 s43, s35\n"
"s_cbranch_scc1 L_oe_sumd\n"
"s_lshl_b32 s44, s33, 18\n"
"s_add_u32 s37, s44, 0\n"
"buffer_load_dwordx4 v[64:67], v246, s[60:63], s37 offen offset:0 sc1\n"
"buffer_load_dwordx4 v[68:71], v246, s[60:63], s37 offen offset:512 sc1\n"
"s_add_u32 s37, s44, 4096\n"
"buffer_load_dwordx4 v[72:75], v246, s[60:63], s37 offen offset:0 sc1\n"
"buffer_load_dwordx4 v[76:79], v246, s[60:63], s37 offen offset:512 sc1\n"
"s_add_u32 s37, s44, 8192\n"
"buffer_load_dwordx4 v[80:83], v246, s[60:63], s37 offen offset:0 sc1\n"
"buffer_load_dwordx4 v[84:87], v246, s[60:63], s37 offen offset:512 sc1\n"
"s_add_u32 s37, s44, 12288\n"
"buffer_load_dwordx4 v[88:91], v246, s[60:63], s37 offen offset:0 sc1\n"
"buffer_load_dwordx4 v[92:95], v246, s[60:63], s37 offen offset:512 sc1\n"
"s_lshl_b32 s44, s33, 16\n"
"s_waitcnt vmcnt(4)\n"
"v_pk_add_f32 v[64:65], v[64:65], v[72:73]\n"
"v_pk_add_f32 v[66:67], v[66:67], v[74:75]\n"
"v_pk_add_f32 v[68:69], v[68:69], v[76:77]\n"
"v_pk_add_f32 v[70:71], v[70:71], v[78:79]\n"
"s_waitcnt vmcnt(0)\n"
"v_pk_add_f32 v[80:81], v[80:81], v[88:89]\n"
"v_pk_add_f32 v[82:83], v[82:83], v[90:91]\n"
"v_pk_add_f32 v[84:85], v[84:85], v[92:93]\n"
"v_pk_add_f32 v[86:87], v[86:87], v[94:95]\n"
"v_pk_add_f32 v[64:65], v[64:65], v[80:81]\n"
"v_pk_add_f32 v[66:67], v[66:67], v[82:83]\n"
"v_pk_add_f32 v[68:69], v[68:69], v[84:85]\n"
"v_pk_add_f32 v[70:71], v[70:71], v[86:87]\n"
"v_cvt_pk_bf16_f32 v96, v64, v65\n"
"v_cvt_pk_bf16_f32 v97, v66, v67\n"
"v_cvt_pk_bf16_f32 v98, v68, v69\n"
"v_cvt_pk_bf16_f32 v99, v70, v71\n"
"buffer_store_dwordx4 v[96:99], v247, s[24:27], s44 offen\n"
"s_add_u32 s43, s43, 2\n"
"s_lshl_b32 s3, s50, 1\n"
"s_add_u32 s33, s33, s3\n"
"s_branch L_oe_sum\n"
"L_oe_sumd:\n"
"s_mul_i32 s3, s35, s50\n"
"s_add_u32 s47, s47, s3\n"
"s_sub_u32 s36, s36, 1\n"
"s_cmp_lg_u32 s36, 0\n"
"s_cbranch_scc1 L_oe_batch\n"
"s_branch L_next\n"
"L_ax:\n"
"s_add_u32 s35, s65, s5\n"
"s_mov_b32 s33, s66\n"
"s_mov_b32 s34, s68\n"
"s_lshl_b32 s48, s35, 19\n"
"s_add_u32 s16, s10, 0x0\n"
"s_addc_u32 s17, s11, 0\n"
"s_add_u32 s16, s16, s48\n"
"s_addc_u32 s17, s17, 0\n"
"s_and_b32 s17, s17, 0xffff\n"
"s_mov_b32 s18, 0x80000\n"
"s_mov_b32 s19, 0x20000\n"
"s_add_u32 s20, s10, 0x1000000\n"
"s_addc_u32 s21, s11, 0\n"
"s_add_u32 s20, s20, s48\n"
"s_addc_u32 s21, s21, 0\n"
"s_and_b32 s21, s21, 0xffff\n"
"s_mov_b32 s22, 0x80000\n"
"s_mov_b32 s23, 0x20000\n"
"s_add_u32 s24, s10, 0x2000000\n"
"s_addc_u32 s25, s11, 0\n"
"s_add_u32 s24, s24, s48\n"
"s_addc_u32 s25, s25, 0\n"
"s_and_b32 s25, s25, 0xffff\n"
"s_mov_b32 s26, 0x80000\n"
"s_mov_b32 s27, 0x20000\n"
"s_lshl_b32 s48, s35, 12\n"
"s_add_u32 s28, s10, 0x8000000\n"
"s_addc_u32 s29, s11, 0\n"
"s_add_u32 s28, s28, s48\n"
"s_addc_u32 s29, s29, 0\n"
"s_and_b32 s29, s29, 0xffff\n"
"s_mov_b32 s30, 0x800000\n"
"s_mov_b32 s31, 0x20000\n"
"s_sub_u32 s30, s30, s48\n"
"s_lshl_b32 s48, s35, 13\n"
"s_add_u32 s36, s10, 0x6000000\n"
"s_addc_u32 s37, s11, 0\n"
"s_add_u32 s36, s36, s48\n"
"s_addc_u32 s37, s37, 0\n"
"s_and_b32 s37, s37, 0xffff\n"
"s_mov_b32 s38, 0x1000000\n"
"s_mov_b32 s39, 0x20000\n"
"s_sub_u32 s38, s38, s48\n"
"v_lshlrev_b32 v212, 4, v255\n"
"v_mov_b32 v213, 0x0\n"
"v_bfe_i32 v221, v255, 1, 1\n"
"v_and_b32 v221, 0x20, v221\n"
"v_add_u32 v213, v213, v221\n"
"v_bfe_i32 v221, v255, 2, 1\n"
"v_and_b32 v221, 0x40, v221\n"
"v_add_u32 v213, v213, v221\n"
"v_bfe_i32 v221, v255, 5, 1\n"
"v_and_b32 v221, 0x80, v221\n"
"v_add_u32 v213, v213, v221\n"
"v_mov_b32 v214, 0x800\n"
"v_bfe_i32 v221, v255, 0, 1\n"
"v_and_b32 v221, 0x20, v221\n"
"v_add_u32 v214, v214, v221\n"
"v_bfe_i32 v221, v255, 1, 1\n"
"v_and_b32 v221, 0x40, v221\n"
"v_add_u32 v214, v214, v221\n"
"v_bfe_i32 v221, v255, 2, 1\n"
"v_and_b32 v221, 0x80, v221\n"
"v_add_u32 v214, v214, v221\n"
"v_bfe_i32 v221, v255, 3, 1\n"
"v_and_b32 v221, 0x100, v221\n"
"v_add_u32 v214, v214, v221\n"
"v_bfe_i32 v221, v255, 5, 1\n"
"v_and_b32 v221, 0x10, v221\n"
"v_add_u32 v214, v214, v221\n"
"s_mov_b32 s44, 0xaaaaaaaa\n"
"s_mov_b32 s45, 0xaaaaaaaa\n"
"s_mov_b32 s46, 0xffff0000\n"
"s_mov_b32 s47, 0xffff0000\n"
"v_and_b32 v220, 31, v255\n"
"v_lshrrev_b32 v221, 5, v255\n"
"v_lshlrev_b32 v221, 2, v221\n"
"v_sub_u32 v220, v220, v221\n"
"v_cmp_le_i32 s[56:57], 0, v220\n"
"v_cmp_le_i32 s[58:59], 1, v220\n"
"v_cmp_le_i32 s[60:61], 2, v220\n"
"v_cmp_le_i32 s[62:63], 3, v220\n"
"v_cmp_le_i32 s[72:73], 8, v220\n"
"v_cmp_le_i32 s[74:75], 9, v220\n"
"v_cmp_le_i32 s[80:81], 10, v220\n"
"v_cmp_le_i32 s[82:83], 11, v220\n"
"v_cmp_le_i32 s[84:85], 16, v220\n"
"v_cmp_le_i32 s[86:87], 17, v220\n"
"v_cmp_le_i32 s[88:89], 18, v220\n"
"v_cmp_le_i32 s[90:91], 19, v220\n"
"v_cmp_le_i32 s[92:93], 24, v220\n"
"v_cmp_le_i32 s[94:95], 25, v220\n"
"v_cmp_le_i32 s[96:97], 26, v220\n"
"v_cmp_le_i32 s[98:99], 27, v220\n"
"s_lshr_b32 s48, s35, 1\n"
"s_lshl_b32 s49, s48, 7\n"
"s_lshl_b32 s48, s48, 9\n"
"s_add_u32 s48, s48, 0x2800\n"
"v_lshlrev_b32 v219, 2, v255\n"
"v_add_u32 v219, s49, v219\n"
"v_mov_b32 v221, s48\n"
"v_cmp_gt_u32 vcc, 2, v255\n"
"s_nop 1\n"
"v_cndmask_b32 v219, v221, v219, vcc\n"
"v_mov_b32 v216, -1\n"
"v_mov_b32 v221, 0\n"
"v_cndmask_b32 v216, v216, v221, vcc\n"
"v_and_b32 v217, 3, v255\n"
"v_lshlrev_b32 v217, 2, v217\n"
"s_lshl_b32 s49, s35, 7\n"
"s_add_u32 s50, s49, 0x1000\n"
"s_lshl_b32 s49, s35, 6\n"
"s_add_u32 s42, s49, 0x4800\n"
"v_mov_b32 v210, 0\n"
"v_mov_b32 v211, 0\n"
"s_mov_b32 s51, 0\n"
"L_ax_seg:\n"
"s_lshr_b32 s43, s33, s70\n"
"s_lshl_b32 s3, s43, 7\n"
"v_and_b32 v215, s3, v216\n"
"v_add_u32 v215, v215, v219\n"
"s_lshl_b32 s3, s43, s70\n"
"v_and_b32 v222, s3, v216\n"
"s_add_u32 s49, s33, 1\n"
"v_sub_u32 v222, s49, v222\n"
"s_lshl_b32 s40, s33, 13\n"
"Lw_15:\n"
"buffer_load_dword v221, v215, s[12:15], s51 offen sc1\n"
"s_waitcnt vmcnt(0)\n"
"v_cmp_lt_i32 vcc, v221, v222\n"
"s_cbranch_vccz Lwok_16\n"
"s_sub_u32 s8, s8, 1\n"
"s_cbranch_scc1 L_bulk_fail\n"
"s_sleep 4\n"
"s_branch Lw_15\n"
"Lwok_16:\n"
"s_add_u32 s41, s40, 0x1000\n"
"buffer_load_dwordx4 a[32:35], v212, s[20:23], s40 offen offset:0 sc1\n"
"buffer_load_dwordx4 a[36:39], v212, s[20:23], s40 offen offset:1024 sc1\n"
"buffer_load_dwordx4 a[40:43], v212, s[20:23], s40 offen offset:2048 sc1\n"
"buffer_load_dwordx4 a[44:47], v212, s[20:23], s40 offen offset:3072 sc1\n"
"buffer_load_dwordx4 a[48:51], v212, s[20:23], s41 offen offset:0 sc1\n"
"buffer_load_dwordx4 a[52:55], v212, s[20:23], s41 offen offset:1024 sc1\n"
"buffer_load_dwordx4 a[56:59], v212, s[20:23], s41 offen offset:2048 sc1\n"
"buffer_load_dwordx4 a[60:63], v212, s[20:23], s41 offen offset:3072 sc1\n"
"buffer_load_dwordx4 a[0:3], v212, s[16:19], s40 offen offset:0 sc1\n"
"buffer_load_dwordx4 a[4:7], v212, s[16:19], s40 offen offset:1024 sc1\n"
"buffer_load_dwordx4 a[8:11], v212, s[16:19], s40 offen offset:2048 sc1\n"
"buffer_load_dwordx4 a[12:15], v212, s[16:19], s40 offen offset:3072 sc1\n"
"buffer_load_dwordx4 a[16:19], v212, s[16:19], s41 offen offset:0 sc1\n"
"buffer_load_dwordx4 a[20:23], v212, s[16:19], s41 offen offset:1024 sc1\n"
"buffer_load_dwordx4 a[24:27], v212, s[16:19], s41 offen offset:2048 sc1\n"
"buffer_load_dwordx4 a[28:31], v212, s[16:19], s41 offen offset:3072 sc1\n"
"buffer_load_dwordx4 a[64:67], v212, s[24:27], s40 offen offset:0 sc1\n"
"buffer_load_dwordx4 a[68:71], v212, s[24:27], s40 offen offset:1024 sc1\n"
"buffer_load_dwordx4 a[72:75], v212, s[24:27], s40 offen offset:2048 sc1\n"
"buffer_load_dwordx4 a[76:79], v212, s[24:27], s40 offen offset:3072 sc1\n"
"buffer_load_dwordx4 a[80:83], v212, s[24:27], s41 offen offset:0 sc1\n"
"buffer_load_dwordx4 a[84:87], v212, s[24:27], s41 offen offset:1024 sc1\n"
"buffer_load_dwordx4 a[88:91], v212, s[24:27], s41 offen offset:2048 sc1\n"
"buffer_load_dwordx4 a[92:95], v212, s[24:27], s41 offen offset:3072 sc1\n"
"s_add_u32 s48, s33, 1\n"
"v_mov_b32 v218, s48\n"
"s_lshl_b32 s40, s33, 17\n"
"Lw_17:\n"
"buffer_load_dword v221, v217, s[12:15], s50 offen sc1\n"
"s_waitcnt vmcnt(0)\n"
"v_cmp_lt_i32 vcc, v221, v218\n"
"s_cbranch_vccz Lwok_18\n"
"s_sub_u32 s8, s8, 1\n"
"s_cbranch_scc1 L_bulk_fail\n"
"s_sleep 4\n"
"s_branch Lw_17\n"
"Lwok_18:\n"
"buffer_load_dwordx4 v[0:3], v213, s[28:31], s40 offen offset:0 sc1\n"
"buffer_load_dwordx4 v[4:7], v213, s[28:31], s40 offen offset:16 sc1\n"
"buffer_load_dwordx4 v[8:11], v213, s[28:31], s40 offen offset:256 sc1\n"
"buffer_load_dwordx4 v[12:15], v213, s[28:31], s40 offen offset:272 sc1\n"
"buffer_load_dwordx4 v[16:19], v213, s[28:31], s40 offen offset:512 sc1\n"
"buffer_load_dwordx4 v[20:23], v213, s[28:31], s40 offen offset:528 sc1\n"
"buffer_load_dwordx4 v[24:27], v213, s[28:31], s40 offen offset:768 sc1\n"
"buffer_load_dwordx4 v[28:31], v213, s[28:31], s40 offen offset:784 sc1\n"
"buffer_load_dwordx4 v[32:35], v213, s[28:31], s40 offen offset:1024 sc1\n"
"buffer_load_dwordx4 v[36:39], v213, s[28:31], s40 offen offset:1040 sc1\n"
"buffer_load_dwordx4 v[40:43], v213, s[28:31], s40 offen offset:1280 sc1\n"
"buffer_load_dwordx4 v[44:47], v213, s[28:31], s40 offen offset:1296 sc1\n"
"buffer_load_dwordx4 v[48:51], v213, s[28:31], s40 offen offset:1536 sc1\n"
"buffer_load_dwordx4 v[52:55], v213, s[28:31], s40 offen offset:1552 sc1\n"
"buffer_load_dwordx4 v[56:59], v213, s[28:31], s40 offen offset:1792 sc1\n"
"buffer_load_dwordx4 v[60:63], v213, s[28:31], s40 offen offset:1808 sc1\n"
"buffer_load_dwordx4 v[96:99], v214, s[28:31], s40 offen offset:0 sc1\n"
"buffer_load_dwordx4 v[100:103], v214, s[28:31], s40 offen offset:512 sc1\n"
"buffer_load_dwordx4 v[104:107], v214, s[28:31], s40 offen offset:1024 sc1\n"
"buffer_load_dwordx4 v[108:111], v214, s[28:31], s40 offen offset:1536 sc1\n"
"v_mfma_f32_32x32x16_bf16 v[112:127], a[32:35], a[0:3], 0\n"
"v_mfma_f32_32x32x16_bf16 v[112:127], a[36:39], a[4:7], v[112:127]\n"
"v_mfma_f32_32x32x16_bf16 v[112:127], a[40:43], a[8:11], v[112:127]\n"
"v_mfma_f32_32x32x16_bf16 v[112:127], a[44:47], a[12:15], v[112:127]\n"
"v_mfma_f32_32x32x16_bf16 v[112:127], a[48:51], a[16:19], v[112:127]\n"
"v_mfma_f32_32x32x16_bf16 v[112:127], a[52:55], a[20:23], v[112:127]\n"
"v_mfma_f32_32x32x16_bf16 v[112:127], a[56:59], a[24:27], v[112:127]\n"
"v_mfma_f32_32x32x16_bf16 v[112:127], a[60:63], a[28:31], v[112:127]\n"
"v_mov_b32 v222, 0xff800000\n"
"s_waitcnt vmcnt(18)\n"
"v_cndmask_b32 v64, v0, v1, s[44:45]\n"
"v_cndmask_b32 v65, v2, v3, s[44:45]\n"
"v_cndmask_b32 v66, v4, v5, s[44:45]\n"
"v_cndmask_b32 v67, v6, v7, s[44:45]\n"
"s_waitcnt vmcnt(16)\n"
"v_cndmask_b32 v68, v8, v9, s[44:45]\n"
"v_cndmask_b32 v69, v10, v11, s[44:45]\n"
"v_cndmask_b32 v70, v12, v13, s[44:45]\n"
"v_cndmask_b32 v71, v14, v15, s[44:45]\n"
"v_mfma_f32_32x32x16_bf16 v[128:143], v[64:67], a[0:3], 0\n"
"s_waitcnt vmcnt(14)\n"
"v_cndmask_b32 v72, v16, v17, s[44:45]\n"
"v_cndmask_b32 v73, v18, v19, s[44:45]\n"
"v_cndmask_b32 v74, v20, v21, s[44:45]\n"
"v_cndmask_b32 v75, v22, v23, s[44:45]\n"
"v_mfma_f32_32x32x16_bf16 v[128:143], v[68:71], a[4:7], v[128:143]\n"
"s_waitcnt vmcnt(12)\n"
"v_cndmask_b32 v76, v24, v25, s[44:45]\n"
"v_cndmask_b32 v77, v26, v27, s[44:45]\n"
"v_cndmask_b32 v78, v28, v29, s[44:45]\n"
"v_cndmask_b32 v79, v30, v31, s[44:45]\n"
"v_mfma_f32_32x32x16_bf16 v[128:143], v[72:75], a[8:11], v[128:143]\n"
"s_waitcnt vmcnt(10)\n"
"v_cndmask_b32 v80, v32, v33, s[44:45]\n"
"v_cndmask_b32 v81, v34, v35, s[44:45]\n"
"v_cndmask_b32 v82, v36, v37, s[44:45]\n"
"v_cndmask_b32 v83, v38, v39, s[44:45]\n"
"v_mfma_f32_32x32x16_bf16 v[128:143], v[76:79], a[12:15], v[128:143]\n"
"s_waitcnt vmcnt(8)\n"
"v_cndmask_b32 v84, v40, v41, s[44:45]\n"
"v_cndmask_b32 v85, v42, v43, s[44:45]\n"
"v_cndmask_b32 v86, v44, v45, s[44:45]\n"
"v_cndmask_b32 v87, v46, v47, s[44:45]\n"
"v_mfma_f32_32x32x16_bf16 v[128:143], v[80:83], a[16:19], v[128:143]\n"
"s_waitcnt vmcnt(6)\n"
"v_cndmask_b32 v88, v48, v49, s[44:45]\n"
"v_cndmask_b32 v89, v50, v51, s[44:45]\n"
"v_cndmask_b32 v90, v52, v53, s[44:45]\n"
"v_cndmask_b32 v91, v54, v55, s[44:45]\n"
"v_mfma_f32_32x32x16_bf16 v[128:143], v[84:87], a[20:23], v[128:143]\n"
"s_waitcnt vmcnt(4)\n"
"v_cndmask_b32 v92, v56, v57, s[44:45]\n"
"v_cndmask_b32 v93, v58, v59, s[44:45]\n"
"v_cndmask_b32 v94, v60, v61, s[44:45]\n"
"v_cndmask_b32 v95, v62, v63, s[44:45]\n"
"v_mfma_f32_32x32x16_bf16 v[128:143], v[88:91], a[24:27], v[128:143]\n"
"v_cndmask_b32 v112, v222, v112, s[56:57]\n"
"v_cndmask_b32 v113, v222, v113, s[58:59]\n"
"v_mfma_f32_32x32x16_bf16 v[128:143], v[92:95], a[28:31], v[128:143]\n"
"v_cndmask_b32 v114, v222, v114, s[60:61]\n"
"v_cndmask_b32 v115, v222, v115, s[62:63]\n"
"v_cndmask_b32 v116, v222, v116, s[72:73]\n"
"v_cndmask_b32 v117, v222, v117, s[74:75]\n"
"v_cndmask_b32 v118, v222, v118, s[80:81]\n"
"v_cndmask_b32 v119, v222, v119, s[82:83]\n"
"v_cndmask_b32 v120, v222, v120, s[84:85]\n"
"v_cndmask_b32 v121, v222, v121, s[86:87]\n"
"v_cndmask_b32 v122, v222, v122, s[88:89]\n"
"v_cndmask_b32 v123, v222, v123, s[90:91]\n"
"v_cndmask_b32 v124, v222, v124, s[92:93]\n"
"v_cndmask_b32 v125, v222, v125, s[94:95]\n"
"v_cndmask_b32 v126, v222, v126, s[96:97]\n"
"v_cndmask_b32 v127, v222, v127, s[98:99]\n"
"v_max3_f32 v223, v112, v113, v114\n"
"v_max3_f32 v221, v115, v116, v117\n"
"v_max3_f32 v223, v223, v118, v119\n"
"v_max3_f32 v221, v221, v120, v121\n"
"v_max3_f32 v223, v223, v122, v123\n"
"v_max3_f32 v221, v221, v124, v125\n"
"v_max3_f32 v223, v223, v126, v127\n"
"v_max3_f32 v221, v221, v128, v129\n"
"v_max3_f32 v223, v223, v130, v131\n"
"v_max_f32 v223, v223, v221\n"
"v_mov_b32 v221, v223\n"
"s_waitcnt vmcnt(0)\n"
"s_nop 0\n"
"v_permlane32_swap_b32 v223, v221\n"
"v_max_f32 v223, v223, v221\n"
"v_cndmask_b32 v96, v96, v97, s[46:47]\n"
"v_cndmask_b32 v97, v98, v99, s[46:47]\n"
"v_cndmask_b32 v100, v100, v101, s[46:47]\n"
"v_cndmask_b32 v101, v102, v103, s[46:47]\n"
"v_cndmask_b32 v104, v104, v105, s[46:47]\n"
"v_cndmask_b32 v105, v106, v107, s[46:47]\n"
"v_cndmask_b32 v108, v108, v109, s[46:47]\n"
"v_cndmask_b32 v109, v110, v111, s[46:47]\n"
"v_sub_f32 v112, v112, v223\n"
"v_sub_f32 v113, v113, v223\n"
"v_sub_f32 v114, v114, v223\n"
"v_sub_f32 v115, v115, v223\n"
"v_sub_f32 v116, v116, v223\n"
"v_sub_f32 v117, v117, v223\n"
"v_sub_f32 v118, v118, v223\n"
"v_sub_f32 v119, v119, v223\n"
"v_sub_f32 v120, v120, v223\n"
"v_sub_f32 v121, v121, v223\n"
"v_sub_f32 v122, v122, v223\n"
"v_sub_f32 v123, v123, v223\n"
"v_sub_f32 v124, v124, v223\n"
"v_sub_f32 v125, v125, v223\n"
"v_sub_f32 v126, v126, v223\n"
"v_sub_f32 v127, v127, v223\n"
"v_sub_f32 v128, v128, v223\n"
"v_sub_f32 v129, v129, v223\n"
"v_sub_f32 v130, v130, v223\n"
"v_sub_f32 v131, v131, v223\n"
"v_exp_f32 v112, v112\n"
"v_exp_f32 v113, v113\n"
"v_exp_f32 v114, v114\n"
"v_exp_f32 v115, v115\n"
"v_exp_f32 v116, v116\n"
"v_exp_f32 v117, v117\n"
"v_exp_f32 v118, v118\n"
"v_exp_f32 v119, v119\n"
"v_exp_f32 v120, v120\n"
"v_exp_f32 v121, v121\n"
"v_exp_f32 v122, v122\n"
"v_exp_f32 v123, v123\n"
"v_exp_f32 v124, v124\n"
"v_exp_f32 v125, v125\n"
"v_exp_f32 v126, v126\n"
"v_exp_f32 v127, v127\n"
"v_exp_f32 v128, v128\n"
"v_exp_f32 v129, v129\n"
"v_exp_f32 v130, v130\n"
"v_exp_f32 v131, v131\n"
"v_add_f32 v224, v112, v113\n"
"v_add_f32 v225, v114, v115\n"
"v_add_f32 v224, v224, v116\n"
"v_add_f32 v225, v225, v117\n"
"v_add_f32 v224, v224, v118\n"
"v_add_f32 v225, v225, v119\n"
"v_add_f32 v224, v224, v120\n"
"v_add_f32 v225, v225, v121\n"
"v_add_f32 v224, v224, v122\n"
"v_add_f32 v225, v225, v123\n"
"v_add_f32 v224, v224, v124\n"
"v_add_f32 v225, v225, v125\n"
"v_add_f32 v224, v224, v126\n"
"v_add_f32 v225, v225, v127\n"
"v_add_f32 v224, v224, v128\n"
"v_add_f32 v225, v225, v129\n"
"v_add_f32 v224, v224, v130\n"
"v_add_f32 v225, v225, v131\n"
"v_cvt_pk_bf16_f32 v112, v112, v113\n"
"v_cvt_pk_bf16_f32 v113, v114, v115\n"
"v_cvt_pk_bf16_f32 v114, v116, v117\n"
"v_cvt_pk_bf16_f32 v115, v118, v119\n"
"v_cvt_pk_bf16_f32 v120, v120, v121\n"
"v_cvt_pk_bf16_f32 v121, v122, v123\n"
"v_cvt_pk_bf16_f32 v122, v124, v125\n"
"v_cvt_pk_bf16_f32 v123, v126, v127\n"
"v_cvt_pk_bf16_f32 v208, v128, v129\n"
"v_cvt_pk_bf16_f32 v209, v130, v131\n"
"v_add_f32 v224, v224, v225\n"
"v_mov_b32 v225, v224\n"
"v_mfma_f32_32x32x16_bf16 v[144:159], a[64:67], v[112:115], 0\n"
"s_nop 0\n"
"v_mfma_f32_32x32x16_bf16 v[144:159], a[68:71], v[120:123], v[144:159]\n"
"v_mfma_f32_32x32x16_bf16 v[144:159], v[96:99], v[208:211], v[144:159]\n"
"v_mfma_f32_32x32x16_bf16 v[160:175], a[72:75], v[112:115], 0\n"
"v_permlane32_swap_b32 v224, v225\n"
"v_mfma_f32_32x32x16_bf16 v[160:175], a[76:79], v[120:123], v[160:175]\n"
"v_mfma_f32_32x32x16_bf16 v[160:175], v[100:103], v[208:211], v[160:175]\n"
"v_mfma_f32_32x32x16_bf16 v[176:191], a[80:83], v[112:115], 0\n"
"v_add_f32 v224, v224, v225\n"
"v_mfma_f32_32x32x16_bf16 v[176:191], a[84:87], v[120:123], v[176:191]\n"
"v_mfma_f32_32x32x16_bf16 v[176:191], v[104:107], v[208:211], v[176:191]\n"
"v_mfma_f32_32x32x16_bf16 v[192:207], a[88:91], v[112:115], 0\n"
"v_rcp_f32 v224, v224\n"
"v_mfma_f32_32x32x16_bf16 v[192:207], a[92:95], v[120:123], v[192:207]\n"
"v_mfma_f32_32x32x16_bf16 v[192:207], v[108:111], v[208:211], v[192:207]\n"
"s_lshl_b32 s40, s33, 18\n"
"s_add_u32 s41, s40, 0x1000\n"
"s_nop 7\n"
"s_nop 3\n"
"v_pk_mul_f32 v[144:145], v[144:145], v[224:225] op_sel:[0,0] op_sel_hi:[1,0]\n"
"v_pk_mul_f32 v[146:147], v[146:147], v[224:225] op_sel:[0,0] op_sel_hi:[1,0]\n"
"v_pk_mul_f32 v[148:149], v[148:149], v[224:225] op_sel:[0,0] op_sel_hi:[1,0]\n"
"v_pk_mul_f32 v[150:151], v[150:151], v[224:225] op_sel:[0,0] op_sel_hi:[1,0]\n"
"v_cvt_pk_bf16_f32 v144, v144, v145\n"
"v_cvt_pk_bf16_f32 v145, v146, v147\n"
"v_cvt_pk_bf16_f32 v146, v148, v149\n"
"v_cvt_pk_bf16_f32 v147, v150, v151\n"
"buffer_store_dwordx4 v[144:147], v212, s[36:39], s40 offen offset:0 sc1\n"
"v_pk_mul_f32 v[152:153], v[152:153], v[224:225] op_sel:[0,0] op_sel_hi:[1,0]\n"
"v_pk_mul_f32 v[154:155], v[154:155], v[224:225] op_sel:[0,0] op_sel_hi:[1,0]\n"
"v_pk_mul_f32 v[156:157], v[156:157], v[224:225] op_sel:[0,0] op_sel_hi:[1,0]\n"
"v_pk_mul_f32 v[158:159], v[158:159], v[224:225] op_sel:[0,0] op_sel_hi:[1,0]\n"
"v_cvt_pk_bf16_f32 v152, v152, v153\n"
"v_cvt_pk_bf16_f32 v153, v154, v155\n"
"v_cvt_pk_bf16_f32 v154, v156, v157\n"
"v_cvt_pk_bf16_f32 v155, v158, v159\n"
"buffer_store_dwordx4 v[152:155], v212, s[36:39], s40 offen offset:1024 sc1\n"
"v_pk_mul_f32 v[160:161], v[160:161], v[224:225] op_sel:[0,0] op_sel_hi:[1,0]\n"
"v_pk_mul_f32 v[162:163], v[162:163], v[224:225] op_sel:[0,0] op_sel_hi:[1,0]\n"
"v_pk_mul_f32 v[164:165], v[164:165], v[224:225] op_sel:[0,0] op_sel_hi:[1,0]\n"
"v_pk_mul_f32 v[166:167], v[166:167], v[224:225] op_sel:[0,0] op_sel_hi:[1,0]\n"
"v_cvt_pk_bf16_f32 v160, v160, v161\n"
"v_cvt_pk_bf16_f32 v161, v162, v163\n"
"v_cvt_pk_bf16_f32 v162, v164, v165\n"
"v_cvt_pk_bf16_f32 v163, v166, v167\n"
"buffer_store_dwordx4 v[160:163], v212, s[36:39], s40 offen offset:2048 sc1\n"
"v_pk_mul_f32 v[168:169], v[168:169], v[224:225] op_sel:[0,0] op_sel_hi:[1,0]\n"
"v_pk_mul_f32 v[170:171], v[170:171], v[224:225] op_sel:[0,0] op_sel_hi:[1,0]\n"
"v_pk_mul_f32 v[172:173], v[172:173], v[224:225] op_sel:[0,0] op_sel_hi:[1,0]\n"
"v_pk_mul_f32 v[174:175], v[174:175], v[224:225] op_sel:[0,0] op_sel_hi:[1,0]\n"
"v_cvt_pk_bf16_f32 v168, v168, v169\n"
"v_cvt_pk_bf16_f32 v169, v170, v171\n"
"v_cvt_pk_bf16_f32 v170, v172, v173\n"
"v_cvt_pk_bf16_f32 v171, v174, v175\n"
"buffer_store_dwordx4 v[168:171], v212, s[36:39], s40 offen offset:3072 sc1\n"
"v_pk_mul_f32 v[176:177], v[176:177], v[224:225] op_sel:[0,0] op_sel_hi:[1,0]\n"
"v_pk_mul_f32 v[178:179], v[178:179], v[224:225] op_sel:[0,0] op_sel_hi:[1,0]\n"
"v_pk_mul_f32 v[180:181], v[180:181], v[224:225] op_sel:[0,0] op_sel_hi:[1,0]\n"
"v_pk_mul_f32 v[182:183], v[182:183], v[224:225] op_sel:[0,0] op_sel_hi:[1,0]\n"
"v_cvt_pk_bf16_f32 v176, v176, v177\n"
"v_cvt_pk_bf16_f32 v177, v178, v179\n"
"v_cvt_pk_bf16_f32 v178, v180, v181\n"
"v_cvt_pk_bf16_f32 v179, v182, v183\n"
"buffer_store_dwordx4 v[176:179], v212, s[36:39], s41 offen offset:0 sc1\n"
"v_pk_mul_f32 v[184:185], v[184:185], v[224:225] op_sel:[0,0] op_sel_hi:[1,0]\n"
"v_pk_mul_f32 v[186:187], v[186:187], v[224:225] op_sel:[0,0] op_sel_hi:[1,0]\n"
"v_pk_mul_f32 v[188:189], v[188:189], v[224:225] op_sel:[0,0] op_sel_hi:[1,0]\n"
"v_pk_mul_f32 v[190:191], v[190:191], v[224:225] op_sel:[0,0] op_sel_hi:[1,0]\n"
"v_cvt_pk_bf16_f32 v184, v184, v185\n"
"v_cvt_pk_bf16_f32 v185, v186, v187\n"
"v_cvt_pk_bf16_f32 v186, v188, v189\n"
"v_cvt_pk_bf16_f32 v187, v190, v191\n"
"buffer_store_dwordx4 v[184:187], v212, s[36:39], s41 offen offset:1024 sc1\n"
"v_pk_mul_f32 v[192:193], v[192:193], v[224:225] op_sel:[0,0] op_sel_hi:[1,0]\n"
"v_pk_mul_f32 v[194:195], v[194:195], v[224:225] op_sel:[0,0] op_sel_hi:[1,0]\n"
"v_pk_mul_f32 v[196:197], v[196:197], v[224:225] op_sel:[0,0] op_sel_hi:[1,0]\n"
"v_pk_mul_f32 v[198:199], v[198:199], v[224:225] op_sel:[0,0] op_sel_hi:[1,0]\n"
"v_cvt_pk_bf16_f32 v192, v192, v193\n"
"v_cvt_pk_bf16_f32 v193, v194, v195\n"
"v_cvt_pk_bf16_f32 v194, v196, v197\n"
"v_cvt_pk_bf16_f32 v195, v198, v199\n"
"buffer_store_dwordx4 v[192:195], v212, s[36:39], s41 offen offset:2048 sc1\n"
"v_pk_mul_f32 v[200:201], v[200:201], v[224:225] op_sel:[0,0] op_sel_hi:[1,0]\n"
"v_pk_mul_f32 v[202:203], v[202:203], v[224:225] op_sel:[0,0] op_sel_hi:[1,0]\n"
"v_pk_mul_f32 v[204:205], v[204:205], v[224:225] op_sel:[0,0] op_sel_hi:[1,0]\n"
"v_pk_mul_f32 v[206:207], v[206:207], v[224:225] op_sel:[0,0] op_sel_hi:[1,0]\n"
"v_cvt_pk_bf16_f32 v200, v200, v201\n"
"v_cvt_pk_bf16_f32 v201, v202, v203\n"
"v_cvt_pk_bf16_f32 v202, v204, v205\n"
"v_cvt_pk_bf16_f32 v203, v206, v207\n"
"buffer_store_dwordx4 v[200:203], v212, s[36:39], s41 offen offset:3072 sc1\n"
"s_and_b32 s52, s33, 3\n"
"s_lshl_b32 s52, s52, 11\n"
"s_add_u32 s52, s52, s42\n"
"s_waitcnt vmcnt(0)\n"
"s_mov_b64 exec, 1\n"
"buffer_store_dword v218, off, s[12:15], s52 sc1\n"
"s_mov_b64 exec, -1\n"
"s_add_u32 s33, s33, s67\n"
"s_sub_u32 s34, s34, 1\n"
"s_cmp_eq_u32 s34, 0\n"
"s_cbranch_scc1 L_next\n"
"s_branch L_ax_seg\n"
"L_cal:\n"
"s_cmp_lg_u32 s5, 0\n"
"s_cbranch_scc1 L_next\n"
"s_load_dwordx2 s[40:41], s[0:1], 112\n"
"s_waitcnt lgkmcnt(0)\n"
"s_add_u32 s16, s40, 0x19c000\n"
"s_addc_u32 s17, s41, 0\n"
"s_and_b32 s17, s17, 0xffff\n"
"s_mov_b32 s18, 0x4000\n"
"s_mov_b32 s19, 0x20000\n"
"s_mov_b64 exec, 1\n"
"s_mov_b32 s33, -1\n"
"s_mov_b32 s34, -1\n"
"v_mov_b32 v0, 0\n"
"s_mov_b32 s3, 0\n"
"buffer_store_dword v0, off, s[16:19], s3 sc1\n"
"s_waitcnt vmcnt(0)\n"
"s_memtime s[36:37]\n"
"s_waitcnt lgkmcnt(0)\n"
"buffer_load_dword v1, off, s[16:19], s3 sc1\n"
"s_waitcnt vmcnt(0)\n"
"s_memtime s[38:39]\n"
"s_waitcnt lgkmcnt(0)\n"
"s_sub_u32 s36, s38, s36\n"
"s_min_u32 s33, s33, s36\n"
"s_mov_b32 s3, 8192\n"
"buffer_store_dword v0, off, s[16:19], s3 sc1\n"
"s_waitcnt vmcnt(0)\n"
"s_memtime s[36:37]\n"
"s_waitcnt lgkmcnt(0)\n"
"buffer_load_dword v1, off, s[16:19], s3 sc1\n"
"s_waitcnt vmcnt(0)\n"
"s_memtime s[38:39]\n"
"s_waitcnt lgkmcnt(0)\n"
"s_sub_u32 s36, s38, s36\n"
"s_min_u32 s34, s34, s36\n"
"s_mov_b32 s3, 1152\n"
"buffer_store_dword v0, off, s[16:19], s3 sc1\n"
"s_waitcnt vmcnt(0)\n"
"s_memtime s[36:37]\n"
"s_waitcnt lgkmcnt(0)\n"
"buffer_load_dword v1, off, s[16:19], s3 sc1\n"
"s_waitcnt vmcnt(0)\n"
"s_memtime s[38:39]\n"
"s_waitcnt lgkmcnt(0)\n"
"s_sub_u32 s36, s38, s36\n"
"s_min_u32 s33, s33, s36\n"
"s_mov_b32 s3, 9344\n"
"buffer_store_dword v0, off, s[16:19], s3 sc1\n"
"s_waitcnt vmcnt(0)\n"
"s_memtime s[36:37]\n"
"s_waitcnt lgkmcnt(0)\n"
"buffer_load_dword v1, off, s[16:19], s3 sc1\n"
"s_waitcnt vmcnt(0)\n"
"s_memtime s[38:39]\n"
"s_waitcnt lgkmcnt(0)\n"
"s_sub_u32 s36, s38, s36\n"
"s_min_u32 s34, s34, s36\n"
"s_mov_b32 s3, 2304\n"
"buffer_store_dword v0, off, s[16:19], s3 sc1\n"
"s_waitcnt vmcnt(0)\n"
"s_memtime s[36:37]\n"
"s_waitcnt lgkmcnt(0)\n"
"buffer_load_dword v1, off, s[16:19], s3 sc1\n"
"s_waitcnt vmcnt(0)\n"
"s_memtime s[38:39]\n"
"s_waitcnt lgkmcnt(0)\n"
"s_sub_u32 s36, s38, s36\n"
"s_min_u32 s33, s33, s36\n"
"s_mov_b32 s3, 10496\n"
"buffer_store_dword v0, off, s[16:19], s3 sc1\n"
"s_waitcnt vmcnt(0)\n"
"s_memtime s[36:37]\n"
"s_waitcnt lgkmcnt(0)\n"
"buffer_load_dword v1, off, s[16:19], s3 sc1\n"
"s_waitcnt vmcnt(0)\n"
"s_memtime s[38:39]\n"
"s_waitcnt lgkmcnt(0)\n"
"s_sub_u32 s36, s38, s36\n"
"s_min_u32 s34, s34, s36\n"
"s_mov_b32 s3, 3456\n"
"buffer_store_dword v0, off, s[16:19], s3 sc1\n"
"s_waitcnt vmcnt(0)\n"
"s_memtime s[36:37]\n"
"s_waitcnt lgkmcnt(0)\n"
"buffer_load_dword v1, off, s[16:19], s3 sc1\n"
"s_waitcnt vmcnt(0)\n"
"s_memtime s[38:39]\n"
"s_waitcnt lgkmcnt(0)\n"
"s_sub_u32 s36, s38, s36\n"
"s_min_u32 s33, s33, s36\n"
"s_mov_b32 s3, 11648\n"
"buffer_store_dword v0, off, s[16:19], s3 sc1\n"
"s_waitcnt vmcnt(0)\n"
"s_memtime s[36:37]\n"
"s_waitcnt lgkmcnt(0)\n"
"buffer_load_dword v1, off, s[16:19], s3 sc1\n"
"s_waitcnt vmcnt(0)\n"
"s_memtime s[38:39]\n"
"s_waitcnt lgkmcnt(0)\n"
"s_sub_u32 s36, s38, s36\n"
"s_min_u32 s34, s34, s36\n"
"s_mov_b32 s3, 4608\n"
"buffer_store_dword v0, off, s[16:19], s3 sc1\n"
"s_waitcnt vmcnt(0)\n"
"s_memtime s[36:37]\n"
"s_waitcnt lgkmcnt(0)\n"
"buffer_load_dword v1, off, s[16:19], s3 sc1\n"
"s_waitcnt vmcnt(0)\n"
"s_memtime s[38:39]\n"
"s_waitcnt lgkmcnt(0)\n"
"s_sub_u32 s36, s38, s36\n"
"s_min_u32 s33, s33, s36\n"
"s_mov_b32 s3, 12800\n"
"buffer_store_dword v0, off, s[16:19], s3 sc1\n"
"s_waitcnt vmcnt(0)\n"
"s_memtime s[36:37]\n"
"s_waitcnt lgkmcnt(0)\n"
"buffer_load_dword v1, off, s[16:19], s3 sc1\n"
"s_waitcnt vmcnt(0)\n"
"s_memtime s[38:39]\n"
"s_waitcnt lgkmcnt(0)\n"
"s_sub_u32 s36, s38, s36\n"
"s_min_u32 s34, s34, s36\n"
"s_mov_b32 s3, 5760\n"
"buffer_store_dword v0, off, s[16:19], s3 sc1\n"
"s_waitcnt vmcnt(0)\n"
"s_memtime s[36:37]\n"
"s_waitcnt lgkmcnt(0)\n"
"buffer_load_dword v1, off, s[16:19], s3 sc1\n"
"s_waitcnt vmcnt(0)\n"
"s_memtime s[38:39]\n"
"s_waitcnt lgkmcnt(0)\n"
"s_sub_u32 s36, s38, s36\n"
"s_min_u32 s33, s33, s36\n"
"s_mov_b32 s3, 13952\n"
"buffer_store_dword v0, off, s[16:19], s3 sc1\n"
"s_waitcnt vmcnt(0)\n"
"s_memtime s[36:37]\n"
"s_waitcnt lgkmcnt(0)\n"
"buffer_load_dword v1, off, s[16:19], s3 sc1\n"
"s_waitcnt vmcnt(0)\n"
"s_memtime s[38:39]\n"
"s_waitcnt lgkmcnt(0)\n"
"s_sub_u32 s36, s38, s36\n"
"s_min_u32 s34, s34, s36\n"
"s_cmp_lt_u32 s34, s33\n"
"s_cselect_b32 s3, 1, 0\n"
"v_mov_b32 v0, s3\n"
"s_mov_b32 s3, 0x2000\n"
"buffer_store_dword v0, off, s[12:15], s3 sc1\n"
"s_mov_b64 exec, -1\n"
"s_branch L_next\n"
    :: "s"(ka), "s"(blockIdx.x), "v"(threadIdx.x), "s"(ldsb), "s"(kItems)
    : "memory","vcc","v1","v2","v3","v4","v5","v6","v7","v8","v9","v10","v11","v12","v13","v14","v15","v16","v17","v18","v19","v20","v21","v22","v23","v24","v25","v26","v27","v28","v29","v30","v31","v32","v33","v34","v35","v36","v37","v38","v39","v40","v41","v42","v43","v44","v45","v46","v47","v48","v49","v50","v51","v52","v53","v54","v55","v56","v57","v58","v59","v60","v61","v62","v63","v64","v65","v66","v67","v68","v69","v70","v71","v72","v73","v74","v75","v76","v77","v78","v79","v80","v81","v82","v83","v84","v85","v86","v87","v88","v89","v90","v91","v92","v93","v94","v95","v96","v97","v98","v99","v100","v101","v102","v103","v104","v105","v106","v107","v108","v109","v110","v111","v112","v113","v114","v115","v116","v117","v118","v119","v120","v121","v122","v123","v124","v125","v126","v127","v128","v129","v130","v131","v132","v133","v134","v135","v136","v137","v138","v139","v140","v141","v142","v143","v144","v145","v146","v147","v148","v149","v150","v151","v152","v153","v154","v155","v156","v157","v158","v159","v160","v161","v162","v163","v164","v165","v166","v167","v168","v169","v170","v171","v172","v173","v174","v175","v176","v177","v178","v179","v180","v181","v182","v183","v184","v185","v186","v187","v188","v189","v190","v191","v192","v193","v194","v195","v196","v197","v198","v199","v200","v201","v202","v203","v204","v205","v206","v207","v208","v209","v210","v211","v212","v213","v214","v215","v216","v217","v218","v219","v220","v221","v222","v223","v224","v225","v226","v227","v228","v229","v230","v231","v232","v233","v234","v235","v236","v237","v238","v239","v240","v241","v242","v243","v244","v245","v246","v247","v248","v249","v250","v251","v252","v253","v254","v255","a0","a1","a2","a3","a4","a5","a6","a7","a8","a9","a10","a11","a12","a13","a14","a15","a16","a17","a18","a19","a20","a21","a22","a23","a24","a25","a26","a27","a28","a29","a30","a31","a32","a33","a34","a35","a36","a37","a38","a39","a40","a41","a42","a43","a44","a45","a46","a47","a48","a49","a50","a51","a52","a53","a54","a55","a56","a57","a58","a59","a60","a61","a62","a63","a64","a65","a66","a67","a68","a69","a70","a71","a72","a73","a74","a75","a76","a77","a78","a79","a80","a81","a82","a83","a84","a85","a86","a87","a88","a89","a90","a91","a92","a93","a94","a95","a96","a97","a98","a99","a100","a101","a102","a103","a104","a105","a106","a107","a108","a109","a110","a111","a112","a113","a114","a115","a116","a117","a118","a119","a120","a121","a122","a123","a124","a125","a126","a127","a128","a129","a130","a131","a132","a133","a134","a135","a136","a137","a138","a139","a140","a141","a142","a143","a144","a145","a146","a147","a148","a149","a150","a151","a152","a153","a154","a155","a156","a157","a158","a159","a160","a161","a162","a163","a164","a165","a166","a167","a168","a169","a170","a171","a172","a173","a174","a175","a176","a177","a178","a179","a180","a181","a182","a183","a184","a185","a186","a187","a188","a189","a190","a191","a192","a193","a194","a195","a196","a197","a198","a199","a200","a201","a202","a203","a204","a205","a206","a207","a208","a209","a210","a211","a212","a213","a214","a215","a216","a217","a218","a219","a220","a221","a222","a223","a224","a225","a226","a227","a228","a229","a230","a231","a232","a233","a234","a235","a236","a237","a238","a239","a240","a241","a242","a243","a244","a245","a246","a247","a248","a249","a250","a251","a252","a253","a254","a255","s6","s7","s8","s9","s10","s11","s12","s13","s14","s15","s16","s17","s18","s19","s20","s21","s22","s23","s24","s25","s26","s27","s28","s29","s30","s31","s33","s34","s35","s36","s37","s38","s39","s40","s41","s42","s43","s44","s45","s46","s47","s48","s49","s50","s51","s52","s53","s54","s55","s56","s57","s58","s59","s60","s61","s62","s63","s64","s65","s66","s67","s68","s69","s70","s71","s72","s73","s74","s75","s76","s77","s78","s79","s80","s81","s82","s83","s84","s85","s86","s87","s88","s89","s90","s91","s92","s93","s94","s95","s96","s97","s98","s99");
}
extern "C" void kernel_launch(void* const* d_in, const int* in_sizes, int n_in, void* d_out, int out_size, void* d_ws, size_t ws_size, hipStream_t stream) {
  static const int order[12] = {0, 1, 2, 3, 4, 5, 7, 8, 9, 10, 11, 12};
  Params p;
  for (int i = 0; i < 12; ++i) p.p[i] = d_in[order[i]];
  char* comm = (char*)(((uintptr_t)d_ws + 2097151) & ~(uintptr_t)2097151);
  p.p[12] = d_out; p.p[13] = comm + 2097152; p.p[14] = comm;
  p.t[0] = p.t[1] = p.t[2] = p.t[3] = 0;
  (void)hipMemsetAsync(comm, 0xff, 1687552, stream);
  mega<<<256, 256, 0, stream>>>(p);
}
```
